```python
import torch

def _softmax(x, axis=-1):
    return torch.softmax(x, dim=axis)
def _gelu(x):
    return torch.nn.functional.gelu(x, approximate="tanh")
def _silu(x):
    return torch.nn.functional.silu(x)
def _sigmoid(x):
    return torch.sigmoid(x)
def _layernorm(x, axis=-1, eps=1e-5):
    m = x.mean(dim=axis, keepdim=True)
    v = x.var(dim=axis, keepdim=True, unbiased=False)
    return (x - m) / torch.sqrt(v + eps)
def _scatter_add(tgt, idx, src, axis=0):
    return tgt.clone().index_add_(axis, idx, src)
def _segment_sum(src, seg, n):
    out = torch.zeros((n,) + src.shape[1:], dtype=src.dtype)
    return out.index_add_(0, seg, src)
def _pad(x, pw):
    flat = tuple(v for pair in reversed(pw) for v in pair)
    return torch.nn.functional.pad(x, flat)

kernel_name = 'recurrent_scan__softmax_full_mha__glu_gated_swiglu__prompt'

def setup_inputs():

    _w = (lambda a: (lambda t: t.to(torch.bfloat16) if t.is_floating_point() else t)(torch.as_tensor(a)))
    def _A_setup():
        B, S, D = 1, 2048, 448
        return {
            "x": _w((lambda *s: torch.randn(*s, dtype=torch.bfloat16) * 0.05)(B, S, D)),
            "wqkv": _w((lambda *s: torch.randn(*s, dtype=torch.bfloat16) * 0.2)(D, 3 * D)),
            "wo": _w((lambda *s: torch.randn(*s, dtype=torch.bfloat16) * 0.2)(D, D)),
        }
    _A_in = _A_setup()

    def _B_setup():
        B, S, D, F = 1, 2048, 448, 1792
        return {
            "x": _w((lambda *s: torch.randn(*s, dtype=torch.bfloat16) * 0.05)(B, S, D)),
            "w1": _w((lambda *s: torch.randn(*s, dtype=torch.bfloat16) * 0.1)(D, F) * (2.0 / D) ** 0.5),
            "w3": _w((lambda *s: torch.randn(*s, dtype=torch.bfloat16) * 0.1)(D, F) * (2.0 / D) ** 0.5),
            "w2": _w((lambda *s: torch.randn(*s, dtype=torch.bfloat16) * 0.1)(F, D) * (2.0 / F) ** 0.5),
        }
    _B_in = _B_setup()

    return {
        "A_x": _A_in["x"],
        "A_wqkv": _A_in["wqkv"],
        "A_wo": _A_in["wo"],
        "B_x": _B_in["x"],
        "B_w1": _B_in["w1"],
        "B_w3": _B_in["w3"],
        "B_w2": _B_in["w2"],
    }

def reference(A_x, A_wqkv, A_wo, B_x, B_w1, B_w3, B_w2):
    def _A_ref(x, wqkv, wo):
        B, S, D = x.shape
        H = 16; Dh = D // H
        qkv = (lambda x, *d: x.permute(*d))((x @ wqkv).reshape(B, S, 3, H, Dh), 2, 0, 3, 1, 4)
        q, k, v = qkv[0], qkv[1], qkv[2]
        scale = Dh ** -0.5
        scores = torch.einsum("bhqd,bhkd->bhqk", q, k) * scale
        p = _softmax(scores, axis=-1)
        o = torch.einsum("bhqk,bhkd->bhqd", p, v)
        return (lambda x, *d: x.permute(*d))(o, 0, 2, 1, 3).reshape(B, S, D) @ wo

    def _B_ref(x, w1, w3, w2):
        gate = torch.einsum("bsd,df->bsf", x, w1)
        up = torch.einsum("bsd,df->bsf", x, w3)
        h = _silu(gate) * up
        out = torch.einsum("bsf,fd->bsd", h, w2)
        return out

    x = A_x
    xn = _layernorm(x, axis=-1)
    s = _A_ref(xn, A_wqkv, A_wo)
    g = _silu(_B_ref(xn, B_w1, B_w3, B_w2))
    return x + s * g
```

```cpp
#include <hip/hip_runtime.h>
#include <hip/hip_bf16.h>
#define MIMIC 1
typedef unsigned short u16;
__device__ __forceinline__ float b2f(u16 v){ return __uint_as_float(((unsigned)v)<<16); }
__device__ __forceinline__ u16 f2b(float f){ __hip_bfloat16 h = __float2bfloat16(f); return *(u16*)&h; }
__device__ __forceinline__ float rb(float f){ return b2f(f2b(f)); }
constexpr int S=2048,D=448,H=16,DH=28,F=1792;

__global__ void k_ln(const u16* x, float* xn){
  int r=blockIdx.x; int t=threadIdx.x;
  float v[7]; float s=0;
  for(int i=0;i<7;i++){ v[i]=b2f(x[r*D+t+64*i]); s+=v[i]; }
  for(int o=32;o;o>>=1) s+=__shfl_xor(s,o);
  float m=s/D; float q=0;
  for(int i=0;i<7;i++){ q+=(v[i]-m)*(v[i]-m); }
  for(int o=32;o;o>>=1) q+=__shfl_xor(q,o);
  float var=q/D;
  float mb=rb(m), vb=rb(var), den=rb(sqrtf(rb(vb+1e-5f)));
  for(int i=0;i<7;i++) xn[r*D+t+64*i]=rb(rb(v[i]-mb)/den);
}
__global__ void k_gemm(const float* A, const u16* W, float* C, int K, int N, int round){
  int n=blockIdx.x*64+threadIdx.x; int r0=blockIdx.y*8;
  if(n>=N) return;
  float acc[8]={0,0,0,0,0,0,0,0};
  for(int k=0;k<K;k++){ float w=b2f(W[k*N+n]);
    #pragma unroll
    for(int i=0;i<8;i++) acc[i]+=A[(r0+i)*K+k]*w; }
  #pragma unroll
  for(int i=0;i<8;i++) C[(r0+i)*N+n]= round? rb(acc[i]) : acc[i];
}
__global__ void k_attn(const float* qkv, float* o){
  int r=blockIdx.x, h=blockIdx.y, t=threadIdx.x;
  __shared__ float p[S]; __shared__ float q[DH];
  if(t<DH) q[t]=qkv[r*3*D+h*DH+t];
  __syncthreads();
  float scale=0.18898223650461362f; float mx=-1e30f;
  for(int j=t;j<S;j+=64){ float s=0; for(int d=0;d<DH;d++) s+=q[d]*qkv[j*3*D+D+h*DH+d];
    s=s*scale;
    p[j]=s; mx=fmaxf(mx,s);}
  for(int o2=32;o2;o2>>=1) mx=fmaxf(mx,__shfl_xor(mx,o2));
  float l=0; for(int j=t;j<S;j+=64){ float e=expf(p[j]-mx); p[j]=e; l+=e; }
  for(int o2=32;o2;o2>>=1) l+=__shfl_xor(l,o2);
  __syncthreads();
  if(t<DH){ float a=0;
    for(int j=0;j<S;j++) a+=rb(p[j])*qkv[j*3*D+2*D+h*DH+t]; o[r*D+h*DH+t]=rb(a/l);
  }
}
__global__ void k_h(const float* g, const float* u, float* h){ int i=blockIdx.x*256+threadIdx.x; float a=g[i];
 h[i]=rb(a/(1.f+expf(-a))*u[i]);
}
__global__ void k_fin(const u16* x,const float* s,const float* f,u16* out){ int i=blockIdx.x*256+threadIdx.x; float a=f[i];
 float g=a/(1.f+expf(-a)); out[i]=f2b(b2f(x[i])+s[i]*g);
}
extern "C" void kernel_launch(void* const* d_in, const int* in_sizes, int n_in, void* d_out, int out_size, void* d_ws, size_t ws_size, hipStream_t stream){
  const u16* x=(const u16*)d_in[0]; const u16* wqkv=(const u16*)d_in[1]; const u16* wo=(const u16*)d_in[2];
  const u16* w1=(const u16*)d_in[4]; const u16* w3=(const u16*)d_in[5]; const u16* w2=(const u16*)d_in[6];
  float* ws=(float*)d_ws; float* xn=ws; float* qkv=xn+S*D; float* o=qkv+S*3*D; float* g=o+S*D; float* u=g+S*F; float* h=u+S*F; float* s=h+S*F; float* f=s+S*D;
  int rr = MIMIC>=2;
  k_ln<<<S,64,0,stream>>>(x,xn);
  k_gemm<<<dim3(21,S/8),64,0,stream>>>(xn,wqkv,qkv,D,3*D,1);
  k_attn<<<dim3(S,H),64,0,stream>>>(qkv,o);
  k_gemm<<<dim3(28,S/8),64,0,stream>>>(xn,w1,g,D,F,rr);
  k_gemm<<<dim3(28,S/8),64,0,stream>>>(xn,w3,u,D,F,rr);
  k_h<<<S*F/256,256,0,stream>>>(g,u,h);
  k_gemm<<<dim3(7,S/8),64,0,stream>>>(o,wo,s,D,D,rr);
  k_gemm<<<dim3(7,S/8),64,0,stream>>>(h,w2,f,F,D,rr);
  k_fin<<<S*D/256,256,0,stream>>>(x,s,f,(u16*)d_out);
}
```

```cpp
#include <hip/hip_runtime.h>
#include <hip/hip_bf16.h>
#define MIMIC 0
typedef unsigned short u16;
__device__ __forceinline__ float b2f(u16 v){ return __uint_as_float(((unsigned)v)<<16); }
__device__ __forceinline__ u16 f2b(float f){ __hip_bfloat16 h = __float2bfloat16(f); return *(u16*)&h; }
__device__ __forceinline__ float rb(float f){ return b2f(f2b(f)); }
constexpr int S=2048,D=448,H=16,DH=28,F=1792;

__global__ void k_ln(const u16* x, float* xn){
  int r=blockIdx.x; int t=threadIdx.x;
  float v[7]; float s=0;
  for(int i=0;i<7;i++){ v[i]=b2f(x[r*D+t+64*i]); s+=v[i]; }
  for(int o=32;o;o>>=1) s+=__shfl_xor(s,o);
  float m=s/D; float q=0;
  for(int i=0;i<7;i++){ q+=(v[i]-m)*(v[i]-m); }
  for(int o=32;o;o>>=1) q+=__shfl_xor(q,o);
  float var=q/D;
  float rs=1.0f/sqrtf(var+1e-5f);
  for(int i=0;i<7;i++) xn[r*D+t+64*i]=(v[i]-m)*rs;
}
__global__ void k_gemm(const float* A, const u16* W, float* C, int K, int N, int round){
  int n=blockIdx.x*64+threadIdx.x; int r0=blockIdx.y*8;
  if(n>=N) return;
  float acc[8]={0,0,0,0,0,0,0,0};
  for(int k=0;k<K;k++){ float w=b2f(W[k*N+n]);
    #pragma unroll
    for(int i=0;i<8;i++) acc[i]+=A[(r0+i)*K+k]*w; }
  #pragma unroll
  for(int i=0;i<8;i++) C[(r0+i)*N+n]= round? rb(acc[i]) : acc[i];
}
__global__ void k_attn(const float* qkv, float* o){
  int r=blockIdx.x, h=blockIdx.y, t=threadIdx.x;
  __shared__ float p[S]; __shared__ float q[DH];
  if(t<DH) q[t]=qkv[r*3*D+h*DH+t];
  __syncthreads();
  float scale=0.18898223650461362f; float mx=-1e30f;
  for(int j=t;j<S;j+=64){ float s=0; for(int d=0;d<DH;d++) s+=q[d]*qkv[j*3*D+D+h*DH+d];
    s=s*scale;
    p[j]=s; mx=fmaxf(mx,s);}
  for(int o2=32;o2;o2>>=1) mx=fmaxf(mx,__shfl_xor(mx,o2));
  float l=0; for(int j=t;j<S;j+=64){ float e=expf(p[j]-mx); p[j]=e; l+=e; }
  for(int o2=32;o2;o2>>=1) l+=__shfl_xor(l,o2);
  __syncthreads();
  if(t<DH){ float a=0;
    for(int j=0;j<S;j++) a+=rb(p[j])*qkv[j*3*D+2*D+h*DH+t]; o[r*D+h*DH+t]=rb(a/l);
  }
}
__global__ void k_h(const float* g, const float* u, float* h){ int i=blockIdx.x*256+threadIdx.x; float a=g[i];
 h[i]=rb(a/(1.f+expf(-a))*u[i]);
}
__global__ void k_fin(const u16* x,const float* s,const float* f,u16* out){ int i=blockIdx.x*256+threadIdx.x; float a=f[i];
 float g=a/(1.f+expf(-a)); out[i]=f2b(b2f(x[i])+s[i]*g);
}
#define PROBE 0
#define NREG 16
#define R4(x) x x x x
#define R16(x) R4(R4(x))
#define R64(x) R4(R16(x))
#define R256(x) R4(R64(x))
#define R1024(x) R4(R256(x))
#define UNIT2 "v_max3_f32 v24, v25, v26, v27\n\tv_max3_f32 v28, v25, v26, v27\n\tv_max3_f32 v24, v25, v26, v27\n\t"
#define UNITM "v_mfma_f32_32x32x16_bf16 v[32:47], v[24:27], v[24:27], v[32:47]\n\t"
#define CL "v24","v25","v26","v27","v28","v32","v33","v34","v35","v36","v37","v38","v39","v40","v41","v42","v43","v44","v45","v46","v47"
typedef unsigned u32x4p __attribute__((ext_vector_type(4)));
__global__ void __launch_bounds__(512) k_probe(const char* x, const char* w2, unsigned long long* out){
  unsigned long long t0,t1,r0,r1;
  asm volatile("s_memrealtime %0\n\ts_memtime %1\n\ts_waitcnt lgkmcnt(0)":"=s"(r0),"=s"(t0)::"memory");
  unsigned long long res=0;
  asm volatile("s_memrealtime %0\n\ts_memtime %1\n\ts_waitcnt lgkmcnt(0)":"=s"(r1),"=s"(t1)::"memory");
  if(threadIdx.x==0){ out[blockIdx.x*4]=t1-t0; out[blockIdx.x*4+1]=r0; out[blockIdx.x*4+2]=r1; }
}
__global__ void k_spin(const unsigned long long* in){
  unsigned long long sum=0,mn=~0ull,mx=0,rs=0;
  for(int i=0;i<256;i++){ sum+=in[i*4]; mn=min(mn,in[i*4+1]); mx=max(mx,in[i*4+1]); rs+=in[i*4+2]-in[i*4+1]; }
  unsigned long long ticks;
  ticks=sum/256*10;
  if(ticks>1000000) ticks=1000000;
  unsigned long long s=__builtin_amdgcn_s_memrealtime();
  while(__builtin_amdgcn_s_memrealtime()-s<ticks) __builtin_amdgcn_s_sleep(4);
}
extern "C" void kernel_launch(void* const* d_in, const int* in_sizes, int n_in, void* d_out, int out_size, void* d_ws, size_t ws_size, hipStream_t stream){
  const u16* x=(const u16*)d_in[0]; const u16* wqkv=(const u16*)d_in[1]; const u16* wo=(const u16*)d_in[2];
  const u16* w1=(const u16*)d_in[4]; const u16* w3=(const u16*)d_in[5]; const u16* w2=(const u16*)d_in[6];
  float* ws=(float*)d_ws; float* xn=ws; float* qkv=xn+S*D; float* o=qkv+S*3*D; float* g=o+S*D; float* u=g+S*F; float* h=u+S*F; float* s=h+S*F; float* f=s+S*D;
  int rr = MIMIC>=2;
  { unsigned long long* pr=(unsigned long long*)((char*)d_ws+200*1024*1024); k_probe<<<256,512,65536,stream>>>((const char*)x,(const char*)w2,pr); k_spin<<<1,1,0,stream>>>(pr); }
  k_ln<<<S,64,0,stream>>>(x,xn);
  k_gemm<<<dim3(21,S/8),64,0,stream>>>(xn,wqkv,qkv,D,3*D,1);
  k_attn<<<dim3(S,H),64,0,stream>>>(qkv,o);
  k_gemm<<<dim3(28,S/8),64,0,stream>>>(xn,w1,g,D,F,rr);
  k_gemm<<<dim3(28,S/8),64,0,stream>>>(xn,w3,u,D,F,rr);
  k_h<<<S*F/256,256,0,stream>>>(g,u,h);
  k_gemm<<<dim3(7,S/8),64,0,stream>>>(o,wo,s,D,D,rr);
  k_gemm<<<dim3(7,S/8),64,0,stream>>>(h,w2,f,F,D,rr);
  k_fin<<<S*D/256,256,0,stream>>>(x,s,f,(u16*)d_out);
}
```

```cpp
#include <hip/hip_runtime.h>
#include <hip/hip_bf16.h>

__global__ void __launch_bounds__(512, 2)
fused_block_kernel(const void* x, const void* wqkv, const void* wo, const void* w1, const void* w3, const void* w2, void* out, void* ws) {
  __shared__ __attribute__((aligned(16))) char lds[163840];
  unsigned tid = threadIdx.x;
  asm volatile(
    "Lstart:\n"
    "s_load_dwordx2 s[28:29], %1, 0x0\n"
    "s_load_dwordx2 s[32:33], %1, 0x8\n"
    "s_load_dwordx2 s[40:41], %1, 0x18\n"
    "s_load_dwordx2 s[44:45], %1, 0x20\n"
    "s_lshr_b32 s27, %2, 3\n"
    "s_getpc_b64 s[8:9]\n"
    "Lpc:\n"
    "v_mul_u32_u24 v1, 3, v0\n"
    "v_lshrrev_b32 v1, 6, v1\n"
    "v_mad_u32_u24 v1, s27, 24, v1\n"
    "v_lshlrev_b32 v1, 7, v1\n"
    "v_min_u32 v1, Lend-Lpc, v1\n"
    "global_load_dword v249, v1, s[8:9]\n"
    "v_lshrrev_b32 v1, 6, v0\n"
    "v_and_b32 v0, 63, v0\n"
    "v_readfirstlane_b32 s25, v1\n"
    "s_and_b32 s64, %2, 1\n"
    "s_lshl_b32 s64, s64, 3\n"
    "s_and_b32 s65, s27, 7\n"
    "s_add_u32 s68, s65, s64\n"
    "s_lshr_b32 s70, s27, 3\n"
    "s_mul_i32 s99, s68, 114688\n"
    "s_bfe_u32 s69, %2, 0x20001\n"
    "s_lshl_b32 s64, s69, 1\n"
    "s_add_u32 s64, s64, s70\n"
    "s_add_u32 s65, s64, 16\n"
    "s_lshl_b32 s64, s64, 1\n"
    "s_sub_u32 s64, s64, 4\n"
    "s_cmp_lt_u32 s70, 2\n"
    "s_cselect_b32 s77, s65, s64\n"
    "s_cselect_b32 s67, 36, 73\n"
    "s_mul_i32 s71, s77, 112\n"
    "s_lshr_b32 s79, s25, 2\n"
    "s_and_b32 s66, s25, 3\n"
    "s_lshl_b32 s64, s66, 1\n"
    "s_add_u32 s64, s64, s79\n"
    "s_lshl_b32 s73, s64, 10\n"
    "s_lshl_b32 s64, s64, 4\n"
    "v_lshrrev_b32 v80, 3, v0\n"
    "v_and_b32 v81, 7, v0\n"
    "v_add_u32 v82, s64, v80\n"
    "v_mul_u32_u24 v82, 896, v82\n"
    "v_lshl_add_u32 v82, v81, 4, v82\n"
    "s_mov_b32 s30, 0x1c0000\n"
    "s_mov_b32 s31, 0x20000\n"
    "s_mov_b32 s34, 0x126000\n"
    "s_mov_b32 s35, 0x20000\n"
    "s_cmp_lg_u32 s79, 0\n"
    "s_cbranch_scc1 Lhalf1\n"
    "s_lshl_b32 s64, s69, 2\n"
    "s_add_u32 s64, s64, s70\n"
    "s_lshl_b32 s64, s64, 2\n"
    "s_add_u32 s64, s64, s25\n"
    "s_mul_i32 s64, s64, 1792\n"
    "s_add_u32 s64, s64, s99\n"
    "v_min_u32 v85, 13, v0\n"
    "v_lshlrev_b32 v85, 7, v85\n"
    "s_waitcnt lgkmcnt(0)\n"
    "buffer_load_dword v247, v85, s[28:31], s64 offen\n"
    "buffer_load_dwordx4 v[132:135], v82, s[28:31], s99 offen offset:0\n"
    "buffer_load_dwordx4 v[140:143], v82, s[28:31], s99 offen offset:128\n"
    "buffer_load_dwordx4 v[148:151], v82, s[28:31], s99 offen offset:256\n"
    "buffer_load_dwordx4 v[156:159], v82, s[28:31], s99 offen offset:384\n"
    "buffer_load_dwordx4 v[164:167], v82, s[28:31], s99 offen offset:512\n"
    "buffer_load_dwordx4 v[172:175], v82, s[28:31], s99 offen offset:640\n"
    "buffer_load_dwordx4 v[180:183], v82, s[28:31], s99 offen offset:768\n"
    "s_load_dwordx2 s[36:37], %1, 0x10\n"
    "s_load_dwordx2 s[48:49], %1, 0x28\n"
    "s_load_dwordx2 s[52:53], %1, 0x30\n"
    "s_load_dwordx2 s[56:57], %1, 0x38\n"
    "v_add_u32 v83, 7168, v82\n"
    "v_lshrrev_b32 v84, 4, v0\n"
    "s_mov_b32 s24, %2\n"
    "s_and_b32 s26, s24, 7\n"
    "s_add_u32 s72, s71, 112\n"
    "s_mov_b32 s38, 0x62000\n"
    "s_mov_b32 s39, 0x20000\n"
    "s_mov_b32 s42, 0x188000\n"
    "s_mov_b32 s43, 0x20000\n"
    "s_mov_b32 s46, 0x188000\n"
    "s_mov_b32 s47, 0x20000\n"
    "s_mov_b32 s50, 0x188000\n"
    "s_mov_b32 s51, 0x20000\n"
    "s_mov_b32 s54, 0x1c0000\n"
    "s_mov_b32 s55, 0x20000\n"
    "s_mov_b32 s58, 0xf10000\n"
    "s_mov_b32 s59, 0x20000\n"
    "v_lshlrev_b32 v1, 4, v0\n"
    "s_lshl_b32 s62, s25, 10\n"
    "s_lshl_b32 s16, s66, 11\n"
    "s_lshl_b32 s17, s66, 12\n"
    "v_and_b32 v85, 15, v0\n"
    "v_bfe_u32 v86, v0, 2, 2\n"
    "v_mov_b32 v88, 0x3f803f80\n"
    "v_cmp_eq_u32 vcc, v84, v86\n"
    "v_mov_b32 v52, 0\n"
    "v_mov_b32 v53, 0\n"
    "v_cndmask_b32 v48, 0, v88, vcc\n"
    "v_cndmask_b32 v49, 0, v88, vcc\n"
    "v_cndmask_b32 v50, 0, v88, vcc\n"
    "v_cndmask_b32 v51, 0, v88, vcc\n"
    "v_mov_b32 v56, 0\n"
    "v_mov_b32 v57, 0\n"
    "v_and_b32 v87, 1, v0\n"
    "v_lshlrev_b32 v87, 4, v87\n"
    "v_mov_b32 v88, 0x3f80\n"
    "v_lshlrev_b32 v87, v87, v88\n"
    "s_mov_b32 s8, 0x00300003\n"
    "s_mov_b32 s9, 0x30000300\n"
    "v_cndmask_b32 v54, 0, v87, s[8:9]\n"
    "s_lshl_b64 s[8:9], s[8:9], 2\n"
    "v_cndmask_b32 v55, 0, v87, s[8:9]\n"
    "s_add_u32 s64, s73, 0xc000\n"
    "v_xor_b32 v87, v85, v84\n"
    "v_lshlrev_b32 v87, 4, v87\n"
    "v_lshl_add_u32 v87, v84, 8, v87\n"
    "v_add_u32 v94, s64, v87\n"
    "v_xor_b32 v95, 64, v94\n"
    "v_xor_b32 v4, 0x400, v94\n"
    "v_xor_b32 v12, 0x400, v95\n"
    "v_xor_b32 v87, v80, v81\n"
    "v_lshlrev_b32 v87, 4, v87\n"
    "v_and_b32 v88, 3, v81\n"
    "v_lshl_add_u32 v87, v88, 8, v87\n"
    "v_lshrrev_b32 v88, 2, v81\n"
    "v_lshl_add_u32 v87, v88, 13, v87\n"
    "v_add_u32 v60, s64, v87\n"
    "s_lshl_b32 s64, s66, 3\n"
    "v_add_u32 v87, s64, v80\n"
    "s_and_b32 s64, s25, 1\n"
    "s_lshl_b32 s64, s64, 2\n"
    "v_and_b32 v88, 2, v80\n"
    "v_or_b32 v88, s64, v88\n"
    "v_xor_b32 v88, v88, v81\n"
    "v_min_u32 v89, 6, v88\n"
    "v_mul_u32_u24 v2, 2688, v87\n"
    "v_lshl_add_u32 v2, v89, 4, v2\n"
    "v_mul_u32_u24 v3, 3584, v87\n"
    "v_lshl_add_u32 v3, v88, 4, v3\n"
    "s_cmp_lt_u32 s70, 2\n"
    "s_cselect_b32 s5, 0, 112\n"
    "s_cselect_b32 s4, 172032, s5\n"
    "s_sub_u32 s5, 172032, s5\n"
    "s_mov_b32 s6, s71\n"
    "s_lshl_b32 s63, s66, 10\n"
    "v_and_b32 v87, 3, v80\n"
    "v_lshlrev_b32 v87, 5, v87\n"
    "v_lshl_add_u32 v88, v84, 3, v86\n"
    "v_lshlrev_b32 v91, 6, v88\n"
    "v_lshlrev_b32 v88, 7, v88\n"
    "v_and_b32 v90, 3, v0\n"
    "v_lshlrev_b32 v90, 3, v90\n"
    "s_mul_i32 s64, s79, 56\n"
    "v_add_u32 v89, s64, v90\n"
    "v_xor_b32 v92, v89, v87\n"
    "v_add_u32 v6, v88, v92\n"
    "v_add_u32 v89, 32, v89\n"
    "v_xor_b32 v89, v89, v87\n"
    "v_add_u32 v7, v88, v89\n"
    "s_lshl_b32 s64, s79, 6\n"
    "v_add_u32 v89, s64, v90\n"
    "v_xor_b32 v89, v89, v87\n"
    "v_add_u32 v8, v88, v89\n"
    "v_xor_b32 v9, 32, v8\n"
    "v_and_b32 v89, 1, v84\n"
    "v_xor_b32 v89, s79, v89\n"
    "v_lshl_add_u32 v89, v89, 5, v90\n"
    "v_add_u32 v5, v91, v89\n"
    "s_lshr_b32 s64, s73, 4\n"
    "v_lshl_add_u32 v74, v85, 2, s64\n"
    "v_xor_b32 v75, 64, v74\n"
    "v_lshl_add_u32 v76, v84, 4, s64\n"
    "v_xor_b32 v77, 64, v76\n"
    "v_lshl_add_u32 v79, v0, 3, s63\n"
    "v_lshl_add_u32 v73, v80, 2, s64\n"
    "s_nop 0\n"
    "s_mul_i32 s64, s69, 7\n"
    "s_mul_i32 s65, s70, 5\n"
    "s_add_u32 s67, s65, 1\n"
    "s_lshl_b32 s65, s65, 1\n"
    "s_add_u32 s80, s70, 4\n"
    "s_lshl_b32 s81, s70, 1\n"
    "s_sub_u32 s81, s81, 2\n"
    "s_cmp_lt_u32 s70, 2\n"
    "s_cselect_b32 s80, s65, s80\n"
    "s_cselect_b32 s81, s67, s81\n"
    "s_add_u32 s81, s81, s64\n"
    "s_lshl_b32 s81, s81, 7\n"
    "s_lshl_b32 s64, s64, 1\n"
    "s_add_u32 s80, s80, s64\n"
    "s_lshl_b32 s80, s80, 6\n"
    "s_cmp_ge_u32 s70, 2\n"
    "s_cbranch_scc1 Ls1_h0\n"
    "s_and_b32 s66, s25, 3\n"
    "s_lshl_b32 s67, s68, 2\n"
    "s_add_u32 s67, s67, s66\n"
    "s_and_b32 s65, s77, 7\n"
    "s_lshl_b32 s65, s65, 1\n"
    "s_add_u32 s65, s65, s79\n"
    "s_lshl_b32 s65, s65, 6\n"
    "s_add_u32 s65, s65, s67\n"
    "s_lshl_b32 s74, s65, 12\n"
    "s_add_u32 s74, s74, 0x210000\n"
    "s_lshl_b32 s77, s77, 4\n"
    "s_add_u32 s77, s77, s68\n"
    "s_lshl_b32 s77, s77, 3\n"
    "s_add_u32 s77, s77, 256\n"
    "s_lshl_b32 s67, s67, 1\n"
    "s_add_u32 s67, s67, s79\n"
    "s_mul_i32 s67, s67, 57344\n"
    "s_add_u32 s67, s67, 0x610000\n"
    "s_mul_i32 s76, s79, 0xfffe4000\n"
    "s_add_u32 s76, s76, 57344\n"
    "s_lshl_b32 s82, s80, 4\n"
    "s_add_u32 s82, s82, s67\n"
    "s_lshl_b32 s83, s81, 4\n"
    "s_add_u32 s83, s83, s67\n"
    "s_lshl_b32 s66, s79, 10\n"
    "s_add_u32 s83, s83, s66\n"
    "s_add_u32 s82, s82, s66\n"
    "s_lshl_b32 s95, s68, 4\n"
    "s_lshl_b32 s66, s69, 2\n"
    "s_add_u32 s66, s66, s70\n"
    "s_add_u32 s95, s95, s66\n"
    "s_lshl_b32 s95, s95, 3\n"
    "s_add_u32 s95, s95, 4096\n"
    "v_lshrrev_b32 v90, 4, v0\n"
    "s_mov_b32 s88, 0xf000f000\n"
    "s_mov_b32 s89, 0xf000f000\n"
    "v_and_b32 v91, 15, v0\n"
    "v_lshlrev_b32 v92, 4, v91\n"
    "v_lshl_add_u32 v11, v90, 9, v92\n"
    "v_mov_b32 v92, 0x3f803f80\n"
    "v_cmp_eq_u32 vcc, 12, v91\n"
    "s_nop 1\n"
    "v_cndmask_b32 v78, 0, v92, vcc\n"
    "s_branch Ls2_h0\n"
    "Ls1_h0:\n"
    "s_and_b32 s65, s25, 3\n"
    "s_lshl_b32 s65, s65, 4\n"
    "v_lshrrev_b32 v90, 2, v0\n"
    "v_add_u32 v90, s65, v90\n"
    "v_mul_u32_u24 v72, 3584, v90\n"
    "v_lshrrev_b32 v90, 5, v0\n"
    "v_lshlrev_b32 v90, 1, v90\n"
    "v_and_b32 v91, 3, v0\n"
    "v_xor_b32 v90, v90, v91\n"
    "v_lshl_add_u32 v72, v90, 4, v72\n"
    "s_and_b32 s66, s25, 3\n"
    "s_lshl_b32 s67, s68, 2\n"
    "s_add_u32 s67, s67, s66\n"
    "s_and_b32 s65, s77, 7\n"
    "s_lshl_b32 s65, s65, 1\n"
    "s_add_u32 s65, s65, s79\n"
    "s_lshl_b32 s65, s65, 6\n"
    "s_add_u32 s65, s65, s67\n"
    "s_lshl_b32 s74, s65, 12\n"
    "s_add_u32 s74, s74, 0x210000\n"
    "s_lshl_b32 s77, s77, 4\n"
    "s_add_u32 s77, s77, s68\n"
    "s_lshl_b32 s77, s77, 3\n"
    "s_add_u32 s77, s77, 256\n"
    "s_lshl_b32 s67, s67, 1\n"
    "s_add_u32 s67, s67, s79\n"
    "s_mul_i32 s67, s67, 57344\n"
    "s_add_u32 s67, s67, 0x610000\n"
    "s_mul_i32 s76, s79, 0xfffe4000\n"
    "s_add_u32 s76, s76, 57344\n"
    "s_lshl_b32 s82, s80, 4\n"
    "s_add_u32 s82, s82, s67\n"
    "s_lshl_b32 s83, s81, 4\n"
    "s_add_u32 s83, s83, s67\n"
    "s_lshl_b32 s66, s79, 10\n"
    "s_add_u32 s83, s83, s66\n"
    "s_add_u32 s82, s82, 0\n"
    "s_lshl_b32 s95, s68, 4\n"
    "s_lshl_b32 s66, s69, 2\n"
    "s_add_u32 s66, s66, s70\n"
    "s_add_u32 s95, s95, s66\n"
    "s_lshl_b32 s95, s95, 3\n"
    "s_add_u32 s95, s95, 4096\n"
    "v_lshrrev_b32 v90, 4, v0\n"
    "s_lshl_b32 s65, s65, 11\n"
    "s_add_u32 s65, s65, 0x10000\n"
    "s_cmp_lt_u32 s69, 2\n"
    "s_cselect_b32 s74, s65, s74\n"
    "s_cselect_b32 s65, 1, 2\n"
    "s_cselect_b32 s84, 0x3e8b97f6, 1.0\n"
    "s_cselect_b32 s85, 0, 0x3f803f80\n"
    "s_lshl_b32 s65, s65, 18\n"
    "s_add_u32 s75, s74, s65\n"
    "s_add_u32 s78, s77, 128\n"
    "s_mov_b32 s86, 0\n"
    "s_mov_b32 s87, 0xffff0000\n"
    "v_mov_b32 v118, s85\n"
    "v_and_b32 v91, 8, v0\n"
    "v_and_b32 v92, 7, v0\n"
    "v_lshl_add_u32 v91, v91, 1, v92\n"
    "v_lshlrev_b32 v91, 4, v91\n"
    "v_lshl_add_u32 v10, v90, 9, v91\n"
    "Ls2_h0:\n"
    "Lhop_h0:\n"
    "s_cmp_lt_u32 s70, 2\n"
    "s_movk_i32 s64, 0x1fc\n"
    "s_cmovk_i32 s64, 0x1c3\n"
    "s_and_b32 s65, s25, 3\n"
    "s_lshl_b32 s65, 0x11111111, s65\n"
    "s_and_b32 s64, s64, s65\n"
    "s_mov_b64 exec, 0\n"
    "s_bitcmp1_b32 s64, 0\n"
    "s_cbranch_scc1 Lst0_0\n"
    "Lr0_0:\n"
    "s_bitcmp1_b32 s64, 1\n"
    "s_cbranch_scc1 Lst0_1\n"
    "Lr0_1:\n"
    "s_bitcmp1_b32 s64, 2\n"
    "s_cbranch_scc1 Lst0_2\n"
    "Lr0_2:\n"
    "s_bitcmp1_b32 s64, 3\n"
    "s_cbranch_scc1 Lst0_3\n"
    "Lr0_3:\n"
    "s_bitcmp1_b32 s64, 4\n"
    "s_cbranch_scc1 Lst0_4\n"
    "Lr0_4:\n"
    "s_bitcmp1_b32 s64, 5\n"
    "s_cbranch_scc1 Lst0_5\n"
    "Lr0_5:\n"
    "s_bitcmp1_b32 s64, 6\n"
    "s_cbranch_scc1 Lst0_6\n"
    "Lr0_6:\n"
    "s_bitcmp1_b32 s64, 7\n"
    "s_cbranch_scc1 Lst0_7\n"
    "Lr0_7:\n"
    "s_bitcmp1_b32 s64, 8\n"
    "s_cbranch_scc1 Lst0_8\n"
    "Lr0_8:\n"
    "s_bitcmp1_b32 s64, 9\n"
    "s_cbranch_scc1 Lr0_9\n"
    "Lr0_9:\n"
    "s_mov_b64 exec, -1\n"
    "s_waitcnt lgkmcnt(0)\n"
    "s_waitcnt vmcnt(6)\n"
    "v_mfma_f32_16x16x32_bf16 v[16:19], v[48:51], v[132:135], 0\n"
    "s_waitcnt vmcnt(5)\n"
    "v_mfma_f32_16x16x32_bf16 v[16:19], v[48:51], v[140:143], v[16:19]\n"
    "s_waitcnt vmcnt(4)\n"
    "v_mfma_f32_16x16x32_bf16 v[16:19], v[48:51], v[148:151], v[16:19]\n"
    "s_waitcnt vmcnt(3)\n"
    "v_mfma_f32_16x16x32_bf16 v[16:19], v[48:51], v[156:159], v[16:19]\n"
    "s_waitcnt vmcnt(2)\n"
    "v_mfma_f32_16x16x32_bf16 v[16:19], v[48:51], v[164:167], v[16:19]\n"
    "s_waitcnt vmcnt(1)\n"
    "v_mfma_f32_16x16x32_bf16 v[16:19], v[48:51], v[172:175], v[16:19]\n"
    "buffer_load_dwordx4 v[188:191], v83, s[28:31], s99 offen offset:0\n"
    "s_waitcnt vmcnt(1)\n"
    "v_mfma_f32_16x16x32_bf16 v[16:19], v[48:51], v[180:183], v[16:19]\n"
    "buffer_load_dwordx4 v[196:199], v83, s[28:31], s99 offen offset:128\n"
    "buffer_load_dwordx4 v[204:207], v83, s[28:31], s99 offen offset:256\n"
    "buffer_load_dwordx4 v[212:215], v83, s[28:31], s99 offen offset:384\n"
    "buffer_load_dwordx4 v[220:223], v83, s[28:31], s99 offen offset:512\n"
    "buffer_load_dwordx4 v[228:231], v83, s[28:31], s99 offen offset:640\n"
    "buffer_load_dwordx4 v[236:239], v83, s[28:31], s99 offen offset:768\n"
    "s_nop 1\n"
    "v_add_f32_dpp v16, v16, v16 quad_perm:[1,0,3,2] row_mask:0xf bank_mask:0xf\n"
    "s_nop 1\n"
    "v_add_f32_dpp v16, v16, v16 quad_perm:[2,3,0,1] row_mask:0xf bank_mask:0xf\n"
    "s_nop 1\n"
    "v_add_f32_dpp v16, v16, v16 row_half_mirror row_mask:0xf bank_mask:0xf\n"
    "v_mul_f32 v19, 0xbb124925, v16\n"
    "v_mul_f32 v18, 0xbb124925, v16\n"
    "v_mul_f32 v17, 0xbb124925, v16\n"
    "v_mul_f32 v16, 0xbb124925, v16\n"
    "s_nop 1\n"
    "v_mfma_f32_16x16x32_bf16 v[24:27], v[54:57], v[132:135], v[16:19]\n"
    "v_mfma_f32_16x16x32_bf16 v[28:31], v[52:55], v[132:135], v[16:19]\n"
    "v_mfma_f32_16x16x32_bf16 v[32:35], v[54:57], v[140:143], v[16:19]\n"
    "v_mfma_f32_16x16x32_bf16 v[36:39], v[52:55], v[140:143], v[16:19]\n"
    "v_mfma_f32_16x16x32_bf16 v[40:43], v[54:57], v[148:151], v[16:19]\n"
    "s_nop 3\n"
    "v_cvt_pk_bf16_f32 v132, v24, v25\n"
    "v_cvt_pk_bf16_f32 v133, v26, v27\n"
    "v_mfma_f32_16x16x32_bf16 v[44:47], v[52:55], v[148:151], v[16:19]\n"
    "v_cvt_pk_bf16_f32 v134, v28, v29\n"
    "v_cvt_pk_bf16_f32 v135, v30, v31\n"
    "ds_write_b128 v60, v[132:135] offset:0\n"
    "v_mfma_f32_16x16x32_bf16 v[24:27], v[54:57], v[156:159], v[16:19]\n"
    "v_cvt_pk_bf16_f32 v140, v32, v33\n"
    "v_cvt_pk_bf16_f32 v141, v34, v35\n"
    "v_mfma_f32_16x16x32_bf16 v[28:31], v[52:55], v[156:159], v[16:19]\n"
    "v_cvt_pk_bf16_f32 v142, v36, v37\n"
    "v_cvt_pk_bf16_f32 v143, v38, v39\n"
    "ds_write_b128 v60, v[140:143] offset:16384\n"
    "v_mfma_f32_16x16x32_bf16 v[32:35], v[54:57], v[164:167], v[16:19]\n"
    "v_cvt_pk_bf16_f32 v148, v40, v41\n"
    "v_cvt_pk_bf16_f32 v149, v42, v43\n"
    "v_mfma_f32_16x16x32_bf16 v[36:39], v[52:55], v[164:167], v[16:19]\n"
    "v_cvt_pk_bf16_f32 v150, v44, v45\n"
    "v_cvt_pk_bf16_f32 v151, v46, v47\n"
    "ds_write_b128 v60, v[148:151] offset:32768\n"
    "v_mfma_f32_16x16x32_bf16 v[40:43], v[54:57], v[172:175], v[16:19]\n"
    "v_cvt_pk_bf16_f32 v156, v24, v25\n"
    "v_cvt_pk_bf16_f32 v157, v26, v27\n"
    "v_mfma_f32_16x16x32_bf16 v[44:47], v[52:55], v[172:175], v[16:19]\n"
    "v_cvt_pk_bf16_f32 v158, v28, v29\n"
    "v_cvt_pk_bf16_f32 v159, v30, v31\n"
    "ds_write_b128 v60, v[156:159] offset:49152\n"
    "v_mfma_f32_16x16x32_bf16 v[24:27], v[54:57], v[180:183], v[16:19]\n"
    "v_cvt_pk_bf16_f32 v164, v32, v33\n"
    "v_cvt_pk_bf16_f32 v165, v34, v35\n"
    "v_mfma_f32_16x16x32_bf16 v[28:31], v[52:55], v[180:183], v[16:19]\n"
    "v_cvt_pk_bf16_f32 v166, v36, v37\n"
    "v_cvt_pk_bf16_f32 v167, v38, v39\n"
    "v_add_u32 v60, 0x10000, v60\n"
    "ds_write_b128 v60, v[164:167] offset:0\n"
    "v_cvt_pk_bf16_f32 v172, v40, v41\n"
    "v_cvt_pk_bf16_f32 v173, v42, v43\n"
    "v_cvt_pk_bf16_f32 v174, v44, v45\n"
    "v_cvt_pk_bf16_f32 v175, v46, v47\n"
    "ds_write_b128 v60, v[172:175] offset:16384\n"
    "v_cvt_pk_bf16_f32 v180, v24, v25\n"
    "v_cvt_pk_bf16_f32 v181, v26, v27\n"
    "v_cvt_pk_bf16_f32 v182, v28, v29\n"
    "v_cvt_pk_bf16_f32 v183, v30, v31\n"
    "ds_write_b128 v60, v[180:183] offset:32768\n"
    "v_dot2_f32_bf16 v20, v132, v132, 0\n"
    "s_nop 2\n"
    "v_dot2c_f32_bf16 v20, v133, v133\n"
    "v_dot2c_f32_bf16 v20, v134, v134\n"
    "v_dot2c_f32_bf16 v20, v135, v135\n"
    "v_dot2c_f32_bf16 v20, v140, v140\n"
    "v_dot2c_f32_bf16 v20, v141, v141\n"
    "v_dot2c_f32_bf16 v20, v142, v142\n"
    "v_dot2c_f32_bf16 v20, v143, v143\n"
    "v_dot2c_f32_bf16 v20, v148, v148\n"
    "v_dot2c_f32_bf16 v20, v149, v149\n"
    "v_dot2c_f32_bf16 v20, v150, v150\n"
    "v_dot2c_f32_bf16 v20, v151, v151\n"
    "v_dot2c_f32_bf16 v20, v156, v156\n"
    "v_dot2c_f32_bf16 v20, v157, v157\n"
    "v_dot2c_f32_bf16 v20, v158, v158\n"
    "v_dot2c_f32_bf16 v20, v159, v159\n"
    "v_dot2c_f32_bf16 v20, v164, v164\n"
    "v_dot2c_f32_bf16 v20, v165, v165\n"
    "v_dot2c_f32_bf16 v20, v166, v166\n"
    "v_dot2c_f32_bf16 v20, v167, v167\n"
    "v_dot2c_f32_bf16 v20, v172, v172\n"
    "v_dot2c_f32_bf16 v20, v173, v173\n"
    "v_dot2c_f32_bf16 v20, v174, v174\n"
    "v_dot2c_f32_bf16 v20, v175, v175\n"
    "v_dot2c_f32_bf16 v20, v180, v180\n"
    "v_dot2c_f32_bf16 v20, v181, v181\n"
    "v_dot2c_f32_bf16 v20, v182, v182\n"
    "v_dot2c_f32_bf16 v20, v183, v183\n"
    "v_mov_b32 v22, 0x3727c5ac\n"
    "s_nop 1\n"
    "v_add_f32_dpp v20, v20, v20 quad_perm:[1,0,3,2] row_mask:0xf bank_mask:0xf\n"
    "s_nop 1\n"
    "v_add_f32_dpp v20, v20, v20 quad_perm:[2,3,0,1] row_mask:0xf bank_mask:0xf\n"
    "s_nop 1\n"
    "v_add_f32_dpp v20, v20, v20 row_half_mirror row_mask:0xf bank_mask:0xf\n"
    "v_fmamk_f32 v13, v20, 0x3b124925, v22\n"
    "s_waitcnt vmcnt(6)\n"
    "v_mfma_f32_16x16x32_bf16 v[16:19], v[48:51], v[188:191], 0\n"
    "s_waitcnt vmcnt(5)\n"
    "v_mfma_f32_16x16x32_bf16 v[16:19], v[48:51], v[196:199], v[16:19]\n"
    "s_waitcnt vmcnt(4)\n"
    "v_mfma_f32_16x16x32_bf16 v[16:19], v[48:51], v[204:207], v[16:19]\n"
    "s_waitcnt vmcnt(3)\n"
    "v_mfma_f32_16x16x32_bf16 v[16:19], v[48:51], v[212:215], v[16:19]\n"
    "s_waitcnt vmcnt(2)\n"
    "v_mfma_f32_16x16x32_bf16 v[16:19], v[48:51], v[220:223], v[16:19]\n"
    "s_waitcnt vmcnt(1)\n"
    "v_mfma_f32_16x16x32_bf16 v[16:19], v[48:51], v[228:231], v[16:19]\n"
    "s_waitcnt vmcnt(0)\n"
    "v_mfma_f32_16x16x32_bf16 v[16:19], v[48:51], v[236:239], v[16:19]\n"
    "s_nop 7\n"
    "v_add_f32_dpp v16, v16, v16 quad_perm:[1,0,3,2] row_mask:0xf bank_mask:0xf\n"
    "s_nop 1\n"
    "v_add_f32_dpp v16, v16, v16 quad_perm:[2,3,0,1] row_mask:0xf bank_mask:0xf\n"
    "s_nop 1\n"
    "v_add_f32_dpp v16, v16, v16 row_half_mirror row_mask:0xf bank_mask:0xf\n"
    "v_mul_f32 v19, 0xbb124925, v16\n"
    "v_mul_f32 v18, 0xbb124925, v16\n"
    "v_mul_f32 v17, 0xbb124925, v16\n"
    "v_mul_f32 v16, 0xbb124925, v16\n"
    "s_nop 1\n"
    "v_mfma_f32_16x16x32_bf16 v[24:27], v[54:57], v[188:191], v[16:19]\n"
    "v_mfma_f32_16x16x32_bf16 v[28:31], v[52:55], v[188:191], v[16:19]\n"
    "v_mfma_f32_16x16x32_bf16 v[32:35], v[54:57], v[196:199], v[16:19]\n"
    "v_mfma_f32_16x16x32_bf16 v[36:39], v[52:55], v[196:199], v[16:19]\n"
    "s_lshl_b32 s64, %2, 2\n"
    "s_add_u32 s64, s64, s25\n"
    "s_mul_i32 s64, s64, 1280\n"
    "v_min_u32 v119, 9, v0\n"
    "v_lshlrev_b32 v119, 7, v119\n"
    "buffer_load_dword v245, v119, s[32:35], s64 offen\n"
    "s_add_u32 m0, s63, 0x0\n"
    "s_add_u32 s6, s6, 0\n"
    "buffer_load_dwordx4 v2, s[32:35], s6 offen lds\n"
    "s_bitcmp1_b32 s25, 0\n"
    "s_cselect_b64 s[12:13], s[44:45], s[40:41]\n"
    "s_mov_b32 s14, 1605632\n"
    "s_mov_b32 s15, 0x20000\n"
    "s_lshr_b32 s64, s25, 1\n"
    "s_add_u32 s64, s64, %2\n"
    "s_add_u32 s64, s64, %2\n"
    "s_mul_i32 s64, s64, 3200\n"
    "v_min_u32 v119, 24, v0\n"
    "v_lshlrev_b32 v119, 7, v119\n"
    "buffer_load_dword v243, v119, s[12:15], s64 offen\n"
    "v_mfma_f32_16x16x32_bf16 v[40:43], v[54:57], v[204:207], v[16:19]\n"
    "v_cvt_pk_bf16_f32 v188, v24, v25\n"
    "v_cvt_pk_bf16_f32 v189, v26, v27\n"
    "v_mfma_f32_16x16x32_bf16 v[44:47], v[52:55], v[204:207], v[16:19]\n"
    "v_cvt_pk_bf16_f32 v190, v28, v29\n"
    "v_cvt_pk_bf16_f32 v191, v30, v31\n"
    "v_add_u32 v60, 0xffff0000, v60\n"
    "ds_write_b128 v60, v[188:191] offset:128\n"
    "v_mfma_f32_16x16x32_bf16 v[24:27], v[54:57], v[212:215], v[16:19]\n"
    "v_cvt_pk_bf16_f32 v196, v32, v33\n"
    "v_cvt_pk_bf16_f32 v197, v34, v35\n"
    "v_mfma_f32_16x16x32_bf16 v[28:31], v[52:55], v[212:215], v[16:19]\n"
    "s_add_u32 m0, s63, 0x1000\n"
    "v_cvt_pk_bf16_f32 v198, v36, v37\n"
    "v_cvt_pk_bf16_f32 v199, v38, v39\n"
    "ds_write_b128 v60, v[196:199] offset:16512\n"
    "s_add_u32 s64, s6, 86016\n"
    "buffer_load_dwordx4 v2, s[32:35], s64 offen lds\n"
    "v_mfma_f32_16x16x32_bf16 v[32:35], v[54:57], v[220:223], v[16:19]\n"
    "v_cvt_pk_bf16_f32 v204, v40, v41\n"
    "v_cvt_pk_bf16_f32 v205, v42, v43\n"
    "v_mfma_f32_16x16x32_bf16 v[36:39], v[52:55], v[220:223], v[16:19]\n"
    "v_cvt_pk_bf16_f32 v206, v44, v45\n"
    "v_cvt_pk_bf16_f32 v207, v46, v47\n"
    "ds_write_b128 v60, v[204:207] offset:32896\n"
    "v_mfma_f32_16x16x32_bf16 v[40:43], v[54:57], v[228:231], v[16:19]\n"
    "v_cvt_pk_bf16_f32 v212, v24, v25\n"
    "v_cvt_pk_bf16_f32 v213, v26, v27\n"
    "v_mfma_f32_16x16x32_bf16 v[44:47], v[52:55], v[228:231], v[16:19]\n"
    "s_add_u32 m0, s63, 0x2000\n"
    "v_cvt_pk_bf16_f32 v214, v28, v29\n"
    "v_cvt_pk_bf16_f32 v215, v30, v31\n"
    "ds_write_b128 v60, v[212:215] offset:49280\n"
    "s_add_u32 s6, s6, s4\n"
    "buffer_load_dwordx4 v2, s[32:35], s6 offen lds\n"
    "v_mfma_f32_16x16x32_bf16 v[24:27], v[54:57], v[236:239], v[16:19]\n"
    "v_cvt_pk_bf16_f32 v220, v32, v33\n"
    "v_cvt_pk_bf16_f32 v221, v34, v35\n"
    "v_mfma_f32_16x16x32_bf16 v[28:31], v[52:55], v[236:239], v[16:19]\n"
    "s_add_u32 m0, s63, 0x3000\n"
    "v_cvt_pk_bf16_f32 v222, v36, v37\n"
    "v_cvt_pk_bf16_f32 v223, v38, v39\n"
    "v_add_u32 v60, 0x10000, v60\n"
    "ds_write_b128 v60, v[220:223] offset:128\n"
    "v_cvt_pk_bf16_f32 v228, v40, v41\n"
    "v_cvt_pk_bf16_f32 v229, v42, v43\n"
    "v_cvt_pk_bf16_f32 v230, v44, v45\n"
    "v_cvt_pk_bf16_f32 v231, v46, v47\n"
    "ds_write_b128 v60, v[228:231] offset:16512\n"
    "s_add_u32 s64, s6, 86016\n"
    "buffer_load_dwordx4 v2, s[32:35], s64 offen lds\n"
    "v_cvt_pk_bf16_f32 v236, v24, v25\n"
    "v_cvt_pk_bf16_f32 v237, v26, v27\n"
    "v_cvt_pk_bf16_f32 v238, v28, v29\n"
    "v_cvt_pk_bf16_f32 v239, v30, v31\n"
    "ds_write_b128 v60, v[236:239] offset:32896\n"
    "s_cmp_ge_u32 s70, 2\n"
    "s_cbranch_scc1 LpathY_h0\n"
    "Lpx_h0:\n"
    "ds_read_b128 v[128:131], v94 offset:0\n"
    "ds_read_b128 v[136:139], v95 offset:8192\n"
    "ds_read_b128 v[144:147], v94 offset:16384\n"
    "ds_read_b128 v[152:155], v95 offset:24576\n"
    "ds_read_b128 v[160:163], v94 offset:32768\n"
    "ds_read_b128 v[168:171], v95 offset:40960\n"
    "ds_read_b128 v[176:179], v94 offset:49152\n"
    "ds_read_b128 v[184:187], v95 offset:57344\n"
    "v_add_u32 v94, 0x10000, v94\n"
    "ds_read_b128 v[192:195], v94 offset:0\n"
    "v_add_u32 v95, 0x10000, v95\n"
    "ds_read_b128 v[200:203], v95 offset:8192\n"
    "ds_read_b128 v[208:211], v94 offset:16384\n"
    "ds_read_b128 v[216:219], v95 offset:24576\n"
    "ds_read_b128 v[224:227], v94 offset:32768\n"
    "ds_read_b128 v[232:235], v95 offset:40960\n"
    "v_dot2_f32_bf16 v21, v188, v188, 0\n"
    "s_nop 2\n"
    "v_dot2c_f32_bf16 v21, v189, v189\n"
    "v_dot2c_f32_bf16 v21, v190, v190\n"
    "v_dot2c_f32_bf16 v21, v191, v191\n"
    "v_dot2c_f32_bf16 v21, v196, v196\n"
    "v_dot2c_f32_bf16 v21, v197, v197\n"
    "v_dot2c_f32_bf16 v21, v198, v198\n"
    "v_dot2c_f32_bf16 v21, v199, v199\n"
    "v_dot2c_f32_bf16 v21, v204, v204\n"
    "v_dot2c_f32_bf16 v21, v205, v205\n"
    "v_dot2c_f32_bf16 v21, v206, v206\n"
    "v_dot2c_f32_bf16 v21, v207, v207\n"
    "v_dot2c_f32_bf16 v21, v212, v212\n"
    "v_dot2c_f32_bf16 v21, v213, v213\n"
    "v_dot2c_f32_bf16 v21, v214, v214\n"
    "v_dot2c_f32_bf16 v21, v215, v215\n"
    "v_dot2c_f32_bf16 v21, v220, v220\n"
    "v_dot2c_f32_bf16 v21, v221, v221\n"
    "v_dot2c_f32_bf16 v21, v222, v222\n"
    "v_dot2c_f32_bf16 v21, v223, v223\n"
    "v_dot2c_f32_bf16 v21, v228, v228\n"
    "v_dot2c_f32_bf16 v21, v229, v229\n"
    "v_dot2c_f32_bf16 v21, v230, v230\n"
    "v_dot2c_f32_bf16 v21, v231, v231\n"
    "v_dot2c_f32_bf16 v21, v236, v236\n"
    "v_dot2c_f32_bf16 v21, v237, v237\n"
    "v_dot2c_f32_bf16 v21, v238, v238\n"
    "v_dot2c_f32_bf16 v21, v239, v239\n"
    "v_mov_b32 v22, 0x3727c5ac\n"
    "s_nop 1\n"
    "v_add_f32_dpp v21, v21, v21 quad_perm:[1,0,3,2] row_mask:0xf bank_mask:0xf\n"
    "s_nop 1\n"
    "v_add_f32_dpp v21, v21, v21 quad_perm:[2,3,0,1] row_mask:0xf bank_mask:0xf\n"
    "s_nop 1\n"
    "v_add_f32_dpp v21, v21, v21 row_half_mirror row_mask:0xf bank_mask:0xf\n"
    "v_fmamk_f32 v14, v21, 0x3b124925, v22\n"
    "s_waitcnt vmcnt(0)\n"
    "s_waitcnt lgkmcnt(14)\n"
    "s_barrier\n"
    "s_setprio 3\n"
    "ds_read_b64_tr_b16 v[48:49], v6 offset:0\n"
    "ds_read_b64_tr_b16 v[50:51], v6 offset:512\n"
    "ds_read_b128 v[132:135], v4 offset:0\n"
    "ds_read_b64_tr_b16 v[52:53], v7 offset:0\n"
    "ds_read_b64_tr_b16 v[54:55], v7 offset:512\n"
    "ds_read_b64_tr_b16 v[56:57], v6 offset:4096\n"
    "ds_read_b64_tr_b16 v[58:59], v6 offset:4608\n"
    "ds_read_b128 v[140:143], v12 offset:8192\n"
    "ds_read_b64_tr_b16 v[60:61], v7 offset:4096\n"
    "ds_read_b64_tr_b16 v[62:63], v7 offset:4608\n"
    "s_waitcnt lgkmcnt(8)\n"
    "v_mfma_f32_16x16x32_bf16 v[16:19], v[128:131], v[48:51], 0\n"
    "s_waitcnt lgkmcnt(5)\n"
    "ds_read_b128 v[148:151], v4 offset:16384\n"
    "ds_read_b64_tr_b16 v[64:65], v6 offset:8192\n"
    "v_mfma_f32_16x16x32_bf16 v[24:27], v[128:131], v[52:55], 0\n"
    "ds_read_b64_tr_b16 v[66:67], v6 offset:8704\n"
    "v_mfma_f32_16x16x32_bf16 v[28:31], v[132:135], v[52:55], 0\n"
    "ds_read_b128 v[156:159], v12 offset:24576\n"
    "ds_read_b64_tr_b16 v[68:69], v7 offset:8192\n"
    "v_mfma_f32_16x16x32_bf16 v[20:23], v[132:135], v[48:51], 0\n"
    "s_waitcnt lgkmcnt(8)\n"
    "ds_read_b64_tr_b16 v[70:71], v7 offset:8704\n"
    "v_mfma_f32_16x16x32_bf16 v[16:19], v[136:139], v[56:59], v[16:19]\n"
    "s_waitcnt lgkmcnt(6)\n"
    "ds_read_b128 v[164:167], v4 offset:32768\n"
    "ds_read_b64_tr_b16 v[48:49], v6 offset:12288\n"
    "buffer_load_dwordx2 v[252:253], off, s[56:59], 0 sc1\n"
    "v_mfma_f32_16x16x32_bf16 v[24:27], v[136:139], v[60:63], v[24:27]\n"
    "ds_read_b64_tr_b16 v[50:51], v6 offset:12800\n"
    "v_mfma_f32_16x16x32_bf16 v[28:31], v[140:143], v[60:63], v[28:31]\n"
    "s_setprio 1\n"
    "ds_read_b128 v[172:175], v12 offset:40960\n"
    "ds_read_b64_tr_b16 v[52:53], v7 offset:12288\n"
    "v_mfma_f32_16x16x32_bf16 v[20:23], v[140:143], v[56:59], v[20:23]\n"
    "s_setprio 0\n"
    "ds_read_b64_tr_b16 v[54:55], v7 offset:12800\n"
    "s_add_u32 m0, s63, 0xc000\n"
    "s_add_u32 s18, s71, 0xfc000\n"
    "s_barrier\n"
    "s_setprio 3\n"
    "s_waitcnt lgkmcnt(9)\n"
    "v_mfma_f32_16x16x32_bf16 v[16:19], v[144:147], v[64:67], v[16:19]\n"
    "s_waitcnt lgkmcnt(6)\n"
    "ds_read_b128 v[180:183], v4 offset:49152\n"
    "ds_read_b64_tr_b16 v[56:57], v6 offset:16384\n"
    "buffer_load_dwordx4 v2, s[32:35], s18 offen lds\n"
    "s_add_u32 m0, s63, 0xd000\n"
    "v_mfma_f32_16x16x32_bf16 v[24:27], v[144:147], v[68:71], v[24:27]\n"
    "s_add_u32 s18, s71, 0x111000\n"
    "ds_read_b64_tr_b16 v[58:59], v6 offset:16896\n"
    "v_mfma_f32_16x16x32_bf16 v[28:31], v[148:151], v[68:71], v[28:31]\n"
    "ds_read_b64_tr_b16 v[60:61], v7 offset:16384\n"
    "buffer_load_dwordx4 v2, s[32:35], s18 offen lds\n"
    "s_add_u32 m0, s63, 0x10000\n"
    "s_add_u32 s18, s80, 0x0\n"
    "v_mfma_f32_16x16x32_bf16 v[20:23], v[148:151], v[64:67], v[20:23]\n"
    "s_waitcnt lgkmcnt(7)\n"
    "ds_read_b64_tr_b16 v[62:63], v7 offset:16896\n"
    "ds_write_b32 v73, v13 offset:57344\n"
    "v_mfma_f32_16x16x32_bf16 v[16:19], v[152:155], v[48:51], v[16:19]\n"
    "s_waitcnt lgkmcnt(6)\n"
    "ds_read_b128 v[188:191], v12 offset:57344\n"
    "ds_read_b64_tr_b16 v[64:65], v6 offset:20480\n"
    "ds_write_b32 v73, v14 offset:57376\n"
    "v_mfma_f32_16x16x32_bf16 v[24:27], v[152:155], v[52:55], v[24:27]\n"
    "ds_read_b64_tr_b16 v[66:67], v6 offset:20992\n"
    "v_mfma_f32_16x16x32_bf16 v[28:31], v[156:159], v[52:55], v[28:31]\n"
    "s_setprio 1\n"
    "ds_read_b64_tr_b16 v[68:69], v7 offset:20480\n"
    "v_mfma_f32_16x16x32_bf16 v[20:23], v[156:159], v[48:51], v[20:23]\n"
    "s_setprio 0\n"
    "ds_read_b64_tr_b16 v[70:71], v7 offset:20992\n"
    "s_barrier\n"
    "s_setprio 3\n"
    "s_waitcnt lgkmcnt(9)\n"
    "v_mfma_f32_16x16x32_bf16 v[16:19], v[160:163], v[56:59], v[16:19]\n"
    "Lst0_0:\n"
    "s_cbranch_execz Lr0_0\n"
    "s_waitcnt lgkmcnt(7)\n"
    "v_add_u32 v4, 0x10000, v4\n"
    "ds_read_b128 v[196:199], v4 offset:0\n"
    "ds_read_b64_tr_b16 v[48:49], v6 offset:24576\n"
    "buffer_load_dwordx4 v3, s[40:43], s18 offen lds\n"
    "v_mfma_f32_16x16x32_bf16 v[24:27], v[160:163], v[60:63], v[24:27]\n"
    "s_add_u32 m0, s63, 0x12000\n"
    "ds_read_b64_tr_b16 v[50:51], v6 offset:25088\n"
    "buffer_load_dwordx4 v3, s[44:47], s18 offen lds\n"
    "s_add_u32 m0, s63, 0x11000\n"
    "v_mfma_f32_16x16x32_bf16 v[28:31], v[164:167], v[60:63], v[28:31]\n"
    "s_add_u32 s18, s80, 0x1c000\n"
    "v_add_u32 v12, 0x10000, v12\n"
    "ds_read_b128 v[204:207], v12 offset:8192\n"
    "ds_read_b64_tr_b16 v[52:53], v7 offset:24576\n"
    "v_mfma_f32_16x16x32_bf16 v[20:23], v[164:167], v[56:59], v[20:23]\n"
    "s_waitcnt lgkmcnt(7)\n"
    "ds_read_b64_tr_b16 v[54:55], v7 offset:25088\n"
    "buffer_load_dwordx4 v3, s[40:43], s18 offen lds\n"
    "s_add_u32 m0, s63, 0x13000\n"
    "v_mfma_f32_16x16x32_bf16 v[16:19], v[168:171], v[64:67], v[16:19]\n"
    "s_waitcnt lgkmcnt(6)\n"
    "ds_read_b128 v[212:215], v4 offset:16384\n"
    "ds_read_b64_tr_b16 v[56:57], v6 offset:28672\n"
    "buffer_load_dwordx4 v3, s[44:47], s18 offen lds\n"
    "s_add_u32 m0, s63, 0x14000\n"
    "v_mfma_f32_16x16x32_bf16 v[24:27], v[168:171], v[68:71], v[24:27]\n"
    "s_add_u32 s18, s80, 0x38000\n"
    "ds_read_b64_tr_b16 v[58:59], v6 offset:29184\n"
    "v_mfma_f32_16x16x32_bf16 v[28:31], v[172:175], v[68:71], v[28:31]\n"
    "ds_read_b128 v[220:223], v12 offset:24576\n"
    "ds_read_b64_tr_b16 v[60:61], v7 offset:28672\n"
    "buffer_load_dwordx4 v3, s[40:43], s18 offen lds\n"
    "s_add_u32 m0, s63, 0x16000\n"
    "v_mfma_f32_16x16x32_bf16 v[20:23], v[172:175], v[64:67], v[20:23]\n"
    "s_waitcnt lgkmcnt(8)\n"
    "ds_read_b64_tr_b16 v[62:63], v7 offset:29184\n"
    "buffer_load_dwordx4 v3, s[44:47], s18 offen lds\n"
    "s_add_u32 m0, s63, 0x15000\n"
    "s_add_u32 s18, s80, 0x54000\n"
    "v_mfma_f32_16x16x32_bf16 v[16:19], v[176:179], v[48:51], v[16:19]\n"
    "s_waitcnt lgkmcnt(6)\n"
    "ds_read_b128 v[228:231], v4 offset:32768\n"
    "ds_read_b64_tr_b16 v[64:65], v6 offset:32768\n"
    "v_mfma_f32_16x16x32_bf16 v[24:27], v[176:179], v[52:55], v[24:27]\n"
    "ds_read_b64_tr_b16 v[66:67], v6 offset:33280\n"
    "buffer_load_dwordx4 v3, s[40:43], s18 offen lds\n"
    "v_mfma_f32_16x16x32_bf16 v[28:31], v[180:183], v[52:55], v[28:31]\n"
    "s_add_u32 m0, s63, 0x17000\n"
    "ds_read_b64_tr_b16 v[68:69], v7 offset:32768\n"
    "buffer_load_dwordx4 v3, s[44:47], s18 offen lds\n"
    "s_add_u32 m0, s63, 0x18000\n"
    "s_add_u32 s18, s80, 0x70000\n"
    "v_mfma_f32_16x16x32_bf16 v[20:23], v[180:183], v[48:51], v[20:23]\n"
    "s_waitcnt lgkmcnt(7)\n"
    "ds_read_b64_tr_b16 v[70:71], v7 offset:33280\n"
    "v_mfma_f32_16x16x32_bf16 v[16:19], v[184:187], v[56:59], v[16:19]\n"
    "s_waitcnt lgkmcnt(5)\n"
    "ds_read_b128 v[236:239], v12 offset:40960\n"
    "ds_read_b64_tr_b16 v[48:49], v6 offset:36864\n"
    "v_mfma_f32_16x16x32_bf16 v[24:27], v[184:187], v[60:63], v[24:27]\n"
    "ds_read_b64_tr_b16 v[50:51], v6 offset:37376\n"
    "v_mfma_f32_16x16x32_bf16 v[28:31], v[188:191], v[60:63], v[28:31]\n"
    "ds_read_b64_tr_b16 v[52:53], v7 offset:36864\n"
    "v_mfma_f32_16x16x32_bf16 v[20:23], v[188:191], v[56:59], v[20:23]\n"
    "s_waitcnt lgkmcnt(4)\n"
    "ds_read_b64_tr_b16 v[54:55], v7 offset:37376\n"
    "v_mfma_f32_16x16x32_bf16 v[16:19], v[192:195], v[64:67], v[16:19]\n"
    "ds_read_b64_tr_b16 v[56:57], v6 offset:40960\n"
    "v_mfma_f32_16x16x32_bf16 v[24:27], v[192:195], v[68:71], v[24:27]\n"
    "ds_read_b64_tr_b16 v[58:59], v6 offset:41472\n"
    "v_mfma_f32_16x16x32_bf16 v[28:31], v[196:199], v[68:71], v[28:31]\n"
    "ds_read_b64_tr_b16 v[60:61], v7 offset:40960\n"
    "v_mfma_f32_16x16x32_bf16 v[20:23], v[196:199], v[64:67], v[20:23]\n"
    "s_setprio 1\n"
    "ds_read_b64_tr_b16 v[62:63], v7 offset:41472\n"
    "s_waitcnt lgkmcnt(4)\n"
    "v_mfma_f32_16x16x32_bf16 v[16:19], v[200:203], v[48:51], v[16:19]\n"
    "ds_read_b64_tr_b16 v[64:65], v6 offset:45056\n"
    "v_mfma_f32_16x16x32_bf16 v[24:27], v[200:203], v[52:55], v[24:27]\n"
    "ds_read_b64_tr_b16 v[66:67], v6 offset:45568\n"
    "v_mfma_f32_16x16x32_bf16 v[28:31], v[204:207], v[52:55], v[28:31]\n"
    "s_setprio 0\n"
    "ds_read_b64_tr_b16 v[68:69], v7 offset:45056\n"
    "v_mfma_f32_16x16x32_bf16 v[20:23], v[204:207], v[48:51], v[20:23]\n"
    "ds_read_b64_tr_b16 v[70:71], v7 offset:45568\n"
    "s_waitcnt vmcnt(8)\n"
    "s_barrier\n"
    "s_setprio 3\n"
    "s_waitcnt lgkmcnt(4)\n"
    "v_mfma_f32_16x16x32_bf16 v[16:19], v[208:211], v[56:59], v[16:19]\n"
    "ds_read_b64_tr_b16 v[48:49], v6 offset:49152\n"
    "buffer_load_dwordx4 v3, s[40:43], s18 offen lds\n"
    "ds_read_b32 v114, v74 offset:57344\n"
    "v_mfma_f32_16x16x32_bf16 v[24:27], v[208:211], v[60:63], v[24:27]\n"
    "s_add_u32 m0, s63, 0x1a000\n"
    "ds_read_b64_tr_b16 v[50:51], v6 offset:49664\n"
    "buffer_load_dwordx4 v3, s[44:47], s18 offen lds\n"
    "s_add_u32 m0, s63, 0x19000\n"
    "ds_read_b32 v115, v75 offset:57344\n"
    "v_mfma_f32_16x16x32_bf16 v[28:31], v[212:215], v[60:63], v[28:31]\n"
    "s_add_u32 s18, s80, 0x8c000\n"
    "ds_read_b64_tr_b16 v[52:53], v7 offset:49152\n"
    "ds_read_b128 v[120:123], v76 offset:57344\n"
    "v_mfma_f32_16x16x32_bf16 v[20:23], v[212:215], v[56:59], v[20:23]\n"
    "s_waitcnt lgkmcnt(6)\n"
    "ds_read_b64_tr_b16 v[54:55], v7 offset:49664\n"
    "buffer_load_dwordx4 v3, s[40:43], s18 offen lds\n"
    "ds_read_b128 v[124:127], v77 offset:57344\n"
    "v_mfma_f32_16x16x32_bf16 v[16:19], v[216:219], v[64:67], v[16:19]\n"
    "s_add_u32 m0, s63, 0x1b000\n"
    "ds_read_b64_tr_b16 v[56:57], v6 offset:53248\n"
    "buffer_load_dwordx4 v3, s[44:47], s18 offen lds\n"
    "s_add_u32 m0, s63, 0x1c000\n"
    "v_mfma_f32_16x16x32_bf16 v[24:27], v[216:219], v[68:71], v[24:27]\n"
    "s_add_u32 s18, s80, 0xa8000\n"
    "ds_read_b64_tr_b16 v[58:59], v6 offset:53760\n"
    "v_mfma_f32_16x16x32_bf16 v[28:31], v[220:223], v[68:71], v[28:31]\n"
    "s_setprio 1\n"
    "ds_read_b64_tr_b16 v[60:61], v7 offset:53248\n"
    "buffer_load_dwordx4 v3, s[40:43], s18 offen lds\n"
    "s_add_u32 m0, s63, 0x1e000\n"
    "v_mfma_f32_16x16x32_bf16 v[20:23], v[220:223], v[64:67], v[20:23]\n"
    "s_setprio 0\n"
    "ds_read_b64_tr_b16 v[62:63], v7 offset:53760\n"
    "buffer_load_dwordx4 v3, s[44:47], s18 offen lds\n"
    "s_add_u32 m0, s63, 0x1d000\n"
    "s_add_u32 s18, s80, 0xc4000\n"
    "s_waitcnt vmcnt(2)\n"
    "s_barrier\n"
    "s_setprio 3\n"
    "s_waitcnt lgkmcnt(5)\n"
    "v_mfma_f32_16x16x32_bf16 v[16:19], v[224:227], v[48:51], v[16:19]\n"
    "v_add_u32 v8, 0x10000, v8\n"
    "ds_read_b64_tr_b16 v[64:65], v8 offset:0\n"
    "v_rsq_f32 v116, v114\n"
    "v_rsq_f32 v117, v115\n"
    "v_mfma_f32_16x16x32_bf16 v[24:27], v[224:227], v[52:55], v[24:27]\n"
    "ds_read_b64_tr_b16 v[66:67], v8 offset:512\n"
    "buffer_load_dwordx4 v3, s[40:43], s18 offen lds\n"
    "v_rsq_f32 v120, v120\n"
    "v_mfma_f32_16x16x32_bf16 v[28:31], v[228:231], v[52:55], v[28:31]\n"
    "s_add_u32 m0, s63, 0x1f000\n"
    "ds_read_b64_tr_b16 v[68:69], v8 offset:8192\n"
    "buffer_load_dwordx4 v3, s[44:47], s18 offen lds\n"
    "s_add_u32 m0, s63, 0x0\n"
    "s_add_u32 s18, s80, 0xe0000\n"
    "v_rsq_f32 v121, v121\n"
    "v_mfma_f32_16x16x32_bf16 v[20:23], v[228:231], v[48:51], v[20:23]\n"
    "s_waitcnt lgkmcnt(3)\n"
    "ds_read_b64_tr_b16 v[70:71], v8 offset:8704\n"
    "v_rsq_f32 v122, v122\n"
    "v_mfma_f32_16x16x32_bf16 v[16:19], v[232:235], v[56:59], v[16:19]\n"
    "ds_read_b64_tr_b16 v[48:49], v8 offset:4096\n"
    "buffer_load_dwordx4 v3, s[40:43], s18 offen lds\n"
    "v_rsq_f32 v123, v123\n"
    "v_mfma_f32_16x16x32_bf16 v[24:27], v[232:235], v[60:63], v[24:27]\n"
    "s_add_u32 m0, s63, 0x2000\n"
    "ds_read_b64_tr_b16 v[50:51], v8 offset:4608\n"
    "buffer_load_dwordx4 v3, s[44:47], s18 offen lds\n"
    "s_add_u32 m0, s63, 0x1000\n"
    "v_rsq_f32 v124, v124\n"
    "v_mfma_f32_16x16x32_bf16 v[28:31], v[236:239], v[60:63], v[28:31]\n"
    "s_add_u32 s18, s80, 0xfc000\n"
    "ds_read_b64_tr_b16 v[52:53], v8 offset:12288\n"
    "v_rsq_f32 v125, v125\n"
    "v_mfma_f32_16x16x32_bf16 v[20:23], v[236:239], v[56:59], v[20:23]\n"
    "s_waitcnt lgkmcnt(3)\n"
    "ds_read_b64_tr_b16 v[54:55], v8 offset:12800\n"
    "buffer_load_dwordx4 v3, s[40:43], s18 offen lds\n"
    "v_rsq_f32 v126, v126\n"
    "v_mfma_f32_16x16x32_bf16 v[32:35], v[64:67], v[128:131], 0\n"
    "s_add_u32 m0, s63, 0x3000\n"
    "v_add_u32 v9, 0x10000, v9\n"
    "ds_read_b64_tr_b16 v[56:57], v9 offset:0\n"
    "buffer_load_dwordx4 v3, s[44:47], s18 offen lds\n"
    "s_add_u32 m0, s63, 0x4000\n"
    "v_rsq_f32 v127, v127\n"
    "v_mfma_f32_16x16x32_bf16 v[40:43], v[68:71], v[128:131], 0\n"
    "s_add_u32 s18, s80, 0x118000\n"
    "ds_read_b64_tr_b16 v[58:59], v9 offset:512\n"
    "v_mul_f32 v112, 0xbfb8aa3b, v116\n"
    "v_mul_f32 v113, 0xbfb8aa3b, v117\n"
    "v_mfma_f32_16x16x32_bf16 v[44:47], v[68:71], v[132:135], 0\n"
    "ds_read_b64_tr_b16 v[60:61], v9 offset:8192\n"
    "buffer_load_dwordx4 v3, s[40:43], s18 offen lds\n"
    "s_add_u32 m0, s63, 0x6000\n"
    "v_mfma_f32_16x16x32_bf16 v[36:39], v[64:67], v[132:135], 0\n"
    "s_waitcnt lgkmcnt(3)\n"
    "ds_read_b64_tr_b16 v[62:63], v9 offset:8704\n"
    "buffer_load_dwordx4 v3, s[44:47], s18 offen lds\n"
    "s_add_u32 m0, s63, 0x5000\n"
    "v_mfma_f32_16x16x32_bf16 v[32:35], v[48:51], v[136:139], v[32:35]\n"
    "s_add_u32 s18, s80, 0x134000\n"
    "ds_read_b64_tr_b16 v[64:65], v9 offset:4096\n"
    "v_mfma_f32_16x16x32_bf16 v[40:43], v[52:55], v[136:139], v[40:43]\n"
    "ds_read_b64_tr_b16 v[66:67], v9 offset:4608\n"
    "buffer_load_dwordx4 v3, s[40:43], s18 offen lds\n"
    "v_mfma_f32_16x16x32_bf16 v[44:47], v[52:55], v[140:143], v[44:47]\n"
    "s_add_u32 m0, s63, 0x7000\n"
    "ds_read_b64_tr_b16 v[68:69], v9 offset:12288\n"
    "buffer_load_dwordx4 v3, s[44:47], s18 offen lds\n"
    "s_add_u32 m0, s63, 0x8000\n"
    "s_add_u32 s18, s80, 0x150000\n"
    "v_mfma_f32_16x16x32_bf16 v[36:39], v[48:51], v[140:143], v[36:39]\n"
    "s_waitcnt lgkmcnt(3)\n"
    "ds_read_b64_tr_b16 v[70:71], v9 offset:12800\n"
    "v_mfma_f32_16x16x32_bf16 v[96:99], v[56:59], v[128:131], 0\n"
    "ds_read_b64_tr_b16 v[48:49], v8 offset:16384\n"
    "buffer_load_dwordx4 v3, s[40:43], s18 offen lds\n"
    "v_mfma_f32_16x16x32_bf16 v[104:107], v[60:63], v[128:131], 0\n"
    "s_add_u32 m0, s63, 0xa000\n"
    "ds_read_b64_tr_b16 v[50:51], v8 offset:16896\n"
    "buffer_load_dwordx4 v3, s[44:47], s18 offen lds\n"
    "s_add_u32 m0, s63, 0x9000\n"
    "v_mfma_f32_16x16x32_bf16 v[108:111], v[60:63], v[132:135], 0\n"
    "s_add_u32 s18, s80, 0x16c000\n"
    "ds_read_b64_tr_b16 v[52:53], v8 offset:24576\n"
    "v_mfma_f32_16x16x32_bf16 v[100:103], v[56:59], v[132:135], 0\n"
    "s_waitcnt lgkmcnt(3)\n"
    "ds_read_b64_tr_b16 v[54:55], v8 offset:25088\n"
    "buffer_load_dwordx4 v3, s[40:43], s18 offen lds\n"
    "v_mul_f32 v16, v16, v120\n"
    "v_mul_f32 v17, v17, v121\n"
    "v_mul_f32 v18, v18, v122\n"
    "v_mfma_f32_16x16x32_bf16 v[96:99], v[64:67], v[136:139], v[96:99]\n"
    "s_add_u32 m0, s63, 0xb000\n"
    "ds_read_b64_tr_b16 v[56:57], v8 offset:20480\n"
    "buffer_load_dwordx4 v3, s[44:47], s18 offen lds\n"
    "s_add_u32 m0, s63, 0xc000\n"
    "v_mul_f32 v19, v19, v123\n"
    "v_mul_f32 v20, v20, v124\n"
    "v_mul_f32 v21, v21, v125\n"
    "v_mfma_f32_16x16x32_bf16 v[104:107], v[68:71], v[136:139], v[104:107]\n"
    "s_add_u32 s18, s81, 0x0\n"
    "ds_read_b64_tr_b16 v[58:59], v8 offset:20992\n"
    "v_mul_f32 v22, v22, v126\n"
    "v_mul_f32 v23, v23, v127\n"
    "v_mfma_f32_16x16x32_bf16 v[108:111], v[68:71], v[140:143], v[108:111]\n"
    "ds_read_b64_tr_b16 v[60:61], v8 offset:28672\n"
    "v_cvt_pk_bf16_f32 v80, v16, v17\n"
    "v_cvt_pk_bf16_f32 v81, v18, v19\n"
    "v_cvt_pk_bf16_f32 v82, v20, v21\n"
    "v_mfma_f32_16x16x32_bf16 v[100:103], v[64:67], v[140:143], v[100:103]\n"
    "s_waitcnt lgkmcnt(3)\n"
    "ds_read_b64_tr_b16 v[62:63], v8 offset:29184\n"
    "v_cvt_pk_bf16_f32 v83, v22, v23\n"
    "v_permlane16_swap_b32 v80, v82\n"
    "s_nop 0\n"
    "v_permlane16_swap_b32 v81, v83\n"
    "v_mfma_f32_16x16x32_bf16 v[32:35], v[48:51], v[144:147], v[32:35]\n"
    "ds_read_b64_tr_b16 v[64:65], v9 offset:16384\n"
    "v_mul_f32 v24, v24, v120\n"
    "v_mul_f32 v25, v25, v121\n"
    "v_mfma_f32_16x16x32_bf16 v[40:43], v[52:55], v[144:147], v[40:43]\n"
    "ds_read_b64_tr_b16 v[66:67], v9 offset:16896\n"
    "v_readfirstlane_b32 s60, v252\n"
    "v_mul_f32 v26, v26, v122\n"
    "v_mul_f32 v27, v27, v123\n"
    "v_mul_f32 v28, v28, v124\n"
    "v_mfma_f32_16x16x32_bf16 v[44:47], v[52:55], v[148:151], v[44:47]\n"
    "ds_read_b64_tr_b16 v[68:69], v9 offset:24576\n"
    "v_readfirstlane_b32 s61, v253\n"
    "v_mul_f32 v29, v29, v125\n"
    "v_mul_f32 v30, v30, v126\n"
    "v_mfma_f32_16x16x32_bf16 v[36:39], v[48:51], v[148:151], v[36:39]\n"
    "ds_read_b64_tr_b16 v[70:71], v9 offset:25088\n"
    "s_add_u32 s60, s60, 0x7f4a7c15\n"
    "s_addc_u32 s61, s61, 0x9e3779b9\n"
    "v_mul_f32 v31, v31, v127\n"
    "v_cvt_pk_bf16_f32 v84, v24, v25\n"
    "v_cvt_pk_bf16_f32 v85, v26, v27\n"
    "s_waitcnt lgkmcnt(4)\n"
    "v_mfma_f32_16x16x32_bf16 v[32:35], v[56:59], v[152:155], v[32:35]\n"
    "ds_read_b64_tr_b16 v[48:49], v9 offset:20480\n"
    "v_cvt_pk_bf16_f32 v86, v28, v29\n"
    "v_cvt_pk_bf16_f32 v87, v30, v31\n"
    "s_nop 0\n"
    "v_permlane16_swap_b32 v84, v86\n"
    "v_mfma_f32_16x16x32_bf16 v[40:43], v[60:63], v[152:155], v[40:43]\n"
    "ds_read_b64_tr_b16 v[50:51], v9 offset:20992\n"
    "v_mov_b32 v252, s60\n"
    "v_permlane16_swap_b32 v85, v87\n"
    "v_cndmask_b32 v84, v84, v78, s[88:89]\n"
    "v_mfma_f32_16x16x32_bf16 v[44:47], v[60:63], v[156:159], v[44:47]\n"
    "ds_read_b64_tr_b16 v[52:53], v9 offset:28672\n"
    "v_mov_b32 v253, s61\n"
    "v_cndmask_b32 v85, v85, v78, s[88:89]\n"
    "v_cndmask_b32 v86, v86, v78, s[88:89]\n"
    "v_cndmask_b32 v87, v87, v78, s[88:89]\n"
    "v_mfma_f32_16x16x32_bf16 v[36:39], v[56:59], v[156:159], v[36:39]\n"
    "s_waitcnt lgkmcnt(3)\n"
    "ds_read_b64_tr_b16 v[54:55], v9 offset:29184\n"
    "buffer_store_dwordx4 v[80:83], v11, s[56:59], s74 offen offset:2048 sc1\n"
    "buffer_store_dwordx4 v[84:87], v11, s[56:59], s74 offen offset:2304 sc1\n"
    "v_mfma_f32_16x16x32_bf16 v[96:99], v[64:67], v[144:147], v[96:99]\n"
    "s_setprio 2\n"
    "ds_read_b64_tr_b16 v[56:57], v8 offset:32768\n"
    "v_mfma_f32_16x16x32_bf16 v[104:107], v[68:71], v[144:147], v[104:107]\n"
    "s_setprio 1\n"
    "ds_read_b64_tr_b16 v[58:59], v8 offset:33280\n"
    "s_and_b32 s65, s27, 1\n"
    "s_lshl_b32 s66, s26, 1\n"
    "v_mfma_f32_16x16x32_bf16 v[108:111], v[68:71], v[148:151], v[108:111]\n"
    "s_add_u32 s65, s65, s66\n"
    "ds_read_b64_tr_b16 v[60:61], v8 offset:40960\n"
    "s_lshr_b32 s91, s27, 1\n"
    "v_mfma_f32_16x16x32_bf16 v[100:103], v[64:67], v[148:151], v[100:103]\n"
    "s_lshl_b32 s90, s65, 18\n"
    "ds_read_b64_tr_b16 v[62:63], v8 offset:41472\n"
    "s_add_u32 s90, s90, s17\n"
    "s_waitcnt lgkmcnt(4)\n"
    "v_mfma_f32_16x16x32_bf16 v[96:99], v[48:51], v[152:155], v[96:99]\n"
    "s_add_u32 s90, s90, 0x210000\n"
    "ds_read_b64_tr_b16 v[64:65], v8 offset:36864\n"
    "s_and_b32 s66, s25, 3\n"
    "v_mfma_f32_16x16x32_bf16 v[104:107], v[52:55], v[152:155], v[104:107]\n"
    "s_setprio 0\n"
    "ds_read_b64_tr_b16 v[66:67], v8 offset:37376\n"
    "s_lshl_b32 s67, s91, 2\n"
    "s_add_u32 s67, s67, s66\n"
    "v_mfma_f32_16x16x32_bf16 v[108:111], v[52:55], v[156:159], v[108:111]\n"
    "s_lshl_b32 s92, s65, 6\n"
    "ds_read_b64_tr_b16 v[68:69], v8 offset:45056\n"
    "s_add_u32 s92, s92, s67\n"
    "v_mfma_f32_16x16x32_bf16 v[100:103], v[48:51], v[156:159], v[100:103]\n"
    "s_lshl_b32 s92, s92, 11\n"
    "ds_read_b64_tr_b16 v[70:71], v8 offset:45568\n"
    "s_add_u32 s92, s92, 0x10000\n"
    "s_waitcnt vmcnt(6)\n"
    "s_barrier\n"
    "s_setprio 3\n"
    "s_waitcnt lgkmcnt(4)\n"
    "v_mfma_f32_16x16x32_bf16 v[32:35], v[56:59], v[160:163], v[32:35]\n"
    "s_lshl_b32 s93, s67, 15\n"
    "ds_read_b64_tr_b16 v[48:49], v8 offset:49152\n"
    "s_lshl_b32 s7, s65, 8\n"
    "v_mfma_f32_16x16x32_bf16 v[40:43], v[60:63], v[160:163], v[40:43]\n"
    "s_add_u32 s7, s7, s93\n"
    "ds_read_b64_tr_b16 v[50:51], v8 offset:49664\n"
    "s_add_u32 s7, s7, 0xd13000\n"
    "v_mfma_f32_16x16x32_bf16 v[44:47], v[60:63], v[164:167], v[44:47]\n"
    "s_mul_i32 s66, s65, 768\n"
    "ds_read_b64_tr_b16 v[52:53], v8 offset:57344\n"
    "s_add_u32 s93, s93, s66\n"
    "v_mfma_f32_16x16x32_bf16 v[36:39], v[56:59], v[164:167], v[36:39]\n"
    "s_add_u32 s93, s93, 0xd10000\n"
    "ds_read_b64_tr_b16 v[54:55], v8 offset:57856\n"
    "s_lshl_b32 s94, s67, 4\n"
    "s_waitcnt lgkmcnt(4)\n"
    "v_mfma_f32_16x16x32_bf16 v[32:35], v[64:67], v[168:171], v[32:35]\n"
    "s_add_u32 s94, s94, s65\n"
    "ds_read_b64_tr_b16 v[56:57], v8 offset:53248\n"
    "s_lshl_b32 s94, s94, 3\n"
    "v_mfma_f32_16x16x32_bf16 v[40:43], v[68:71], v[168:171], v[40:43]\n"
    "s_add_u32 s94, s94, 8192\n"
    "ds_read_b64_tr_b16 v[58:59], v8 offset:53760\n"
    "s_lshl_b32 s65, s26, 7\n"
    "v_mfma_f32_16x16x32_bf16 v[44:47], v[68:71], v[172:175], v[44:47]\n"
    "s_lshl_b32 s66, s91, 3\n"
    "ds_read_b64_tr_b16 v[60:61], v8 offset:61440\n"
    "s_add_u32 s66, s66, s65\n"
    "v_mfma_f32_16x16x32_bf16 v[36:39], v[64:67], v[172:175], v[36:39]\n"
    "s_add_u32 s66, s66, 256\n"
    "ds_read_b64_tr_b16 v[62:63], v8 offset:61952\n"
    "s_waitcnt lgkmcnt(4)\n"
    "v_mfma_f32_16x16x32_bf16 v[32:35], v[48:51], v[176:179], v[32:35]\n"
    "s_add_u32 s65, s65, 1280\n"
    "v_add_u32 v8, 0xffff0000, v8\n"
    "ds_read_b64_tr_b16 v[64:65], v8 offset:0\n"
    "v_and_b32 v250, 15, v0\n"
    "v_mfma_f32_16x16x32_bf16 v[40:43], v[52:55], v[176:179], v[40:43]\n"
    "ds_read_b64_tr_b16 v[66:67], v8 offset:512\n"
    "v_and_b32 v251, 16, v0\n"
    "v_lshlrev_b32 v251, 6, v251\n"
    "v_mfma_f32_16x16x32_bf16 v[44:47], v[52:55], v[180:183], v[44:47]\n"
    "ds_read_b64_tr_b16 v[68:69], v8 offset:8192\n"
    "v_lshl_add_u32 v250, v250, 3, v251\n"
    "v_add_u32 v250, s65, v250\n"
    "v_mfma_f32_16x16x32_bf16 v[36:39], v[48:51], v[180:183], v[36:39]\n"
    "s_waitcnt lgkmcnt(3)\n"
    "ds_read_b64_tr_b16 v[70:71], v8 offset:8704\n"
    "v_mov_b32 v251, s66\n"
    "v_cmp_gt_u32 vcc, 32, v0\n"
    "v_mfma_f32_16x16x32_bf16 v[32:35], v[56:59], v[184:187], v[32:35]\n"
    "s_and_b32 s65, s27, 3\n"
    "ds_read_b64_tr_b16 v[48:49], v8 offset:4096\n"
    "v_cndmask_b32 v248, v251, v250, vcc\n"
    "v_mfma_f32_16x16x32_bf16 v[40:43], v[60:63], v[184:187], v[40:43]\n"
    "s_lshl_b32 s66, s26, 2\n"
    "ds_read_b64_tr_b16 v[50:51], v8 offset:4608\n"
    "s_add_u32 s65, s65, s66\n"
    "v_mfma_f32_16x16x32_bf16 v[44:47], v[60:63], v[188:191], v[44:47]\n"
    "s_lshr_b32 s97, s27, 2\n"
    "ds_read_b64_tr_b16 v[52:53], v8 offset:12288\n"
    "s_lshl_b32 s97, s97, 7\n"
    "v_mfma_f32_16x16x32_bf16 v[36:39], v[56:59], v[188:191], v[36:39]\n"
    "s_mul_i32 s99, s65, 57344\n"
    "ds_read_b64_tr_b16 v[54:55], v8 offset:12800\n"
    "s_add_u32 s99, s99, s97\n"
    "s_waitcnt lgkmcnt(4)\n"
    "v_mfma_f32_16x16x32_bf16 v[32:35], v[64:67], v[192:195], v[32:35]\n"
    "s_lshr_b32 s66, s65, 1\n"
    "ds_read_b64_tr_b16 v[56:57], v9 offset:32768\n"
    "buffer_load_dwordx4 v3, s[40:43], s18 offen lds\n"
    "s_add_u32 m0, s63, 0xe000\n"
    "s_lshl_b32 s66, s66, 7\n"
    "v_mfma_f32_16x16x32_bf16 v[40:43], v[68:71], v[192:195], v[40:43]\n"
    "s_add_u32 s66, s66, 4096\n"
    "ds_read_b64_tr_b16 v[58:59], v9 offset:33280\n"
    "buffer_load_dwordx4 v3, s[44:47], s18 offen lds\n"
    "s_add_u32 m0, s63, 0xd000\n"
    "s_add_u32 s18, s81, 0x1c000\n"
    "v_and_b32 v12, 15, v0\n"
    "v_mfma_f32_16x16x32_bf16 v[44:47], v[68:71], v[196:199], v[44:47]\n"
    "s_lshr_b32 s67, s25, 1\n"
    "ds_read_b64_tr_b16 v[60:61], v9 offset:40960\n"
    "v_lshl_add_u32 v75, v12, 3, s66\n"
    "v_mfma_f32_16x16x32_bf16 v[36:39], v[64:67], v[196:199], v[36:39]\n"
    "s_lshl_b32 s65, s65, 1\n"
    "ds_read_b64_tr_b16 v[62:63], v9 offset:41472\n"
    "buffer_load_dwordx4 v3, s[40:43], s18 offen lds\n"
    "s_add_u32 m0, s63, 0xf000\n"
    "s_and_b32 s66, s25, 1\n"
    "s_waitcnt lgkmcnt(4)\n"
    "v_mfma_f32_16x16x32_bf16 v[32:35], v[48:51], v[200:203], v[32:35]\n"
    "s_add_u32 s65, s65, s66\n"
    "ds_read_b64_tr_b16 v[64:65], v9 offset:36864\n"
    "buffer_load_dwordx4 v3, s[44:47], s18 offen lds\n"
    "s_add_u32 m0, s63, 0x10000\n"
    "s_add_u32 s18, s81, 0x38000\n"
    "s_lshl_b32 s96, s65, 7\n"
    "v_mfma_f32_16x16x32_bf16 v[40:43], v[52:55], v[200:203], v[40:43]\n"
    "s_add_u32 s96, s96, 8192\n"
    "ds_read_b64_tr_b16 v[66:67], v9 offset:37376\n"
    "v_mfma_f32_16x16x32_bf16 v[44:47], v[52:55], v[204:207], v[44:47]\n"
    "ds_read_b64_tr_b16 v[68:69], v9 offset:45056\n"
    "buffer_load_dwordx4 v3, s[40:43], s18 offen lds\n"
    "s_add_u32 m0, s63, 0x12000\n"
    "v_lshl_add_u32 v7, v12, 3, s96\n"
    "v_and_b32 v12, 3, v0\n"
    "v_mfma_f32_16x16x32_bf16 v[36:39], v[48:51], v[204:207], v[36:39]\n"
    "s_mul_i32 s96, s65, 114688\n"
    "ds_read_b64_tr_b16 v[70:71], v9 offset:45568\n"
    "buffer_load_dwordx4 v3, s[44:47], s18 offen lds\n"
    "s_add_u32 m0, s63, 0x11000\n"
    "s_add_u32 s18, s81, 0x54000\n"
    "s_mul_i32 s98, s67, 14336\n"
    "s_waitcnt lgkmcnt(4)\n"
    "v_mfma_f32_16x16x32_bf16 v[96:99], v[56:59], v[160:163], v[96:99]\n"
    "s_add_u32 s96, s96, s98\n"
    "ds_read_b64_tr_b16 v[48:49], v8 offset:16384\n"
    "s_add_u32 s96, s96, 0x610000\n"
    "v_mfma_f32_16x16x32_bf16 v[104:107], v[60:63], v[160:163], v[104:107]\n"
    "s_add_u32 s21, s96, 57344\n"
    "ds_read_b64_tr_b16 v[50:51], v8 offset:16896\n"
    "buffer_load_dwordx4 v3, s[40:43], s18 offen lds\n"
    "s_add_u32 m0, s63, 0x13000\n"
    "s_mul_i32 s98, s67, 3072\n"
    "v_mfma_f32_16x16x32_bf16 v[108:111], v[60:63], v[164:167], v[108:111]\n"
    "s_setprio 1\n"
    "ds_read_b64_tr_b16 v[52:53], v8 offset:24576\n"
    "buffer_load_dwordx4 v3, s[44:47], s18 offen lds\n"
    "s_add_u32 m0, s63, 0x14000\n"
    "s_add_u32 s18, s81, 0x70000\n"
    "s_lshl_b32 s65, s65, 15\n"
    "s_add_u32 s98, s98, s65\n"
    "v_mfma_f32_16x16x32_bf16 v[100:103], v[56:59], v[164:167], v[100:103]\n"
    "s_add_u32 s98, s98, 0xd10000\n"
    "ds_read_b64_tr_b16 v[54:55], v8 offset:25088\n"
    "v_lshrrev_b32 v13, 3, v0\n"
    "s_waitcnt lgkmcnt(4)\n"
    "v_mfma_f32_16x16x32_bf16 v[96:99], v[64:67], v[168:171], v[96:99]\n"
    "ds_read_b64_tr_b16 v[56:57], v8 offset:20480\n"
    "buffer_load_dwordx4 v3, s[40:43], s18 offen lds\n"
    "s_add_u32 m0, s63, 0x16000\n"
    "v_and_b32 v14, 3, v13\n"
    "v_lshlrev_b32 v14, 5, v14\n"
    "v_mfma_f32_16x16x32_bf16 v[104:107], v[68:71], v[168:171], v[104:107]\n"
    "s_setprio 0\n"
    "ds_read_b64_tr_b16 v[58:59], v8 offset:20992\n"
    "buffer_load_dwordx4 v3, s[44:47], s18 offen lds\n"
    "s_add_u32 m0, s63, 0x15000\n"
    "s_add_u32 s18, s81, 0x8c000\n"
    "v_lshl_add_u32 v14, v12, 3, v14\n"
    "v_lshrrev_b32 v12, 2, v0\n"
    "v_mfma_f32_16x16x32_bf16 v[108:111], v[68:71], v[172:175], v[108:111]\n"
    "s_lshl_b32 s65, s67, 12\n"
    "ds_read_b64_tr_b16 v[60:61], v8 offset:28672\n"
    "v_lshlrev_b32 v12, 7, v12\n"
    "v_mfma_f32_16x16x32_bf16 v[100:103], v[64:67], v[172:175], v[100:103]\n"
    "ds_read_b64_tr_b16 v[62:63], v8 offset:29184\n"
    "buffer_load_dwordx4 v3, s[40:43], s18 offen lds\n"
    "s_add_u32 m0, s63, 0x17000\n"
    "v_add3_u32 v124, v12, v14, s65\n"
    "v_xor_b32 v125, 32, v124\n"
    "s_waitcnt vmcnt(13)\n"
    "s_barrier\n"
    "s_setprio 3\n"
    "s_waitcnt lgkmcnt(4)\n"
    "v_mfma_f32_16x16x32_bf16 v[32:35], v[48:51], v[208:211], v[32:35]\n"
    "ds_read_b64_tr_b16 v[64:65], v8 offset:32768\n"
    "buffer_load_dwordx4 v3, s[44:47], s18 offen lds\n"
    "s_add_u32 m0, s63, 0x18000\n"
    "v_xor_b32 v126, 64, v124\n"
    "v_xor_b32 v127, 0x60, v124\n"
    "v_mfma_f32_16x16x32_bf16 v[40:43], v[52:55], v[208:211], v[40:43]\n"
    "s_add_u32 s18, s81, 0xa8000\n"
    "ds_read_b64_tr_b16 v[66:67], v8 offset:33280\n"
    "v_and_b32 v12, 7, v0\n"
    "v_and_b32 v14, 6, v13\n"
    "v_mfma_f32_16x16x32_bf16 v[44:47], v[52:55], v[212:215], v[44:47]\n"
    "ds_read_b64_tr_b16 v[68:69], v8 offset:40960\n"
    "buffer_load_dwordx4 v3, s[40:43], s18 offen lds\n"
    "s_add_u32 m0, s63, 0x1a000\n"
    "v_xor_b32 v14, v14, v12\n"
    "v_mul_u32_u24 v116, 896, v13\n"
    "v_mfma_f32_16x16x32_bf16 v[36:39], v[48:51], v[212:215], v[36:39]\n"
    "s_and_b32 s67, s67, 1\n"
    "ds_read_b64_tr_b16 v[70:71], v8 offset:41472\n"
    "buffer_load_dwordx4 v3, s[44:47], s18 offen lds\n"
    "s_add_u32 m0, s63, 0x19000\n"
    "s_add_u32 s18, s81, 0xc4000\n"
    "v_lshl_add_u32 v14, v14, 4, v116\n"
    "s_waitcnt lgkmcnt(4)\n"
    "v_mfma_f32_16x16x32_bf16 v[32:35], v[56:59], v[216:219], v[32:35]\n"
    "s_mul_i32 s65, s67, 448\n"
    "ds_read_b64_tr_b16 v[48:49], v8 offset:36864\n"
    "v_mfma_f32_16x16x32_bf16 v[40:43], v[60:63], v[216:219], v[40:43]\n"
    "s_lshl_b32 s66, s66, 3\n"
    "ds_read_b64_tr_b16 v[50:51], v8 offset:37376\n"
    "buffer_load_dwordx4 v3, s[40:43], s18 offen lds\n"
    "s_add_u32 m0, s63, 0x1b000\n"
    "s_add_u32 s65, s65, s66\n"
    "v_mfma_f32_16x16x32_bf16 v[44:47], v[60:63], v[220:223], v[44:47]\n"
    "s_add_u32 s65, s65, s66\n"
    "ds_read_b64_tr_b16 v[52:53], v8 offset:45056\n"
    "buffer_load_dwordx4 v3, s[44:47], s18 offen lds\n"
    "s_add_u32 m0, s63, 0x20000\n"
    "s_add_u32 s18, s81, 0xe0000\n"
    "s_mul_i32 s65, s65, 896\n"
    "v_mfma_f32_16x16x32_bf16 v[36:39], v[56:59], v[220:223], v[36:39]\n"
    "s_waitcnt lgkmcnt(3)\n"
    "ds_read_b64_tr_b16 v[54:55], v8 offset:45568\n"
    "v_add_u32 v2, s65, v14\n"
    "s_mul_i32 s65, s67, 112\n"
    "v_mfma_f32_16x16x32_bf16 v[32:35], v[64:67], v[224:227], v[32:35]\n"
    "s_add_u32 s65, s65, s66\n"
    "ds_read_b64_tr_b16 v[56:57], v9 offset:49152\n"
    "v_mfma_f32_16x16x32_bf16 v[40:43], v[68:71], v[224:227], v[40:43]\n"
    "s_mul_i32 s65, s65, 896\n"
    "ds_read_b64_tr_b16 v[58:59], v9 offset:49664\n"
    "v_add_u32 v4, s65, v14\n"
    "v_mfma_f32_16x16x32_bf16 v[44:47], v[68:71], v[228:231], v[44:47]\n"
    "s_mul_i32 s65, s25, 7168\n"
    "ds_read_b64_tr_b16 v[60:61], v9 offset:57344\n"
    "v_lshl_add_u32 v14, v12, 4, v116\n"
    "v_add_u32 v6, s65, v14\n"
    "v_mfma_f32_16x16x32_bf16 v[36:39], v[64:67], v[228:231], v[36:39]\n"
    "s_lshl_b32 s65, s79, 13\n"
    "ds_read_b64_tr_b16 v[62:63], v9 offset:57856\n"
    "v_add_u32 v78, s65, v1\n"
    "s_waitcnt lgkmcnt(4)\n"
    "v_mfma_f32_16x16x32_bf16 v[32:35], v[48:51], v[232:235], v[32:35]\n"
    "ds_read_b64_tr_b16 v[64:65], v9 offset:53248\n"
    "v_add_u32 v73, 0xc000, v78\n"
    "v_add_u32 v74, 0xc000, v73\n"
    "v_mfma_f32_16x16x32_bf16 v[40:43], v[52:55], v[232:235], v[40:43]\n"
    "s_and_b32 s65, s25, 3\n"
    "ds_read_b64_tr_b16 v[66:67], v9 offset:53760\n"
    "s_lshl_b32 s65, s65, 12\n"
    "v_mfma_f32_16x16x32_bf16 v[44:47], v[52:55], v[236:239], v[44:47]\n"
    "s_add_u32 s65, s65, 0x18000\n"
    "ds_read_b64_tr_b16 v[68:69], v9 offset:61440\n"
    "v_add_u32 v76, s65, v1\n"
    "v_mfma_f32_16x16x32_bf16 v[36:39], v[48:51], v[236:239], v[36:39]\n"
    "s_and_b32 s65, s27, 1\n"
    "ds_read_b64_tr_b16 v[70:71], v9 offset:61952\n"
    "v_and_b32 v13, 1, v13\n"
    "s_waitcnt lgkmcnt(4)\n"
    "v_mfma_f32_16x16x32_bf16 v[96:99], v[56:59], v[176:179], v[96:99]\n"
    "v_add_u32 v9, 0xffff0000, v9\n"
    "ds_read_b64_tr_b16 v[48:49], v9 offset:0\n"
    "v_xor_b32 v13, s65, v13\n"
    "v_lshrrev_b32 v14, 4, v0\n"
    "v_mfma_f32_16x16x32_bf16 v[104:107], v[60:63], v[176:179], v[104:107]\n"
    "ds_read_b64_tr_b16 v[50:51], v9 offset:512\n"
    "v_lshlrev_b32 v12, 4, v12\n"
    "v_lshl_add_u32 v12, v14, 7, v12\n"
    "v_mfma_f32_16x16x32_bf16 v[108:111], v[60:63], v[180:183], v[108:111]\n"
    "ds_read_b64_tr_b16 v[52:53], v9 offset:8192\n"
    "v_lshl_add_u32 v77, v13, 14, v12\n"
    "v_mfma_f32_16x16x32_bf16 v[100:103], v[56:59], v[180:183], v[100:103]\n"
    "s_waitcnt lgkmcnt(3)\n"
    "ds_read_b64_tr_b16 v[54:55], v9 offset:8704\n"
    "v_mul_f32 v88, v32, v112\n"
    "v_mul_f32 v89, v33, v112\n"
    "Lst0_1:\n"
    "s_cbranch_execz Lr0_1\n"
    "v_mfma_f32_16x16x32_bf16 v[96:99], v[64:67], v[184:187], v[96:99]\n"
    "ds_read_b64_tr_b16 v[56:57], v9 offset:4096\n"
    "v_mul_f32 v90, v34, v112\n"
    "v_mul_f32 v91, v35, v112\n"
    "v_mfma_f32_16x16x32_bf16 v[104:107], v[68:71], v[184:187], v[104:107]\n"
    "ds_read_b64_tr_b16 v[58:59], v9 offset:4608\n"
    "v_exp_f32 v88, v88\n"
    "v_mfma_f32_16x16x32_bf16 v[108:111], v[68:71], v[188:191], v[108:111]\n"
    "ds_read_b64_tr_b16 v[60:61], v9 offset:12288\n"
    "v_mul_f32 v40, v32, v40\n"
    "v_mfma_f32_16x16x32_bf16 v[100:103], v[64:67], v[188:191], v[100:103]\n"
    "s_waitcnt lgkmcnt(3)\n"
    "ds_read_b64_tr_b16 v[62:63], v9 offset:12800\n"
    "v_exp_f32 v89, v89\n"
    "v_mfma_f32_16x16x32_bf16 v[96:99], v[48:51], v[192:195], v[96:99]\n"
    "ds_read_b64_tr_b16 v[64:65], v9 offset:16384\n"
    "v_mul_f32 v41, v33, v41\n"
    "v_mfma_f32_16x16x32_bf16 v[104:107], v[52:55], v[192:195], v[104:107]\n"
    "ds_read_b64_tr_b16 v[66:67], v9 offset:16896\n"
    "v_exp_f32 v90, v90\n"
    "v_mfma_f32_16x16x32_bf16 v[108:111], v[52:55], v[196:199], v[108:111]\n"
    "ds_read_b64_tr_b16 v[68:69], v9 offset:24576\n"
    "v_mul_f32 v42, v34, v42\n"
    "v_mfma_f32_16x16x32_bf16 v[100:103], v[48:51], v[196:199], v[100:103]\n"
    "s_waitcnt lgkmcnt(3)\n"
    "ds_read_b64_tr_b16 v[70:71], v9 offset:25088\n"
    "v_exp_f32 v91, v91\n"
    "v_mfma_f32_16x16x32_bf16 v[96:99], v[56:59], v[200:203], v[96:99]\n"
    "ds_read_b64_tr_b16 v[48:49], v9 offset:20480\n"
    "v_mul_f32 v43, v35, v43\n"
    "v_fma_f32 v88, v88, v114, v114\n"
    "v_mfma_f32_16x16x32_bf16 v[104:107], v[60:63], v[200:203], v[104:107]\n"
    "ds_read_b64_tr_b16 v[50:51], v9 offset:20992\n"
    "v_fma_f32 v89, v89, v114, v114\n"
    "v_mfma_f32_16x16x32_bf16 v[108:111], v[60:63], v[204:207], v[108:111]\n"
    "ds_read_b64_tr_b16 v[52:53], v9 offset:28672\n"
    "v_fma_f32 v90, v90, v114, v114\n"
    "v_fma_f32 v91, v91, v114, v114\n"
    "v_mfma_f32_16x16x32_bf16 v[100:103], v[56:59], v[204:207], v[100:103]\n"
    "s_waitcnt lgkmcnt(3)\n"
    "ds_read_b64_tr_b16 v[54:55], v9 offset:29184\n"
    "v_rcp_f32 v88, v88\n"
    "v_mfma_f32_16x16x32_bf16 v[96:99], v[64:67], v[208:211], v[96:99]\n"
    "s_setprio 2\n"
    "ds_read_b64_tr_b16 v[56:57], v9 offset:32768\n"
    "buffer_load_dwordx4 v3, s[40:43], s18 offen lds\n"
    "s_add_u32 m0, s63, 0x22000\n"
    "v_rcp_f32 v89, v89\n"
    "v_mfma_f32_16x16x32_bf16 v[104:107], v[68:71], v[208:211], v[104:107]\n"
    "s_setprio 1\n"
    "ds_read_b64_tr_b16 v[58:59], v9 offset:33280\n"
    "buffer_load_dwordx4 v3, s[44:47], s18 offen lds\n"
    "s_add_u32 m0, s63, 0x21000\n"
    "v_rcp_f32 v90, v90\n"
    "v_mfma_f32_16x16x32_bf16 v[108:111], v[68:71], v[212:215], v[108:111]\n"
    "s_add_u32 s18, s81, 0xfc000\n"
    "ds_read_b64_tr_b16 v[60:61], v9 offset:40960\n"
    "v_mfma_f32_16x16x32_bf16 v[100:103], v[64:67], v[212:215], v[100:103]\n"
    "s_waitcnt lgkmcnt(3)\n"
    "ds_read_b64_tr_b16 v[62:63], v9 offset:41472\n"
    "buffer_load_dwordx4 v3, s[40:43], s18 offen lds\n"
    "v_rcp_f32 v91, v91\n"
    "v_mfma_f32_16x16x32_bf16 v[96:99], v[48:51], v[216:219], v[96:99]\n"
    "s_add_u32 m0, s63, 0x23000\n"
    "ds_read_b64_tr_b16 v[64:65], v9 offset:36864\n"
    "buffer_load_dwordx4 v3, s[44:47], s18 offen lds\n"
    "s_add_u32 m0, s63, 0x24000\n"
    "s_add_u32 s18, s81, 0x118000\n"
    "v_mul_f32 v40, v40, v88\n"
    "v_mul_f32 v41, v41, v89\n"
    "v_mfma_f32_16x16x32_bf16 v[104:107], v[52:55], v[216:219], v[104:107]\n"
    "s_setprio 0\n"
    "ds_read_b64_tr_b16 v[66:67], v9 offset:37376\n"
    "v_mul_f32 v42, v42, v90\n"
    "v_mfma_f32_16x16x32_bf16 v[108:111], v[52:55], v[220:223], v[108:111]\n"
    "ds_read_b64_tr_b16 v[68:69], v9 offset:45056\n"
    "buffer_load_dwordx4 v3, s[40:43], s18 offen lds\n"
    "v_mul_f32 v43, v43, v91\n"
    "v_cvt_pk_bf16_f32 v240, v40, v41\n"
    "v_mfma_f32_16x16x32_bf16 v[100:103], v[48:51], v[220:223], v[100:103]\n"
    "s_add_u32 m0, s63, 0x26000\n"
    "ds_read_b64_tr_b16 v[70:71], v9 offset:45568\n"
    "buffer_load_dwordx4 v3, s[44:47], s18 offen lds\n"
    "s_add_u32 m0, s63, 0x25000\n"
    "s_add_u32 s18, s81, 0x134000\n"
    "v_cvt_pk_bf16_f32 v241, v42, v43\n"
    "s_waitcnt vmcnt(6)\n"
    "s_barrier\n"
    "s_setprio 3\n"
    "s_waitcnt lgkmcnt(4)\n"
    "v_mfma_f32_16x16x32_bf16 v[96:99], v[56:59], v[224:227], v[96:99]\n"
    "ds_read_b64_tr_b16 v[48:49], v8 offset:49152\n"
    "v_mul_f32 v92, v36, v113\n"
    "v_mul_f32 v93, v37, v113\n"
    "v_mfma_f32_16x16x32_bf16 v[104:107], v[60:63], v[224:227], v[104:107]\n"
    "ds_read_b64_tr_b16 v[50:51], v8 offset:49664\n"
    "buffer_load_dwordx4 v3, s[40:43], s18 offen lds\n"
    "v_mul_f32 v94, v38, v113\n"
    "v_mul_f32 v95, v39, v113\n"
    "v_mfma_f32_16x16x32_bf16 v[108:111], v[60:63], v[228:231], v[108:111]\n"
    "s_add_u32 m0, s63, 0x27000\n"
    "ds_read_b64_tr_b16 v[52:53], v8 offset:57344\n"
    "buffer_load_dwordx4 v3, s[44:47], s18 offen lds\n"
    "s_add_u32 m0, s63, 0x1c000\n"
    "s_add_u32 s18, s81, 0x150000\n"
    "v_exp_f32 v92, v92\n"
    "v_mfma_f32_16x16x32_bf16 v[100:103], v[56:59], v[228:231], v[100:103]\n"
    "s_waitcnt lgkmcnt(3)\n"
    "ds_read_b64_tr_b16 v[54:55], v8 offset:57856\n"
    "v_mul_f32 v44, v36, v44\n"
    "v_mfma_f32_16x16x32_bf16 v[96:99], v[64:67], v[232:235], v[96:99]\n"
    "ds_read_b64_tr_b16 v[56:57], v8 offset:53248\n"
    "buffer_load_dwordx4 v3, s[40:43], s18 offen lds\n"
    "v_exp_f32 v93, v93\n"
    "v_mfma_f32_16x16x32_bf16 v[104:107], v[68:71], v[232:235], v[104:107]\n"
    "s_add_u32 m0, s63, 0x1e000\n"
    "ds_read_b64_tr_b16 v[58:59], v8 offset:53760\n"
    "buffer_load_dwordx4 v3, s[44:47], s18 offen lds\n"
    "s_add_u32 m0, s63, 0x1d000\n"
    "v_mul_f32 v45, v37, v45\n"
    "v_mfma_f32_16x16x32_bf16 v[108:111], v[68:71], v[236:239], v[108:111]\n"
    "s_add_u32 s18, s81, 0x16c000\n"
    "ds_read_b64_tr_b16 v[60:61], v8 offset:61440\n"
    "v_exp_f32 v94, v94\n"
    "v_mfma_f32_16x16x32_bf16 v[100:103], v[64:67], v[236:239], v[100:103]\n"
    "s_waitcnt lgkmcnt(3)\n"
    "ds_read_b64_tr_b16 v[62:63], v8 offset:61952\n"
    "buffer_load_dwordx4 v3, s[40:43], s18 offen lds\n"
    "v_mul_f32 v46, v38, v46\n"
    "v_mfma_f32_16x16x32_bf16 v[16:19], v[48:51], v[128:131], 0\n"
    "s_add_u32 m0, s63, 0x1f000\n"
    "v_add_u32 v8, 0x10000, v8\n"
    "ds_read_b64_tr_b16 v[64:65], v8 offset:0\n"
    "buffer_load_dwordx4 v3, s[44:47], s18 offen lds\n"
    "v_exp_f32 v95, v95\n"
    "v_mfma_f32_16x16x32_bf16 v[24:27], v[52:55], v[128:131], 0\n"
    "ds_read_b64_tr_b16 v[66:67], v8 offset:512\n"
    "v_mul_f32 v47, v39, v47\n"
    "v_fma_f32 v92, v92, v115, v115\n"
    "v_mfma_f32_16x16x32_bf16 v[28:31], v[52:55], v[132:135], 0\n"
    "ds_read_b64_tr_b16 v[68:69], v8 offset:8192\n"
    "v_fma_f32 v93, v93, v115, v115\n"
    "v_mfma_f32_16x16x32_bf16 v[20:23], v[48:51], v[132:135], 0\n"
    "s_waitcnt lgkmcnt(3)\n"
    "ds_read_b64_tr_b16 v[70:71], v8 offset:8704\n"
    "v_fma_f32 v94, v94, v115, v115\n"
    "v_fma_f32 v95, v95, v115, v115\n"
    "v_mfma_f32_16x16x32_bf16 v[16:19], v[56:59], v[136:139], v[16:19]\n"
    "ds_read_b64_tr_b16 v[48:49], v8 offset:4096\n"
    "v_rcp_f32 v92, v92\n"
    "v_mfma_f32_16x16x32_bf16 v[24:27], v[60:63], v[136:139], v[24:27]\n"
    "ds_read_b64_tr_b16 v[50:51], v8 offset:4608\n"
    "v_rcp_f32 v93, v93\n"
    "v_mfma_f32_16x16x32_bf16 v[28:31], v[60:63], v[140:143], v[28:31]\n"
    "ds_read_b64_tr_b16 v[52:53], v8 offset:12288\n"
    "v_rcp_f32 v94, v94\n"
    "v_mfma_f32_16x16x32_bf16 v[20:23], v[56:59], v[140:143], v[20:23]\n"
    "s_waitcnt lgkmcnt(3)\n"
    "ds_read_b64_tr_b16 v[54:55], v8 offset:12800\n"
    "v_mfma_f32_16x16x32_bf16 v[16:19], v[64:67], v[144:147], v[16:19]\n"
    "ds_read_b64_tr_b16 v[56:57], v8 offset:16384\n"
    "v_rcp_f32 v95, v95\n"
    "v_mfma_f32_16x16x32_bf16 v[24:27], v[68:71], v[144:147], v[24:27]\n"
    "ds_read_b64_tr_b16 v[58:59], v8 offset:16896\n"
    "v_mul_f32 v44, v44, v92\n"
    "v_mul_f32 v45, v45, v93\n"
    "v_mfma_f32_16x16x32_bf16 v[28:31], v[68:71], v[148:151], v[28:31]\n"
    "ds_read_b64_tr_b16 v[60:61], v8 offset:24576\n"
    "v_mul_f32 v46, v46, v94\n"
    "v_mfma_f32_16x16x32_bf16 v[20:23], v[64:67], v[148:151], v[20:23]\n"
    "s_waitcnt lgkmcnt(3)\n"
    "ds_read_b64_tr_b16 v[62:63], v8 offset:25088\n"
    "v_mul_f32 v47, v47, v95\n"
    "v_cvt_pk_bf16_f32 v244, v44, v45\n"
    "v_mfma_f32_16x16x32_bf16 v[16:19], v[48:51], v[152:155], v[16:19]\n"
    "ds_read_b64_tr_b16 v[64:65], v8 offset:20480\n"
    "v_cvt_pk_bf16_f32 v245, v46, v47\n"
    "v_mfma_f32_16x16x32_bf16 v[24:27], v[52:55], v[152:155], v[24:27]\n"
    "ds_read_b64_tr_b16 v[66:67], v8 offset:20992\n"
    "v_mul_f32 v88, v96, v112\n"
    "v_mul_f32 v89, v97, v112\n"
    "v_mul_f32 v90, v98, v112\n"
    "v_mfma_f32_16x16x32_bf16 v[28:31], v[52:55], v[156:159], v[28:31]\n"
    "ds_read_b64_tr_b16 v[68:69], v8 offset:28672\n"
    "v_mul_f32 v91, v99, v112\n"
    "v_exp_f32 v88, v88\n"
    "v_mfma_f32_16x16x32_bf16 v[20:23], v[48:51], v[156:159], v[20:23]\n"
    "s_waitcnt lgkmcnt(3)\n"
    "ds_read_b64_tr_b16 v[70:71], v8 offset:29184\n"
    "v_mul_f32 v104, v96, v104\n"
    "v_mfma_f32_16x16x32_bf16 v[16:19], v[56:59], v[160:163], v[16:19]\n"
    "ds_read_b64_tr_b16 v[48:49], v8 offset:32768\n"
    "v_exp_f32 v89, v89\n"
    "v_mfma_f32_16x16x32_bf16 v[24:27], v[60:63], v[160:163], v[24:27]\n"
    "ds_read_b64_tr_b16 v[50:51], v8 offset:33280\n"
    "v_mul_f32 v105, v97, v105\n"
    "v_exp_f32 v90, v90\n"
    "v_mfma_f32_16x16x32_bf16 v[28:31], v[60:63], v[164:167], v[28:31]\n"
    "s_setprio 1\n"
    "ds_read_b64_tr_b16 v[52:53], v8 offset:40960\n"
    "v_mul_f32 v106, v98, v106\n"
    "v_mfma_f32_16x16x32_bf16 v[20:23], v[56:59], v[164:167], v[20:23]\n"
    "s_waitcnt lgkmcnt(3)\n"
    "ds_read_b64_tr_b16 v[54:55], v8 offset:41472\n"
    "v_exp_f32 v91, v91\n"
    "v_mfma_f32_16x16x32_bf16 v[16:19], v[64:67], v[168:171], v[16:19]\n"
    "ds_read_b64_tr_b16 v[56:57], v8 offset:36864\n"
    "v_mul_f32 v107, v99, v107\n"
    "v_fma_f32 v88, v88, v114, v114\n"
    "v_mfma_f32_16x16x32_bf16 v[24:27], v[68:71], v[168:171], v[24:27]\n"
    "s_setprio 0\n"
    "ds_read_b64_tr_b16 v[58:59], v8 offset:37376\n"
    "v_fma_f32 v89, v89, v114, v114\n"
    "v_fma_f32 v90, v90, v114, v114\n"
    "v_mfma_f32_16x16x32_bf16 v[28:31], v[68:71], v[172:175], v[28:31]\n"
    "ds_read_b64_tr_b16 v[60:61], v8 offset:45056\n"
    "v_fma_f32 v91, v91, v114, v114\n"
    "v_rcp_f32 v88, v88\n"
    "v_mfma_f32_16x16x32_bf16 v[20:23], v[64:67], v[172:175], v[20:23]\n"
    "ds_read_b64_tr_b16 v[62:63], v8 offset:45568\n"
    "v_rcp_f32 v89, v89\n"
    "s_waitcnt vmcnt(0)\n"
    "s_barrier\n"
    "s_setprio 3\n"
    "s_waitcnt lgkmcnt(4)\n"
    "v_mfma_f32_16x16x32_bf16 v[16:19], v[48:51], v[176:179], v[16:19]\n"
    "v_add_u32 v8, 0x8000, v8\n"
    "ds_read_b64_tr_b16 v[64:65], v8 offset:32768\n"
    "v_rcp_f32 v90, v90\n"
    "v_mfma_f32_16x16x32_bf16 v[24:27], v[52:55], v[176:179], v[24:27]\n"
    "ds_read_b64_tr_b16 v[66:67], v8 offset:33280\n"
    "v_rcp_f32 v91, v91\n"
    "v_mfma_f32_16x16x32_bf16 v[28:31], v[52:55], v[180:183], v[28:31]\n"
    "ds_read_b64_tr_b16 v[68:69], v8 offset:40960\n"
    "v_mul_f32 v104, v104, v88\n"
    "v_mfma_f32_16x16x32_bf16 v[20:23], v[48:51], v[180:183], v[20:23]\n"
    "s_waitcnt lgkmcnt(3)\n"
    "ds_read_b64_tr_b16 v[70:71], v8 offset:41472\n"
    "v_mul_f32 v105, v105, v89\n"
    "v_mul_f32 v106, v106, v90\n"
    "v_mfma_f32_16x16x32_bf16 v[16:19], v[56:59], v[184:187], v[16:19]\n"
    "ds_read_b64_tr_b16 v[48:49], v8 offset:36864\n"
    "v_mul_f32 v107, v107, v91\n"
    "v_cvt_pk_bf16_f32 v242, v104, v105\n"
    "v_mfma_f32_16x16x32_bf16 v[24:27], v[60:63], v[184:187], v[24:27]\n"
    "ds_read_b64_tr_b16 v[50:51], v8 offset:37376\n"
    "v_cvt_pk_bf16_f32 v243, v106, v107\n"
    "v_mul_f32 v92, v100, v113\n"
    "v_mfma_f32_16x16x32_bf16 v[28:31], v[60:63], v[188:191], v[28:31]\n"
    "ds_read_b64_tr_b16 v[52:53], v8 offset:45056\n"
    "v_mul_f32 v93, v101, v113\n"
    "v_mul_f32 v94, v102, v113\n"
    "v_mfma_f32_16x16x32_bf16 v[20:23], v[56:59], v[188:191], v[20:23]\n"
    "s_waitcnt lgkmcnt(3)\n"
    "ds_read_b64_tr_b16 v[54:55], v8 offset:45568\n"
    "v_mul_f32 v95, v103, v113\n"
    "v_exp_f32 v92, v92\n"
    "v_mfma_f32_16x16x32_bf16 v[16:19], v[64:67], v[192:195], v[16:19]\n"
    "ds_read_b64_tr_b16 v[56:57], v8 offset:49152\n"
    "v_mul_f32 v108, v100, v108\n"
    "v_mfma_f32_16x16x32_bf16 v[24:27], v[68:71], v[192:195], v[24:27]\n"
    "ds_read_b64_tr_b16 v[58:59], v8 offset:49664\n"
    "v_exp_f32 v93, v93\n"
    "v_mfma_f32_16x16x32_bf16 v[28:31], v[68:71], v[196:199], v[28:31]\n"
    "ds_read_b64_tr_b16 v[60:61], v8 offset:57344\n"
    "v_mul_f32 v109, v101, v109\n"
    "v_exp_f32 v94, v94\n"
    "v_mfma_f32_16x16x32_bf16 v[20:23], v[64:67], v[196:199], v[20:23]\n"
    "s_waitcnt lgkmcnt(3)\n"
    "ds_read_b64_tr_b16 v[62:63], v8 offset:57856\n"
    "v_mul_f32 v110, v102, v110\n"
    "v_mfma_f32_16x16x32_bf16 v[16:19], v[48:51], v[200:203], v[16:19]\n"
    "ds_read_b64_tr_b16 v[64:65], v8 offset:53248\n"
    "v_exp_f32 v95, v95\n"
    "v_mfma_f32_16x16x32_bf16 v[24:27], v[52:55], v[200:203], v[24:27]\n"
    "ds_read_b64_tr_b16 v[66:67], v8 offset:53760\n"
    "v_mul_f32 v111, v103, v111\n"
    "v_fma_f32 v92, v92, v115, v115\n"
    "v_mfma_f32_16x16x32_bf16 v[28:31], v[52:55], v[204:207], v[28:31]\n"
    "ds_read_b64_tr_b16 v[68:69], v8 offset:61440\n"
    "v_fma_f32 v93, v93, v115, v115\n"
    "v_fma_f32 v94, v94, v115, v115\n"
    "v_mfma_f32_16x16x32_bf16 v[20:23], v[48:51], v[204:207], v[20:23]\n"
    "s_waitcnt lgkmcnt(3)\n"
    "ds_read_b64_tr_b16 v[70:71], v8 offset:61952\n"
    "v_fma_f32 v95, v95, v115, v115\n"
    "v_rcp_f32 v92, v92\n"
    "v_mfma_f32_16x16x32_bf16 v[16:19], v[56:59], v[208:211], v[16:19]\n"
    "ds_read_b64_tr_b16 v[48:49], v8 offset:16384\n"
    "v_rcp_f32 v93, v93\n"
    "v_mfma_f32_16x16x32_bf16 v[24:27], v[60:63], v[208:211], v[24:27]\n"
    "ds_read_b64_tr_b16 v[50:51], v8 offset:16896\n"
    "v_rcp_f32 v94, v94\n"
    "v_mfma_f32_16x16x32_bf16 v[28:31], v[60:63], v[212:215], v[28:31]\n"
    "ds_read_b64_tr_b16 v[52:53], v8 offset:24576\n"
    "v_rcp_f32 v95, v95\n"
    "v_mfma_f32_16x16x32_bf16 v[20:23], v[56:59], v[212:215], v[20:23]\n"
    "s_waitcnt lgkmcnt(3)\n"
    "ds_read_b64_tr_b16 v[54:55], v8 offset:25088\n"
    "v_mul_f32 v108, v108, v92\n"
    "v_mfma_f32_16x16x32_bf16 v[16:19], v[64:67], v[216:219], v[16:19]\n"
    "ds_read_b64_tr_b16 v[56:57], v8 offset:20480\n"
    "v_mul_f32 v109, v109, v93\n"
    "v_mul_f32 v110, v110, v94\n"
    "v_mfma_f32_16x16x32_bf16 v[24:27], v[68:71], v[216:219], v[24:27]\n"
    "ds_read_b64_tr_b16 v[58:59], v8 offset:20992\n"
    "v_mul_f32 v111, v111, v95\n"
    "v_cvt_pk_bf16_f32 v246, v108, v109\n"
    "v_mfma_f32_16x16x32_bf16 v[28:31], v[68:71], v[220:223], v[28:31]\n"
    "ds_read_b64_tr_b16 v[60:61], v8 offset:28672\n"
    "buffer_store_dwordx4 v[240:243], v1, s[56:59], s82 offen sc1\n"
    "v_cvt_pk_bf16_f32 v247, v110, v111\n"
    "v_mfma_f32_16x16x32_bf16 v[20:23], v[64:67], v[220:223], v[20:23]\n"
    "s_add_u32 s82, s82, s76\n"
    "ds_read_b64_tr_b16 v[62:63], v8 offset:29184\n"
    "buffer_store_dwordx4 v[244:247], v1, s[56:59], s82 offen sc1\n"
    "s_waitcnt lgkmcnt(4)\n"
    "v_mfma_f32_16x16x32_bf16 v[16:19], v[48:51], v[224:227], v[16:19]\n"
    "ds_read_b64_tr_b16 v[64:65], v9 offset:49152\n"
    "v_mfma_f32_16x16x32_bf16 v[24:27], v[52:55], v[224:227], v[24:27]\n"
    "ds_read_b64_tr_b16 v[66:67], v9 offset:49664\n"
    "v_mfma_f32_16x16x32_bf16 v[28:31], v[52:55], v[228:231], v[28:31]\n"
    "ds_read_b64_tr_b16 v[68:69], v9 offset:57344\n"
    "v_mfma_f32_16x16x32_bf16 v[20:23], v[48:51], v[228:231], v[20:23]\n"
    "s_waitcnt lgkmcnt(3)\n"
    "ds_read_b64_tr_b16 v[70:71], v9 offset:57856\n"
    "v_mfma_f32_16x16x32_bf16 v[16:19], v[56:59], v[232:235], v[16:19]\n"
    "ds_read_b64_tr_b16 v[48:49], v9 offset:53248\n"
    "v_mfma_f32_16x16x32_bf16 v[24:27], v[60:63], v[232:235], v[24:27]\n"
    "ds_read_b64_tr_b16 v[50:51], v9 offset:53760\n"
    "v_mfma_f32_16x16x32_bf16 v[28:31], v[60:63], v[236:239], v[28:31]\n"
    "ds_read_b64_tr_b16 v[52:53], v9 offset:61440\n"
    "v_mfma_f32_16x16x32_bf16 v[20:23], v[56:59], v[236:239], v[20:23]\n"
    "s_waitcnt lgkmcnt(3)\n"
    "ds_read_b64_tr_b16 v[54:55], v9 offset:61952\n"
    "v_mfma_f32_16x16x32_bf16 v[32:35], v[64:67], v[128:131], 0\n"
    "v_add_u32 v9, 0x10000, v9\n"
    "ds_read_b64_tr_b16 v[56:57], v9 offset:0\n"
    "v_mfma_f32_16x16x32_bf16 v[40:43], v[68:71], v[128:131], 0\n"
    "ds_read_b64_tr_b16 v[58:59], v9 offset:512\n"
    "v_mfma_f32_16x16x32_bf16 v[44:47], v[68:71], v[132:135], 0\n"
    "ds_read_b64_tr_b16 v[60:61], v9 offset:8192\n"
    "v_mfma_f32_16x16x32_bf16 v[36:39], v[64:67], v[132:135], 0\n"
    "s_waitcnt lgkmcnt(3)\n"
    "ds_read_b64_tr_b16 v[62:63], v9 offset:8704\n"
    "v_mfma_f32_16x16x32_bf16 v[32:35], v[48:51], v[136:139], v[32:35]\n"
    "ds_read_b64_tr_b16 v[64:65], v9 offset:4096\n"
    "v_mfma_f32_16x16x32_bf16 v[40:43], v[52:55], v[136:139], v[40:43]\n"
    "ds_read_b64_tr_b16 v[66:67], v9 offset:4608\n"
    "v_mul_f32 v88, v16, v112\n"
    "v_mul_f32 v89, v17, v112\n"
    "v_mfma_f32_16x16x32_bf16 v[44:47], v[52:55], v[140:143], v[44:47]\n"
    "ds_read_b64_tr_b16 v[68:69], v9 offset:12288\n"
    "v_mul_f32 v90, v18, v112\n"
    "v_mfma_f32_16x16x32_bf16 v[36:39], v[48:51], v[140:143], v[36:39]\n"
    "s_waitcnt lgkmcnt(3)\n"
    "ds_read_b64_tr_b16 v[70:71], v9 offset:12800\n"
    "v_mul_f32 v91, v19, v112\n"
    "v_exp_f32 v88, v88\n"
    "v_mfma_f32_16x16x32_bf16 v[32:35], v[56:59], v[144:147], v[32:35]\n"
    "ds_read_b64_tr_b16 v[48:49], v9 offset:16384\n"
    "v_mfma_f32_16x16x32_bf16 v[40:43], v[60:63], v[144:147], v[40:43]\n"
    "ds_read_b64_tr_b16 v[50:51], v9 offset:16896\n"
    "v_mul_f32 v24, v16, v24\n"
    "v_mfma_f32_16x16x32_bf16 v[44:47], v[60:63], v[148:151], v[44:47]\n"
    "ds_read_b64_tr_b16 v[52:53], v9 offset:24576\n"
    "v_exp_f32 v89, v89\n"
    "v_mfma_f32_16x16x32_bf16 v[36:39], v[56:59], v[148:151], v[36:39]\n"
    "s_waitcnt lgkmcnt(3)\n"
    "ds_read_b64_tr_b16 v[54:55], v9 offset:25088\n"
    "v_mul_f32 v25, v17, v25\n"
    "v_mfma_f32_16x16x32_bf16 v[32:35], v[64:67], v[152:155], v[32:35]\n"
    "ds_read_b64_tr_b16 v[56:57], v9 offset:20480\n"
    "v_exp_f32 v90, v90\n"
    "v_mfma_f32_16x16x32_bf16 v[40:43], v[68:71], v[152:155], v[40:43]\n"
    "s_setprio 2\n"
    "ds_read_b64_tr_b16 v[58:59], v9 offset:20992\n"
    "v_mul_f32 v26, v18, v26\n"
    "v_mfma_f32_16x16x32_bf16 v[44:47], v[68:71], v[156:159], v[44:47]\n"
    "s_setprio 1\n"
    "ds_read_b64_tr_b16 v[60:61], v9 offset:28672\n"
    "v_exp_f32 v91, v91\n"
    "v_mfma_f32_16x16x32_bf16 v[36:39], v[64:67], v[156:159], v[36:39]\n"
    "s_waitcnt lgkmcnt(3)\n"
    "ds_read_b64_tr_b16 v[62:63], v9 offset:29184\n"
    "v_mul_f32 v27, v19, v27\n"
    "v_mfma_f32_16x16x32_bf16 v[32:35], v[48:51], v[160:163], v[32:35]\n"
    "ds_read_b64_tr_b16 v[64:65], v9 offset:32768\n"
    "v_fma_f32 v88, v88, v114, v114\n"
    "v_mfma_f32_16x16x32_bf16 v[40:43], v[52:55], v[160:163], v[40:43]\n"
    "ds_read_b64_tr_b16 v[66:67], v9 offset:33280\n"
    "v_fma_f32 v89, v89, v114, v114\n"
    "v_fma_f32 v90, v90, v114, v114\n"
    "v_mfma_f32_16x16x32_bf16 v[44:47], v[52:55], v[164:167], v[44:47]\n"
    "ds_read_b64_tr_b16 v[68:69], v9 offset:40960\n"
    "v_fma_f32 v91, v91, v114, v114\n"
    "v_mfma_f32_16x16x32_bf16 v[36:39], v[48:51], v[164:167], v[36:39]\n"
    "s_waitcnt lgkmcnt(3)\n"
    "ds_read_b64_tr_b16 v[70:71], v9 offset:41472\n"
    "v_rcp_f32 v88, v88\n"
    "v_mfma_f32_16x16x32_bf16 v[32:35], v[56:59], v[168:171], v[32:35]\n"
    "s_setprio 0\n"
    "ds_read_b64_tr_b16 v[48:49], v9 offset:36864\n"
    "v_rcp_f32 v89, v89\n"
    "v_mfma_f32_16x16x32_bf16 v[40:43], v[60:63], v[168:171], v[40:43]\n"
    "ds_read_b64_tr_b16 v[50:51], v9 offset:37376\n"
    "v_mfma_f32_16x16x32_bf16 v[44:47], v[60:63], v[172:175], v[44:47]\n"
    "ds_read_b64_tr_b16 v[52:53], v9 offset:45056\n"
    "v_rcp_f32 v90, v90\n"
    "v_mfma_f32_16x16x32_bf16 v[36:39], v[56:59], v[172:175], v[36:39]\n"
    "ds_read_b64_tr_b16 v[54:55], v9 offset:45568\n"
    "v_rcp_f32 v91, v91\n"
    "s_barrier\n"
    "s_setprio 1\n"
    "s_waitcnt lgkmcnt(4)\n"
    "v_mfma_f32_16x16x32_bf16 v[32:35], v[64:67], v[176:179], v[32:35]\n"
    "v_add_u32 v9, 0x8000, v9\n"
    "ds_read_b64_tr_b16 v[56:57], v9 offset:32768\n"
    "buffer_load_dwordx4 v[104:107], v1, s[56:59], s92 offen offset:0 sc1\n"
    "v_mfma_f32_16x16x32_bf16 v[40:43], v[68:71], v[176:179], v[40:43]\n"
    "ds_read_b64_tr_b16 v[58:59], v9 offset:33280\n"
    "buffer_load_dwordx4 v[108:111], v1, s[56:59], s92 offen offset:1024 sc1\n"
    "v_mul_f32 v24, v24, v88\n"
    "v_mul_f32 v25, v25, v89\n"
    "v_mfma_f32_16x16x32_bf16 v[44:47], v[68:71], v[180:183], v[44:47]\n"
    "ds_read_b64_tr_b16 v[60:61], v9 offset:40960\n"
    "v_mul_f32 v26, v26, v90\n"
    "v_mfma_f32_16x16x32_bf16 v[36:39], v[64:67], v[180:183], v[36:39]\n"
    "s_add_u32 m0, s17, 0xc000\n"
    "ds_read_b64_tr_b16 v[62:63], v9 offset:41472\n"
    "s_add_u32 s11, s90, 0x0\n"
    "buffer_load_dwordx4 v1, s[56:59], s11 offen sc1 lds\n"
    "v_mul_f32 v27, v27, v91\n"
    "s_waitcnt lgkmcnt(4)\n"
    "v_mfma_f32_16x16x32_bf16 v[32:35], v[48:51], v[184:187], v[32:35]\n"
    "ds_read_b64_tr_b16 v[64:65], v9 offset:36864\n"
    "v_cvt_pk_bf16_f32 v240, v24, v25\n"
    "v_cvt_pk_bf16_f32 v241, v26, v27\n"
    "v_mfma_f32_16x16x32_bf16 v[40:43], v[52:55], v[184:187], v[40:43]\n"
    "ds_read_b64_tr_b16 v[66:67], v9 offset:37376\n"
    "buffer_load_dwordx4 v1, s[56:59], s11 offen offset:1024 sc1 lds\n"
    "v_mul_f32 v92, v20, v113\n"
    "v_mfma_f32_16x16x32_bf16 v[44:47], v[52:55], v[188:191], v[44:47]\n"
    "ds_read_b64_tr_b16 v[68:69], v9 offset:45056\n"
    "buffer_load_dwordx4 v1, s[56:59], s11 offen offset:2048 sc1 lds\n"
    "v_mul_f32 v93, v21, v113\n"
    "v_mul_f32 v94, v22, v113\n"
    "v_mfma_f32_16x16x32_bf16 v[36:39], v[48:51], v[188:191], v[36:39]\n"
    "s_waitcnt lgkmcnt(3)\n"
    "ds_read_b64_tr_b16 v[70:71], v9 offset:45568\n"
    "v_mul_f32 v95, v23, v113\n"
    "v_mfma_f32_16x16x32_bf16 v[32:35], v[56:59], v[192:195], v[32:35]\n"
    "ds_read_b64_tr_b16 v[48:49], v9 offset:49152\n"
    "buffer_load_dwordx4 v1, s[56:59], s11 offen offset:3072 sc1 lds\n"
    "v_exp_f32 v92, v92\n"
    "v_mfma_f32_16x16x32_bf16 v[40:43], v[60:63], v[192:195], v[40:43]\n"
    "ds_read_b64_tr_b16 v[50:51], v9 offset:49664\n"
    "v_mul_f32 v28, v20, v28\n"
    "v_mfma_f32_16x16x32_bf16 v[44:47], v[60:63], v[196:199], v[44:47]\n"
    "s_add_u32 m0, s17, 0x10000\n"
    "ds_read_b64_tr_b16 v[52:53], v9 offset:57344\n"
    "s_add_u32 s11, s90, 0x4000\n"
    "buffer_load_dwordx4 v1, s[56:59], s11 offen sc1 lds\n"
    "v_exp_f32 v93, v93\n"
    "v_mfma_f32_16x16x32_bf16 v[36:39], v[56:59], v[196:199], v[36:39]\n"
    "s_waitcnt lgkmcnt(3)\n"
    "ds_read_b64_tr_b16 v[54:55], v9 offset:57856\n"
    "v_mul_f32 v29, v21, v29\n"
    "v_mfma_f32_16x16x32_bf16 v[32:35], v[64:67], v[200:203], v[32:35]\n"
    "ds_read_b64_tr_b16 v[56:57], v9 offset:53248\n"
    "buffer_load_dwordx4 v1, s[56:59], s11 offen offset:1024 sc1 lds\n"
    "v_exp_f32 v94, v94\n"
    "v_mfma_f32_16x16x32_bf16 v[40:43], v[68:71], v[200:203], v[40:43]\n"
    "ds_read_b64_tr_b16 v[58:59], v9 offset:53760\n"
    "buffer_load_dwordx4 v1, s[56:59], s11 offen offset:2048 sc1 lds\n"
    "v_mfma_f32_16x16x32_bf16 v[44:47], v[68:71], v[204:207], v[44:47]\n"
    "ds_read_b64_tr_b16 v[60:61], v9 offset:61440\n"
    "v_mul_f32 v30, v22, v30\n"
    "v_exp_f32 v95, v95\n"
    "v_mfma_f32_16x16x32_bf16 v[36:39], v[64:67], v[204:207], v[36:39]\n"
    "s_waitcnt lgkmcnt(3)\n"
    "ds_read_b64_tr_b16 v[62:63], v9 offset:61952\n"
    "buffer_load_dwordx4 v1, s[56:59], s11 offen offset:3072 sc1 lds\n"
    "v_mfma_f32_16x16x32_bf16 v[32:35], v[48:51], v[208:211], v[32:35]\n"
    "ds_read_b64_tr_b16 v[64:65], v9 offset:16384\n"
    "v_mul_f32 v31, v23, v31\n"
    "v_mfma_f32_16x16x32_bf16 v[40:43], v[52:55], v[208:211], v[40:43]\n"
    "s_add_u32 m0, s17, 0x14000\n"
    "ds_read_b64_tr_b16 v[66:67], v9 offset:16896\n"
    "s_add_u32 s11, s90, 0x8000\n"
    "buffer_load_dwordx4 v1, s[56:59], s11 offen sc1 lds\n"
    "v_fma_f32 v92, v92, v115, v115\n"
    "v_fma_f32 v93, v93, v115, v115\n"
    "v_mfma_f32_16x16x32_bf16 v[44:47], v[52:55], v[212:215], v[44:47]\n"
    "ds_read_b64_tr_b16 v[68:69], v9 offset:24576\n"
    "buffer_load_dwordx4 v1, s[56:59], s11 offen offset:1024 sc1 lds\n"
    "v_fma_f32 v94, v94, v115, v115\n"
    "v_mfma_f32_16x16x32_bf16 v[36:39], v[48:51], v[212:215], v[36:39]\n"
    "s_waitcnt lgkmcnt(3)\n"
    "ds_read_b64_tr_b16 v[70:71], v9 offset:25088\n"
    "v_fma_f32 v95, v95, v115, v115\n"
    "v_rcp_f32 v92, v92\n"
    "v_mfma_f32_16x16x32_bf16 v[32:35], v[56:59], v[216:219], v[32:35]\n"
    "ds_read_b64_tr_b16 v[48:49], v9 offset:20480\n"
    "buffer_load_dwordx4 v1, s[56:59], s11 offen offset:2048 sc1 lds\n"
    "v_mfma_f32_16x16x32_bf16 v[40:43], v[60:63], v[216:219], v[40:43]\n"
    "ds_read_b64_tr_b16 v[50:51], v9 offset:20992\n"
    "v_rcp_f32 v93, v93\n"
    "v_mfma_f32_16x16x32_bf16 v[44:47], v[60:63], v[220:223], v[44:47]\n"
    "ds_read_b64_tr_b16 v[52:53], v9 offset:28672\n"
    "buffer_load_dwordx4 v1, s[56:59], s11 offen offset:3072 sc1 lds\n"
    "v_rcp_f32 v94, v94\n"
    "v_mfma_f32_16x16x32_bf16 v[36:39], v[56:59], v[220:223], v[36:39]\n"
    "s_waitcnt lgkmcnt(3)\n"
    "ds_read_b64_tr_b16 v[54:55], v9 offset:29184\n"
    "v_mfma_f32_16x16x32_bf16 v[32:35], v[64:67], v[224:227], v[32:35]\n"
    "v_rcp_f32 v95, v95\n"
    "v_mfma_f32_16x16x32_bf16 v[40:43], v[68:71], v[224:227], v[40:43]\n"
    "v_mul_f32 v28, v28, v92\n"
    "v_mfma_f32_16x16x32_bf16 v[44:47], v[68:71], v[228:231], v[44:47]\n"
    "v_mul_f32 v29, v29, v93\n"
    "v_mfma_f32_16x16x32_bf16 v[36:39], v[64:67], v[228:231], v[36:39]\n"
    "s_waitcnt lgkmcnt(0)\n"
    "v_mul_f32 v30, v30, v94\n"
    "v_mul_f32 v31, v31, v95\n"
    "v_mfma_f32_16x16x32_bf16 v[32:35], v[48:51], v[232:235], v[32:35]\n"
    "v_cvt_pk_bf16_f32 v244, v28, v29\n"
    "v_mfma_f32_16x16x32_bf16 v[40:43], v[52:55], v[232:235], v[40:43]\n"
    "v_cvt_pk_bf16_f32 v245, v30, v31\n"
    "v_mfma_f32_16x16x32_bf16 v[44:47], v[52:55], v[236:239], v[44:47]\n"
    "v_mfma_f32_16x16x32_bf16 v[36:39], v[48:51], v[236:239], v[36:39]\n"
    "s_branch Lattn_h0\n"
    "Lidle_h0:\n"
    "s_waitcnt vmcnt(0) lgkmcnt(0)\n"
    "s_cmp_lg_u32 s24, 255\n"
    "s_cbranch_scc1 Lend\n"
    "s_cmp_lg_u32 s25, 0\n"
    "s_cbranch_scc1 Lend\n"
    "v_add_u32 v104, 256, v79\n"
    "buffer_load_dwordx2 v[250:251], v104, s[56:59], 0 offen sc1\n"
    "s_waitcnt vmcnt(0)\n"
    "v_cmp_ne_u64 vcc, s[60:61], v[250:251]\n"
    "s_nop 0\n"
    "s_cbranch_vccz Lok_1\n"
    "s_mov_b32 s10, 0x8000\n"
    "Lspin_2:\n"
    "buffer_load_dwordx2 v[250:251], v104, s[56:59], 0 offen sc1\n"
    "s_sub_u32 s10, s10, 1\n"
    "s_waitcnt vmcnt(0)\n"
    "s_cbranch_scc1 Lok_1\n"
    "v_cmp_ne_u64 vcc, s[60:61], v[250:251]\n"
    "s_nop 0\n"
    "s_cbranch_vccnz Lspin_2\n"
    "Lok_1:\n"
    "v_add_u32 v104, 768, v79\n"
    "buffer_load_dwordx2 v[250:251], v104, s[56:59], 0 offen sc1\n"
    "s_waitcnt vmcnt(0)\n"
    "v_cmp_ne_u64 vcc, s[60:61], v[250:251]\n"
    "s_nop 0\n"
    "s_cbranch_vccz Lok_3\n"
    "s_mov_b32 s10, 0x8000\n"
    "Lspin_4:\n"
    "buffer_load_dwordx2 v[250:251], v104, s[56:59], 0 offen sc1\n"
    "s_sub_u32 s10, s10, 1\n"
    "s_waitcnt vmcnt(0)\n"
    "s_cbranch_scc1 Lok_3\n"
    "v_cmp_ne_u64 vcc, s[60:61], v[250:251]\n"
    "s_nop 0\n"
    "s_cbranch_vccnz Lspin_4\n"
    "Lok_3:\n"
    "v_add_u32 v104, 1280, v79\n"
    "buffer_load_dwordx2 v[250:251], v104, s[56:59], 0 offen sc1\n"
    "s_waitcnt vmcnt(0)\n"
    "v_cmp_ne_u64 vcc, s[60:61], v[250:251]\n"
    "s_nop 0\n"
    "s_cbranch_vccz Lok_5\n"
    "s_mov_b32 s10, 0x8000\n"
    "Lspin_6:\n"
    "buffer_load_dwordx2 v[250:251], v104, s[56:59], 0 offen sc1\n"
    "s_sub_u32 s10, s10, 1\n"
    "s_waitcnt vmcnt(0)\n"
    "s_cbranch_scc1 Lok_5\n"
    "v_cmp_ne_u64 vcc, s[60:61], v[250:251]\n"
    "s_nop 0\n"
    "s_cbranch_vccnz Lspin_6\n"
    "Lok_5:\n"
    "v_add_u32 v104, 1792, v79\n"
    "buffer_load_dwordx2 v[250:251], v104, s[56:59], 0 offen sc1\n"
    "s_waitcnt vmcnt(0)\n"
    "v_cmp_ne_u64 vcc, s[60:61], v[250:251]\n"
    "s_nop 0\n"
    "s_cbranch_vccz Lok_7\n"
    "s_mov_b32 s10, 0x8000\n"
    "Lspin_8:\n"
    "buffer_load_dwordx2 v[250:251], v104, s[56:59], 0 offen sc1\n"
    "s_sub_u32 s10, s10, 1\n"
    "s_waitcnt vmcnt(0)\n"
    "s_cbranch_scc1 Lok_7\n"
    "v_cmp_ne_u64 vcc, s[60:61], v[250:251]\n"
    "s_nop 0\n"
    "s_cbranch_vccnz Lspin_8\n"
    "Lok_7:\n"
    "v_add_u32 v104, 2304, v79\n"
    "buffer_load_dwordx2 v[250:251], v104, s[56:59], 0 offen sc1\n"
    "s_waitcnt vmcnt(0)\n"
    "v_cmp_ne_u64 vcc, s[60:61], v[250:251]\n"
    "s_nop 0\n"
    "s_cbranch_vccz Lok_9\n"
    "s_mov_b32 s10, 0x8000\n"
    "Lspin_10:\n"
    "buffer_load_dwordx2 v[250:251], v104, s[56:59], 0 offen sc1\n"
    "s_sub_u32 s10, s10, 1\n"
    "s_waitcnt vmcnt(0)\n"
    "s_cbranch_scc1 Lok_9\n"
    "v_cmp_ne_u64 vcc, s[60:61], v[250:251]\n"
    "s_nop 0\n"
    "s_cbranch_vccnz Lspin_10\n"
    "Lok_9:\n"
    "v_add_u32 v104, 2816, v79\n"
    "buffer_load_dwordx2 v[250:251], v104, s[56:59], 0 offen sc1\n"
    "s_waitcnt vmcnt(0)\n"
    "v_cmp_ne_u64 vcc, s[60:61], v[250:251]\n"
    "s_nop 0\n"
    "s_cbranch_vccz Lok_11\n"
    "s_mov_b32 s10, 0x8000\n"
    "Lspin_12:\n"
    "buffer_load_dwordx2 v[250:251], v104, s[56:59], 0 offen sc1\n"
    "s_sub_u32 s10, s10, 1\n"
    "s_waitcnt vmcnt(0)\n"
    "s_cbranch_scc1 Lok_11\n"
    "v_cmp_ne_u64 vcc, s[60:61], v[250:251]\n"
    "s_nop 0\n"
    "s_cbranch_vccnz Lspin_12\n"
    "Lok_11:\n"
    "s_mov_b64 exec, 1\n"
    "buffer_store_dwordx2 v[252:253], off, s[56:59], 0 sc1\n"
    "s_mov_b64 exec, -1\n"
    "s_branch Lend\n"
    "LpathY_h0:\n"
    "Lst0_2:\n"
    "s_cbranch_execz Lr0_2\n"
    "ds_read_b128 v[128:131], v94 offset:0\n"
    "ds_read_b128 v[136:139], v95 offset:8192\n"
    "ds_read_b128 v[144:147], v94 offset:16384\n"
    "ds_read_b128 v[152:155], v95 offset:24576\n"
    "ds_read_b128 v[160:163], v94 offset:32768\n"
    "Lst0_3:\n"
    "s_cbranch_execz Lr0_3\n"
    "ds_read_b128 v[168:171], v95 offset:40960\n"
    "ds_read_b128 v[176:179], v94 offset:49152\n"
    "ds_read_b128 v[184:187], v95 offset:57344\n"
    "v_add_u32 v94, 0x10000, v94\n"
    "ds_read_b128 v[192:195], v94 offset:0\n"
    "v_add_u32 v95, 0x10000, v95\n"
    "ds_read_b128 v[200:203], v95 offset:8192\n"
    "ds_read_b128 v[208:211], v94 offset:16384\n"
    "ds_read_b128 v[216:219], v95 offset:24576\n"
    "ds_read_b128 v[224:227], v94 offset:32768\n"
    "ds_read_b128 v[232:235], v95 offset:40960\n"
    "v_dot2_f32_bf16 v21, v188, v188, 0\n"
    "s_nop 2\n"
    "v_dot2c_f32_bf16 v21, v189, v189\n"
    "v_dot2c_f32_bf16 v21, v190, v190\n"
    "v_dot2c_f32_bf16 v21, v191, v191\n"
    "v_dot2c_f32_bf16 v21, v196, v196\n"
    "v_dot2c_f32_bf16 v21, v197, v197\n"
    "v_dot2c_f32_bf16 v21, v198, v198\n"
    "v_dot2c_f32_bf16 v21, v199, v199\n"
    "v_dot2c_f32_bf16 v21, v204, v204\n"
    "v_dot2c_f32_bf16 v21, v205, v205\n"
    "v_dot2c_f32_bf16 v21, v206, v206\n"
    "v_dot2c_f32_bf16 v21, v207, v207\n"
    "v_dot2c_f32_bf16 v21, v212, v212\n"
    "v_dot2c_f32_bf16 v21, v213, v213\n"
    "v_dot2c_f32_bf16 v21, v214, v214\n"
    "v_dot2c_f32_bf16 v21, v215, v215\n"
    "v_dot2c_f32_bf16 v21, v220, v220\n"
    "v_dot2c_f32_bf16 v21, v221, v221\n"
    "v_dot2c_f32_bf16 v21, v222, v222\n"
    "v_dot2c_f32_bf16 v21, v223, v223\n"
    "v_dot2c_f32_bf16 v21, v228, v228\n"
    "v_dot2c_f32_bf16 v21, v229, v229\n"
    "v_dot2c_f32_bf16 v21, v230, v230\n"
    "v_dot2c_f32_bf16 v21, v231, v231\n"
    "v_dot2c_f32_bf16 v21, v236, v236\n"
    "v_dot2c_f32_bf16 v21, v237, v237\n"
    "v_dot2c_f32_bf16 v21, v238, v238\n"
    "v_dot2c_f32_bf16 v21, v239, v239\n"
    "v_mov_b32 v22, 0x3727c5ac\n"
    "s_nop 1\n"
    "v_add_f32_dpp v21, v21, v21 quad_perm:[1,0,3,2] row_mask:0xf bank_mask:0xf\n"
    "s_nop 1\n"
    "v_add_f32_dpp v21, v21, v21 quad_perm:[2,3,0,1] row_mask:0xf bank_mask:0xf\n"
    "s_nop 1\n"
    "v_add_f32_dpp v21, v21, v21 row_half_mirror row_mask:0xf bank_mask:0xf\n"
    "v_fmamk_f32 v14, v21, 0x3b124925, v22\n"
    "s_waitcnt vmcnt(0)\n"
    "s_waitcnt lgkmcnt(14)\n"
    "s_barrier\n"
    "s_setprio 3\n"
    "ds_read_b64_tr_b16 v[48:49], v6 offset:0\n"
    "ds_read_b64_tr_b16 v[50:51], v6 offset:512\n"
    "ds_read_b128 v[132:135], v4 offset:0\n"
    "ds_read_b64_tr_b16 v[52:53], v7 offset:0\n"
    "ds_read_b64_tr_b16 v[54:55], v7 offset:512\n"
    "ds_read_b64_tr_b16 v[56:57], v6 offset:4096\n"
    "ds_read_b64_tr_b16 v[58:59], v6 offset:4608\n"
    "ds_read_b128 v[140:143], v12 offset:8192\n"
    "ds_read_b64_tr_b16 v[60:61], v7 offset:4096\n"
    "ds_read_b64_tr_b16 v[62:63], v7 offset:4608\n"
    "s_waitcnt lgkmcnt(8)\n"
    "v_mfma_f32_16x16x32_bf16 v[16:19], v[48:51], v[128:131], 0\n"
    "s_waitcnt lgkmcnt(5)\n"
    "ds_read_b128 v[148:151], v4 offset:16384\n"
    "ds_read_b64_tr_b16 v[64:65], v6 offset:8192\n"
    "v_mfma_f32_16x16x32_bf16 v[24:27], v[52:55], v[128:131], 0\n"
    "ds_read_b64_tr_b16 v[66:67], v6 offset:8704\n"
    "v_mfma_f32_16x16x32_bf16 v[28:31], v[52:55], v[132:135], 0\n"
    "ds_read_b128 v[156:159], v12 offset:24576\n"
    "ds_read_b64_tr_b16 v[68:69], v7 offset:8192\n"
    "v_mfma_f32_16x16x32_bf16 v[20:23], v[48:51], v[132:135], 0\n"
    "s_waitcnt lgkmcnt(8)\n"
    "ds_read_b64_tr_b16 v[70:71], v7 offset:8704\n"
    "v_mfma_f32_16x16x32_bf16 v[16:19], v[56:59], v[136:139], v[16:19]\n"
    "s_waitcnt lgkmcnt(6)\n"
    "ds_read_b128 v[164:167], v4 offset:32768\n"
    "ds_read_b64_tr_b16 v[48:49], v6 offset:12288\n"
    "buffer_load_dwordx2 v[252:253], off, s[56:59], 0 sc1\n"
    "v_mfma_f32_16x16x32_bf16 v[24:27], v[60:63], v[136:139], v[24:27]\n"
    "ds_read_b64_tr_b16 v[50:51], v6 offset:12800\n"
    "v_mfma_f32_16x16x32_bf16 v[28:31], v[60:63], v[140:143], v[28:31]\n"
    "s_setprio 1\n"
    "ds_read_b128 v[172:175], v12 offset:40960\n"
    "ds_read_b64_tr_b16 v[52:53], v7 offset:12288\n"
    "v_mfma_f32_16x16x32_bf16 v[20:23], v[56:59], v[140:143], v[20:23]\n"
    "s_setprio 0\n"
    "ds_read_b64_tr_b16 v[54:55], v7 offset:12800\n"
    "s_add_u32 m0, s63, 0xc000\n"
    "s_add_u32 s18, s71, 0x7e000\n"
    "s_barrier\n"
    "s_setprio 3\n"
    "s_waitcnt lgkmcnt(9)\n"
    "v_mfma_f32_16x16x32_bf16 v[32:35], v[64:67], v[128:131], 0\n"
    "s_waitcnt lgkmcnt(6)\n"
    "ds_read_b128 v[180:183], v4 offset:49152\n"
    "ds_read_b64_tr_b16 v[56:57], v6 offset:16384\n"
    "buffer_load_dwordx4 v2, s[32:35], s18 offen lds\n"
    "s_add_u32 m0, s63, 0xd000\n"
    "v_mfma_f32_16x16x32_bf16 v[40:43], v[68:71], v[128:131], 0\n"
    "s_add_u32 s18, s71, 0x93000\n"
    "ds_read_b64_tr_b16 v[58:59], v6 offset:16896\n"
    "v_mfma_f32_16x16x32_bf16 v[44:47], v[68:71], v[132:135], 0\n"
    "ds_read_b64_tr_b16 v[60:61], v7 offset:16384\n"
    "buffer_load_dwordx4 v2, s[32:35], s18 offen lds\n"
    "s_add_u32 m0, s63, 0x0\n"
    "s_add_u32 s18, s72, 0x7e000\n"
    "v_mfma_f32_16x16x32_bf16 v[36:39], v[64:67], v[132:135], 0\n"
    "s_waitcnt lgkmcnt(7)\n"
    "ds_read_b64_tr_b16 v[62:63], v7 offset:16896\n"
    "ds_write_b32 v73, v13 offset:57344\n"
    "v_mfma_f32_16x16x32_bf16 v[32:35], v[48:51], v[136:139], v[32:35]\n"
    "s_waitcnt lgkmcnt(6)\n"
    "ds_read_b128 v[188:191], v12 offset:57344\n"
    "ds_read_b64_tr_b16 v[64:65], v6 offset:20480\n"
    "buffer_load_dwordx4 v2, s[32:35], s18 offen lds\n"
    "s_add_u32 m0, s63, 0x1000\n"
    "ds_write_b32 v73, v14 offset:57376\n"
    "v_mfma_f32_16x16x32_bf16 v[40:43], v[52:55], v[136:139], v[40:43]\n"
    "s_add_u32 s18, s72, 0x93000\n"
    "ds_read_b64_tr_b16 v[66:67], v6 offset:20992\n"
    "v_mfma_f32_16x16x32_bf16 v[44:47], v[52:55], v[140:143], v[44:47]\n"
    "s_setprio 1\n"
    "ds_read_b64_tr_b16 v[68:69], v7 offset:20480\n"
    "buffer_load_dwordx4 v2, s[32:35], s18 offen lds\n"
    "s_add_u32 m0, s63, 0x2000\n"
    "s_add_u32 s18, s71, 0xa8000\n"
    "v_mfma_f32_16x16x32_bf16 v[36:39], v[48:51], v[140:143], v[36:39]\n"
    "s_setprio 0\n"
    "ds_read_b64_tr_b16 v[70:71], v7 offset:20992\n"
    "s_barrier\n"
    "s_setprio 3\n"
    "s_waitcnt lgkmcnt(9)\n"
    "v_mfma_f32_16x16x32_bf16 v[16:19], v[56:59], v[144:147], v[16:19]\n"
    "s_waitcnt lgkmcnt(7)\n"
    "v_add_u32 v4, 0x10000, v4\n"
    "ds_read_b128 v[196:199], v4 offset:0\n"
    "ds_read_b64_tr_b16 v[48:49], v6 offset:24576\n"
    "buffer_load_dwordx4 v2, s[32:35], s18 offen lds\n"
    "s_add_u32 m0, s63, 0x3000\n"
    "v_mfma_f32_16x16x32_bf16 v[24:27], v[60:63], v[144:147], v[24:27]\n"
    "s_add_u32 s18, s71, 0xbd000\n"
    "ds_read_b64_tr_b16 v[50:51], v6 offset:25088\n"
    "v_mfma_f32_16x16x32_bf16 v[28:31], v[60:63], v[148:151], v[28:31]\n"
    "v_add_u32 v12, 0x10000, v12\n"
    "ds_read_b128 v[204:207], v12 offset:8192\n"
    "ds_read_b64_tr_b16 v[52:53], v7 offset:24576\n"
    "buffer_load_dwordx4 v2, s[32:35], s18 offen lds\n"
    "s_add_u32 m0, s63, 0x10000\n"
    "s_add_u32 s18, s72, 0xa8000\n"
    "v_mfma_f32_16x16x32_bf16 v[20:23], v[56:59], v[148:151], v[20:23]\n"
    "s_waitcnt lgkmcnt(7)\n"
    "ds_read_b64_tr_b16 v[54:55], v7 offset:25088\n"
    "v_mfma_f32_16x16x32_bf16 v[16:19], v[64:67], v[152:155], v[16:19]\n"
    "s_waitcnt lgkmcnt(6)\n"
    "ds_read_b128 v[212:215], v4 offset:16384\n"
    "ds_read_b64_tr_b16 v[56:57], v6 offset:28672\n"
    "buffer_load_dwordx4 v2, s[32:35], s18 offen lds\n"
    "s_add_u32 m0, s63, 0x11000\n"
    "v_mfma_f32_16x16x32_bf16 v[24:27], v[68:71], v[152:155], v[24:27]\n"
    "s_add_u32 s18, s72, 0xbd000\n"
    "ds_read_b64_tr_b16 v[58:59], v6 offset:29184\n"
    "v_mfma_f32_16x16x32_bf16 v[28:31], v[68:71], v[156:159], v[28:31]\n"
    "ds_read_b128 v[220:223], v12 offset:24576\n"
    "ds_read_b64_tr_b16 v[60:61], v7 offset:28672\n"
    "buffer_load_dwordx4 v2, s[32:35], s18 offen lds\n"
    "s_add_u32 m0, s63, 0x12000\n"
    "s_add_u32 s18, s71, 0xd2000\n"
    "v_mfma_f32_16x16x32_bf16 v[20:23], v[64:67], v[156:159], v[20:23]\n"
    "s_waitcnt lgkmcnt(8)\n"
    "ds_read_b64_tr_b16 v[62:63], v7 offset:29184\n"
    "v_mfma_f32_16x16x32_bf16 v[32:35], v[48:51], v[144:147], v[32:35]\n"
    "s_waitcnt lgkmcnt(6)\n"
    "ds_read_b128 v[228:231], v4 offset:32768\n"
    "ds_read_b64_tr_b16 v[64:65], v6 offset:32768\n"
    "buffer_load_dwordx4 v2, s[32:35], s18 offen lds\n"
    "s_add_u32 m0, s63, 0x13000\n"
    "v_mfma_f32_16x16x32_bf16 v[40:43], v[52:55], v[144:147], v[40:43]\n"
    "s_add_u32 s18, s71, 0xe7000\n"
    "ds_read_b64_tr_b16 v[66:67], v6 offset:33280\n"
    "v_mfma_f32_16x16x32_bf16 v[44:47], v[52:55], v[148:151], v[44:47]\n"
    "ds_read_b64_tr_b16 v[68:69], v7 offset:32768\n"
    "buffer_load_dwordx4 v2, s[32:35], s18 offen lds\n"
    "s_add_u32 m0, s63, 0x14000\n"
    "s_add_u32 s18, s72, 0xd2000\n"
    "v_mfma_f32_16x16x32_bf16 v[36:39], v[48:51], v[148:151], v[36:39]\n"
    "s_waitcnt lgkmcnt(7)\n"
    "ds_read_b64_tr_b16 v[70:71], v7 offset:33280\n"
    "v_mfma_f32_16x16x32_bf16 v[32:35], v[56:59], v[152:155], v[32:35]\n"
    "s_waitcnt lgkmcnt(5)\n"
    "ds_read_b128 v[236:239], v12 offset:40960\n"
    "ds_read_b64_tr_b16 v[48:49], v6 offset:36864\n"
    "buffer_load_dwordx4 v2, s[32:35], s18 offen lds\n"
    "s_add_u32 m0, s63, 0x15000\n"
    "v_mfma_f32_16x16x32_bf16 v[40:43], v[60:63], v[152:155], v[40:43]\n"
    "s_add_u32 s18, s72, 0xe7000\n"
    "ds_read_b64_tr_b16 v[50:51], v6 offset:37376\n"
    "v_mfma_f32_16x16x32_bf16 v[44:47], v[60:63], v[156:159], v[44:47]\n"
    "ds_read_b64_tr_b16 v[52:53], v7 offset:36864\n"
    "buffer_load_dwordx4 v2, s[32:35], s18 offen lds\n"
    "s_add_u32 m0, s63, 0x16000\n"
    "s_add_u32 s18, s71, 0xfc000\n"
    "v_mfma_f32_16x16x32_bf16 v[36:39], v[56:59], v[156:159], v[36:39]\n"
    "s_waitcnt lgkmcnt(4)\n"
    "ds_read_b64_tr_b16 v[54:55], v7 offset:37376\n"
    "v_mfma_f32_16x16x32_bf16 v[16:19], v[64:67], v[160:163], v[16:19]\n"
    "ds_read_b64_tr_b16 v[56:57], v6 offset:40960\n"
    "buffer_load_dwordx4 v2, s[32:35], s18 offen lds\n"
    "s_add_u32 m0, s63, 0x17000\n"
    "v_mfma_f32_16x16x32_bf16 v[24:27], v[68:71], v[160:163], v[24:27]\n"
    "s_add_u32 s18, s71, 0x111000\n"
    "ds_read_b64_tr_b16 v[58:59], v6 offset:41472\n"
    "v_mfma_f32_16x16x32_bf16 v[28:31], v[68:71], v[164:167], v[28:31]\n"
    "ds_read_b64_tr_b16 v[60:61], v7 offset:40960\n"
    "buffer_load_dwordx4 v2, s[32:35], s18 offen lds\n"
    "s_add_u32 m0, s63, 0x8000\n"
    "s_add_u32 s18, s72, 0xfc000\n"
    "v_mfma_f32_16x16x32_bf16 v[20:23], v[64:67], v[164:167], v[20:23]\n"
    "s_setprio 1\n"
    "ds_read_b64_tr_b16 v[62:63], v7 offset:41472\n"
    "s_waitcnt lgkmcnt(4)\n"
    "v_mfma_f32_16x16x32_bf16 v[16:19], v[48:51], v[168:171], v[16:19]\n"
    "ds_read_b64_tr_b16 v[64:65], v6 offset:45056\n"
    "v_mfma_f32_16x16x32_bf16 v[24:27], v[52:55], v[168:171], v[24:27]\n"
    "ds_read_b64_tr_b16 v[66:67], v6 offset:45568\n"
    "v_mfma_f32_16x16x32_bf16 v[28:31], v[52:55], v[172:175], v[28:31]\n"
    "s_setprio 0\n"
    "ds_read_b64_tr_b16 v[68:69], v7 offset:45056\n"
    "v_mfma_f32_16x16x32_bf16 v[20:23], v[48:51], v[172:175], v[20:23]\n"
    "ds_read_b64_tr_b16 v[70:71], v7 offset:45568\n"
    "s_waitcnt vmcnt(10)\n"
    "s_barrier\n"
    "s_setprio 3\n"
    "s_waitcnt lgkmcnt(4)\n"
    "v_mfma_f32_16x16x32_bf16 v[32:35], v[56:59], v[160:163], v[32:35]\n"
    "ds_read_b64_tr_b16 v[48:49], v6 offset:49152\n"
    "buffer_load_dwordx4 v2, s[32:35], s18 offen lds\n"
    "s_add_u32 m0, s63, 0x9000\n"
    "ds_read_b32 v114, v74 offset:57344\n"
    "v_mfma_f32_16x16x32_bf16 v[40:43], v[60:63], v[160:163], v[40:43]\n"
    "s_add_u32 s18, s72, 0x111000\n"
    "ds_read_b64_tr_b16 v[50:51], v6 offset:49664\n"
    "v_mfma_f32_16x16x32_bf16 v[44:47], v[60:63], v[164:167], v[44:47]\n"
    "ds_read_b64_tr_b16 v[52:53], v7 offset:49152\n"
    "buffer_load_dwordx4 v2, s[32:35], s18 offen lds\n"
    "s_add_u32 m0, s63, 0x4000\n"
    "s_add_u32 s18, s80, 0x0\n"
    "ds_read_b32 v115, v75 offset:57344\n"
    "v_mfma_f32_16x16x32_bf16 v[36:39], v[56:59], v[164:167], v[36:39]\n"
    "s_waitcnt lgkmcnt(5)\n"
    "ds_read_b64_tr_b16 v[54:55], v7 offset:49664\n"
    "v_mfma_f32_16x16x32_bf16 v[32:35], v[64:67], v[168:171], v[32:35]\n"
    "ds_read_b64_tr_b16 v[56:57], v6 offset:53248\n"
    "buffer_load_dwordx4 v72, s[40:43], s18 offen lds\n"
    "v_mfma_f32_16x16x32_bf16 v[40:43], v[68:71], v[168:171], v[40:43]\n"
    "s_add_u32 m0, s63, 0x5000\n"
    "ds_read_b64_tr_b16 v[58:59], v6 offset:53760\n"
    "v_mfma_f32_16x16x32_bf16 v[44:47], v[68:71], v[172:175], v[44:47]\n"
    "ds_read_b64_tr_b16 v[60:61], v7 offset:53248\n"
    "buffer_load_dwordx4 v72, s[44:47], s18 offen lds\n"
    "s_add_u32 m0, s63, 0x6000\n"
    "s_add_u32 s18, s80, 0x38000\n"
    "v_mfma_f32_16x16x32_bf16 v[36:39], v[64:67], v[172:175], v[36:39]\n"
    "s_waitcnt lgkmcnt(3)\n"
    "ds_read_b64_tr_b16 v[62:63], v7 offset:53760\n"
    "v_mfma_f32_16x16x32_bf16 v[16:19], v[48:51], v[176:179], v[16:19]\n"
    "ds_read_b64_tr_b16 v[64:65], v6 offset:0\n"
    "buffer_load_dwordx4 v72, s[40:43], s18 offen lds\n"
    "v_rsq_f32 v116, v114\n"
    "v_mfma_f32_16x16x32_bf16 v[24:27], v[52:55], v[176:179], v[24:27]\n"
    "s_add_u32 m0, s63, 0x7000\n"
    "ds_read_b64_tr_b16 v[66:67], v6 offset:512\n"
    "v_mfma_f32_16x16x32_bf16 v[28:31], v[52:55], v[180:183], v[28:31]\n"
    "ds_read_b64_tr_b16 v[68:69], v7 offset:0\n"
    "buffer_load_dwordx4 v72, s[44:47], s18 offen lds\n"
    "s_add_u32 m0, s63, 0x18000\n"
    "s_add_u32 s18, s80, 0x70000\n"
    "v_rsq_f32 v117, v115\n"
    "v_mfma_f32_16x16x32_bf16 v[20:23], v[48:51], v[180:183], v[20:23]\n"
    "s_waitcnt lgkmcnt(3)\n"
    "ds_read_b64_tr_b16 v[70:71], v7 offset:512\n"
    "v_mfma_f32_16x16x32_bf16 v[16:19], v[56:59], v[184:187], v[16:19]\n"
    "ds_read_b64_tr_b16 v[48:49], v6 offset:4096\n"
    "buffer_load_dwordx4 v72, s[40:43], s18 offen lds\n"
    "s_add_u32 m0, s63, 0x19000\n"
    "v_mfma_f32_16x16x32_bf16 v[24:27], v[60:63], v[184:187], v[24:27]\n"
    "s_setprio 1\n"
    "ds_read_b64_tr_b16 v[50:51], v6 offset:4608\n"
    "v_mul_f32 v112, 0xbfb8aa3b, v116\n"
    "v_mfma_f32_16x16x32_bf16 v[28:31], v[60:63], v[188:191], v[28:31]\n"
    "s_setprio 0\n"
    "ds_read_b64_tr_b16 v[52:53], v7 offset:4096\n"
    "buffer_load_dwordx4 v72, s[44:47], s18 offen lds\n"
    "s_add_u32 m0, s63, 0x1a000\n"
    "v_mul_f32 v113, 0xbfb8aa3b, v117\n"
    "v_mfma_f32_16x16x32_bf16 v[20:23], v[56:59], v[188:191], v[20:23]\n"
    "s_add_u32 s18, s80, 0xa8000\n"
    "ds_read_b64_tr_b16 v[54:55], v7 offset:4608\n"
    "v_mul_f32 v116, s84, v116\n"
    "s_waitcnt vmcnt(16)\n"
    "s_barrier\n"
    "s_setprio 3\n"
    "s_waitcnt lgkmcnt(4)\n"
    "v_mfma_f32_16x16x32_bf16 v[32:35], v[64:67], v[176:179], v[32:35]\n"
    "ds_read_b64_tr_b16 v[56:57], v6 offset:8192\n"
    "buffer_load_dwordx4 v72, s[40:43], s18 offen lds\n"
    "v_mul_f32 v117, s84, v117\n"
    "v_mfma_f32_16x16x32_bf16 v[40:43], v[68:71], v[176:179], v[40:43]\n"
    "s_add_u32 m0, s63, 0x1b000\n"
    "ds_read_b64_tr_b16 v[58:59], v6 offset:8704\n"
    "v_mfma_f32_16x16x32_bf16 v[44:47], v[68:71], v[180:183], v[44:47]\n"
    "ds_read_b64_tr_b16 v[60:61], v7 offset:8192\n"
    "buffer_load_dwordx4 v72, s[44:47], s18 offen lds\n"
    "s_add_u32 m0, s63, 0xa000\n"
    "s_add_u32 s18, s80, 0xe0000\n"
    "v_mfma_f32_16x16x32_bf16 v[36:39], v[64:67], v[180:183], v[36:39]\n"
    "s_waitcnt lgkmcnt(3)\n"
    "ds_read_b64_tr_b16 v[62:63], v7 offset:8704\n"
    "v_mfma_f32_16x16x32_bf16 v[32:35], v[48:51], v[184:187], v[32:35]\n"
    "s_and_b32 s65, s27, 1\n"
    "ds_read_b64_tr_b16 v[64:65], v6 offset:12288\n"
    "buffer_load_dwordx4 v72, s[40:43], s18 offen lds\n"
    "s_add_u32 m0, s63, 0xb000\n"
    "v_mfma_f32_16x16x32_bf16 v[40:43], v[52:55], v[184:187], v[40:43]\n"
    "s_lshl_b32 s66, s26, 1\n"
    "ds_read_b64_tr_b16 v[66:67], v6 offset:12800\n"
    "v_mfma_f32_16x16x32_bf16 v[44:47], v[52:55], v[188:191], v[44:47]\n"
    "s_setprio 1\n"
    "ds_read_b64_tr_b16 v[68:69], v7 offset:12288\n"
    "buffer_load_dwordx4 v72, s[44:47], s18 offen lds\n"
    "s_add_u32 m0, s63, 0x0\n"
    "s_add_u32 s18, s80, 0x118000\n"
    "s_add_u32 s65, s65, s66\n"
    "v_mfma_f32_16x16x32_bf16 v[36:39], v[48:51], v[188:191], v[36:39]\n"
    "s_setprio 0\n"
    "ds_read_b64_tr_b16 v[70:71], v7 offset:12800\n"
    "s_lshr_b32 s91, s27, 1\n"
    "s_waitcnt vmcnt(14)\n"
    "s_barrier\n"
    "s_setprio 3\n"
    "s_waitcnt lgkmcnt(4)\n"
    "v_mfma_f32_16x16x32_bf16 v[16:19], v[56:59], v[192:195], v[16:19]\n"
    "s_lshl_b32 s90, s65, 18\n"
    "v_add_u32 v6, 0x10000, v6\n"
    "ds_read_b64_tr_b16 v[48:49], v6 offset:0\n"
    "buffer_load_dwordx4 v72, s[40:43], s18 offen lds\n"
    "s_add_u32 m0, s63, 0x1000\n"
    "v_mfma_f32_16x16x32_bf16 v[24:27], v[60:63], v[192:195], v[24:27]\n"
    "s_add_u32 s90, s90, s17\n"
    "ds_read_b64_tr_b16 v[50:51], v6 offset:512\n"
    "v_mfma_f32_16x16x32_bf16 v[28:31], v[60:63], v[196:199], v[28:31]\n"
    "s_add_u32 s90, s90, 0x210000\n"
    "v_add_u32 v7, 0x10000, v7\n"
    "ds_read_b64_tr_b16 v[52:53], v7 offset:0\n"
    "buffer_load_dwordx4 v72, s[44:47], s18 offen lds\n"
    "s_add_u32 m0, s63, 0x1c000\n"
    "s_add_u32 s18, s80, 0x150000\n"
    "v_mfma_f32_16x16x32_bf16 v[20:23], v[56:59], v[196:199], v[20:23]\n"
    "s_waitcnt lgkmcnt(3)\n"
    "ds_read_b64_tr_b16 v[54:55], v7 offset:512\n"
    "v_mfma_f32_16x16x32_bf16 v[16:19], v[64:67], v[200:203], v[16:19]\n"
    "s_and_b32 s66, s25, 3\n"
    "ds_read_b64_tr_b16 v[56:57], v6 offset:4096\n"
    "buffer_load_dwordx4 v72, s[40:43], s18 offen lds\n"
    "s_add_u32 m0, s63, 0x1d000\n"
    "v_mfma_f32_16x16x32_bf16 v[24:27], v[68:71], v[200:203], v[24:27]\n"
    "s_lshl_b32 s67, s91, 2\n"
    "ds_read_b64_tr_b16 v[58:59], v6 offset:4608\n"
    "v_mfma_f32_16x16x32_bf16 v[28:31], v[68:71], v[204:207], v[28:31]\n"
    "s_add_u32 s67, s67, s66\n"
    "ds_read_b64_tr_b16 v[60:61], v7 offset:4096\n"
    "buffer_load_dwordx4 v72, s[44:47], s18 offen lds\n"
    "s_add_u32 m0, s63, 0xc000\n"
    "s_add_u32 s18, s81, 0x0\n"
    "v_mfma_f32_16x16x32_bf16 v[20:23], v[64:67], v[204:207], v[20:23]\n"
    "s_lshl_b32 s92, s65, 6\n"
    "ds_read_b64_tr_b16 v[62:63], v7 offset:4608\n"
    "s_waitcnt lgkmcnt(4)\n"
    "v_mfma_f32_16x16x32_bf16 v[32:35], v[48:51], v[192:195], v[32:35]\n"
    "s_add_u32 s92, s92, s67\n"
    "ds_read_b64_tr_b16 v[64:65], v6 offset:8192\n"
    "buffer_load_dwordx4 v3, s[40:43], s18 offen lds\n"
    "s_add_u32 m0, s63, 0xe000\n"
    "v_mfma_f32_16x16x32_bf16 v[40:43], v[52:55], v[192:195], v[40:43]\n"
    "s_lshl_b32 s92, s92, 11\n"
    "ds_read_b64_tr_b16 v[66:67], v6 offset:8704\n"
    "buffer_load_dwordx4 v3, s[44:47], s18 offen lds\n"
    "s_add_u32 m0, s63, 0xd000\n"
    "v_readfirstlane_b32 s60, v252\n"
    "v_mfma_f32_16x16x32_bf16 v[44:47], v[52:55], v[196:199], v[44:47]\n"
    "s_add_u32 s18, s81, 0x1c000\n"
    "ds_read_b64_tr_b16 v[68:69], v7 offset:8192\n"
    "v_readfirstlane_b32 s61, v253\n"
    "v_mfma_f32_16x16x32_bf16 v[36:39], v[48:51], v[196:199], v[36:39]\n"
    "ds_read_b64_tr_b16 v[70:71], v7 offset:8704\n"
    "buffer_load_dwordx4 v3, s[40:43], s18 offen lds\n"
    "s_add_u32 m0, s63, 0xf000\n"
    "s_add_u32 s60, s60, 0x7f4a7c15\n"
    "s_addc_u32 s61, s61, 0x9e3779b9\n"
    "s_add_u32 s92, s92, 0x10000\n"
    "s_waitcnt lgkmcnt(4)\n"
    "v_mfma_f32_16x16x32_bf16 v[32:35], v[56:59], v[200:203], v[32:35]\n"
    "s_lshl_b32 s93, s67, 15\n"
    "ds_read_b64_tr_b16 v[48:49], v6 offset:12288\n"
    "buffer_load_dwordx4 v3, s[44:47], s18 offen lds\n"
    "s_add_u32 m0, s63, 0x10000\n"
    "s_add_u32 s18, s81, 0x38000\n"
    "v_mfma_f32_16x16x32_bf16 v[40:43], v[60:63], v[200:203], v[40:43]\n"
    "s_lshl_b32 s7, s65, 8\n"
    "ds_read_b64_tr_b16 v[50:51], v6 offset:12800\n"
    "v_mov_b32 v252, s60\n"
    "v_mfma_f32_16x16x32_bf16 v[44:47], v[60:63], v[204:207], v[44:47]\n"
    "s_add_u32 s7, s7, s93\n"
    "ds_read_b64_tr_b16 v[52:53], v7 offset:12288\n"
    "v_mov_b32 v253, s61\n"
    "v_mfma_f32_16x16x32_bf16 v[36:39], v[56:59], v[204:207], v[36:39]\n"
    "s_add_u32 s7, s7, 0xd13000\n"
    "ds_read_b64_tr_b16 v[54:55], v7 offset:12800\n"
    "s_waitcnt lgkmcnt(4)\n"
    "v_mfma_f32_16x16x32_bf16 v[16:19], v[64:67], v[208:211], v[16:19]\n"
    "s_mul_i32 s66, s65, 768\n"
    "ds_read_b64_tr_b16 v[56:57], v6 offset:16384\n"
    "v_mfma_f32_16x16x32_bf16 v[24:27], v[68:71], v[208:211], v[24:27]\n"
    "ds_read_b64_tr_b16 v[58:59], v6 offset:16896\n"
    "v_mfma_f32_16x16x32_bf16 v[28:31], v[68:71], v[212:215], v[28:31]\n"
    "s_add_u32 s93, s93, s66\n"
    "ds_read_b64_tr_b16 v[60:61], v7 offset:16384\n"
    "v_mfma_f32_16x16x32_bf16 v[20:23], v[64:67], v[212:215], v[20:23]\n"
    "s_setprio 1\n"
    "ds_read_b64_tr_b16 v[62:63], v7 offset:16896\n"
    "s_add_u32 s93, s93, 0xd10000\n"
    "s_waitcnt lgkmcnt(4)\n"
    "v_mfma_f32_16x16x32_bf16 v[16:19], v[48:51], v[216:219], v[16:19]\n"
    "s_lshl_b32 s94, s67, 4\n"
    "ds_read_b64_tr_b16 v[64:65], v6 offset:20480\n"
    "v_mfma_f32_16x16x32_bf16 v[24:27], v[52:55], v[216:219], v[24:27]\n"
    "s_add_u32 s94, s94, s65\n"
    "ds_read_b64_tr_b16 v[66:67], v6 offset:20992\n"
    "v_mfma_f32_16x16x32_bf16 v[28:31], v[52:55], v[220:223], v[28:31]\n"
    "s_setprio 0\n"
    "ds_read_b64_tr_b16 v[68:69], v7 offset:20480\n"
    "s_lshl_b32 s94, s94, 3\n"
    "v_mfma_f32_16x16x32_bf16 v[20:23], v[48:51], v[220:223], v[20:23]\n"
    "s_add_u32 s94, s94, 8192\n"
    "ds_read_b64_tr_b16 v[70:71], v7 offset:20992\n"
    "s_waitcnt vmcnt(18)\n"
    "s_barrier\n"
    "s_setprio 3\n"
    "s_waitcnt lgkmcnt(4)\n"
    "v_mfma_f32_16x16x32_bf16 v[32:35], v[56:59], v[208:211], v[32:35]\n"
    "s_lshl_b32 s65, s26, 7\n"
    "ds_read_b64_tr_b16 v[48:49], v6 offset:24576\n"
    "buffer_load_dwordx4 v3, s[40:43], s18 offen lds\n"
    "v_mfma_f32_16x16x32_bf16 v[40:43], v[60:63], v[208:211], v[40:43]\n"
    "s_add_u32 m0, s63, 0x12000\n"
    "ds_read_b64_tr_b16 v[50:51], v6 offset:25088\n"
    "buffer_load_dwordx4 v3, s[44:47], s18 offen lds\n"
    "s_add_u32 m0, s63, 0x11000\n"
    "s_add_u32 s18, s81, 0x54000\n"
    "v_mfma_f32_16x16x32_bf16 v[44:47], v[60:63], v[212:215], v[44:47]\n"
    "s_lshl_b32 s66, s91, 3\n"
    "ds_read_b64_tr_b16 v[52:53], v7 offset:24576\n"
    "v_mfma_f32_16x16x32_bf16 v[36:39], v[56:59], v[212:215], v[36:39]\n"
    "s_add_u32 s66, s66, s65\n"
    "ds_read_b64_tr_b16 v[54:55], v7 offset:25088\n"
    "buffer_load_dwordx4 v3, s[40:43], s18 offen lds\n"
    "s_add_u32 m0, s63, 0x13000\n"
    "s_waitcnt lgkmcnt(4)\n"
    "v_mfma_f32_16x16x32_bf16 v[32:35], v[64:67], v[216:219], v[32:35]\n"
    "s_add_u32 s66, s66, 256\n"
    "ds_read_b64_tr_b16 v[56:57], v6 offset:28672\n"
    "buffer_load_dwordx4 v3, s[44:47], s18 offen lds\n"
    "s_add_u32 m0, s63, 0x14000\n"
    "s_add_u32 s18, s81, 0x70000\n"
    "v_mfma_f32_16x16x32_bf16 v[40:43], v[68:71], v[216:219], v[40:43]\n"
    "s_add_u32 s65, s65, 1280\n"
    "ds_read_b64_tr_b16 v[58:59], v6 offset:29184\n"
    "v_mfma_f32_16x16x32_bf16 v[44:47], v[68:71], v[220:223], v[44:47]\n"
    "ds_read_b64_tr_b16 v[60:61], v7 offset:28672\n"
    "v_and_b32 v250, 15, v0\n"
    "v_mfma_f32_16x16x32_bf16 v[36:39], v[64:67], v[220:223], v[36:39]\n"
    "s_waitcnt lgkmcnt(3)\n"
    "ds_read_b64_tr_b16 v[62:63], v7 offset:29184\n"
    "v_and_b32 v251, 16, v0\n"
    "v_mfma_f32_16x16x32_bf16 v[16:19], v[48:51], v[224:227], v[16:19]\n"
    "v_add_u32 v6, 0xffff8000, v6\n"
    "ds_read_b64_tr_b16 v[64:65], v6 offset:0\n"
    "v_mfma_f32_16x16x32_bf16 v[24:27], v[52:55], v[224:227], v[24:27]\n"
    "ds_read_b64_tr_b16 v[66:67], v6 offset:512\n"
    "v_lshlrev_b32 v251, 6, v251\n"
    "v_mfma_f32_16x16x32_bf16 v[28:31], v[52:55], v[228:231], v[28:31]\n"
    "v_add_u32 v7, 0xffff8000, v7\n"
    "ds_read_b64_tr_b16 v[68:69], v7 offset:0\n"
    "v_lshl_add_u32 v250, v250, 3, v251\n"
    "v_mfma_f32_16x16x32_bf16 v[20:23], v[48:51], v[228:231], v[20:23]\n"
    "s_waitcnt lgkmcnt(3)\n"
    "ds_read_b64_tr_b16 v[70:71], v7 offset:512\n"
    "v_add_u32 v250, s65, v250\n"
    "v_mfma_f32_16x16x32_bf16 v[16:19], v[56:59], v[232:235], v[16:19]\n"
    "ds_read_b64_tr_b16 v[48:49], v6 offset:4096\n"
    "v_mov_b32 v251, s66\n"
    "v_mfma_f32_16x16x32_bf16 v[24:27], v[60:63], v[232:235], v[24:27]\n"
    "s_setprio 1\n"
    "ds_read_b64_tr_b16 v[50:51], v6 offset:4608\n"
    "v_cmp_gt_u32 vcc, 32, v0\n"
    "v_mfma_f32_16x16x32_bf16 v[28:31], v[60:63], v[236:239], v[28:31]\n"
    "s_setprio 0\n"
    "ds_read_b64_tr_b16 v[52:53], v7 offset:4096\n"
    "v_cndmask_b32 v248, v251, v250, vcc\n"
    "v_mfma_f32_16x16x32_bf16 v[20:23], v[56:59], v[236:239], v[20:23]\n"
    "ds_read_b64_tr_b16 v[54:55], v7 offset:4608\n"
    "s_waitcnt vmcnt(16)\n"
    "s_barrier\n"
    "s_setprio 3\n"
    "s_waitcnt lgkmcnt(4)\n"
    "v_mfma_f32_16x16x32_bf16 v[32:35], v[64:67], v[224:227], v[32:35]\n"
    "ds_read_b64_tr_b16 v[56:57], v5 offset:16384\n"
    "v_mfma_f32_16x16x32_bf16 v[40:43], v[68:71], v[224:227], v[40:43]\n"
    "ds_read_b64_tr_b16 v[58:59], v5 offset:16640\n"
    "v_mfma_f32_16x16x32_bf16 v[44:47], v[68:71], v[228:231], v[44:47]\n"
    "ds_read_b64_tr_b16 v[60:61], v5 offset:20480\n"
    "v_mfma_f32_16x16x32_bf16 v[36:39], v[64:67], v[228:231], v[36:39]\n"
    "s_waitcnt lgkmcnt(3)\n"
    "ds_read_b64_tr_b16 v[62:63], v5 offset:20736\n"
    "v_mul_f32 v16, v16, v116\n"
    "v_mul_f32 v17, v17, v116\n"
    "v_mul_f32 v18, v18, v116\n"
    "v_mfma_f32_16x16x32_bf16 v[32:35], v[48:51], v[232:235], v[32:35]\n"
    "ds_read_b64_tr_b16 v[64:65], v5 offset:18432\n"
    "v_mul_f32 v19, v19, v116\n"
    "v_mul_f32 v24, v24, v116\n"
    "v_mfma_f32_16x16x32_bf16 v[40:43], v[52:55], v[232:235], v[40:43]\n"
    "ds_read_b64_tr_b16 v[66:67], v5 offset:18688\n"
    "v_mul_f32 v25, v25, v116\n"
    "v_mul_f32 v26, v26, v116\n"
    "v_mul_f32 v27, v27, v116\n"
    "v_mfma_f32_16x16x32_bf16 v[44:47], v[52:55], v[236:239], v[44:47]\n"
    "ds_read_b64_tr_b16 v[68:69], v5 offset:22528\n"
    "v_cvt_pk_bf16_f32 v80, v16, v17\n"
    "v_cvt_pk_bf16_f32 v81, v18, v19\n"
    "v_mfma_f32_16x16x32_bf16 v[36:39], v[48:51], v[236:239], v[36:39]\n"
    "s_waitcnt lgkmcnt(3)\n"
    "ds_read_b64_tr_b16 v[70:71], v5 offset:22784\n"
    "v_cvt_pk_bf16_f32 v82, v24, v25\n"
    "v_cvt_pk_bf16_f32 v83, v26, v27\n"
    "v_cndmask_b32 v82, v82, v118, s[86:87]\n"
    "v_mfma_f32_16x16x32_bf16 v[96:99], v[56:59], v[128:131], 0\n"
    "ds_read_b64_tr_b16 v[48:49], v5 offset:24576\n"
    "v_cndmask_b32 v83, v83, 0, s[86:87]\n"
    "v_mul_f32 v20, v20, v117\n"
    "v_mfma_f32_16x16x32_bf16 v[104:107], v[60:63], v[128:131], 0\n"
    "ds_read_b64_tr_b16 v[50:51], v5 offset:24832\n"
    "v_mul_f32 v21, v21, v117\n"
    "v_mul_f32 v22, v22, v117\n"
    "v_mul_f32 v23, v23, v117\n"
    "v_mfma_f32_16x16x32_bf16 v[108:111], v[60:63], v[132:135], 0\n"
    "ds_read_b64_tr_b16 v[52:53], v5 offset:28672\n"
    "v_mul_f32 v28, v28, v117\n"
    "v_mul_f32 v29, v29, v117\n"
    "v_mfma_f32_16x16x32_bf16 v[100:103], v[56:59], v[132:135], 0\n"
    "s_waitcnt lgkmcnt(3)\n"
    "ds_read_b64_tr_b16 v[54:55], v5 offset:28928\n"
    "v_mul_f32 v30, v30, v117\n"
    "v_mul_f32 v31, v31, v117\n"
    "v_cvt_pk_bf16_f32 v84, v20, v21\n"
    "v_mfma_f32_16x16x32_bf16 v[96:99], v[64:67], v[136:139], v[96:99]\n"
    "ds_read_b64_tr_b16 v[56:57], v5 offset:26624\n"
    "v_cvt_pk_bf16_f32 v85, v22, v23\n"
    "v_cvt_pk_bf16_f32 v86, v28, v29\n"
    "v_mfma_f32_16x16x32_bf16 v[104:107], v[68:71], v[136:139], v[104:107]\n"
    "ds_read_b64_tr_b16 v[58:59], v5 offset:26880\n"
    "v_cvt_pk_bf16_f32 v87, v30, v31\n"
    "v_cndmask_b32 v86, v86, v118, s[86:87]\n"
    "v_cndmask_b32 v87, v87, 0, s[86:87]\n"
    "v_mfma_f32_16x16x32_bf16 v[108:111], v[68:71], v[140:143], v[108:111]\n"
    "ds_read_b64_tr_b16 v[60:61], v5 offset:30720\n"
    "buffer_store_dwordx4 v[80:83], v10, s[56:59], s74 offen offset:0 sc1\n"
    "buffer_store_dwordx4 v[84:87], v10, s[56:59], s74 offen offset:128 sc1\n"
    "v_mfma_f32_16x16x32_bf16 v[100:103], v[64:67], v[140:143], v[100:103]\n"
    "s_waitcnt lgkmcnt(3)\n"
    "ds_read_b64_tr_b16 v[62:63], v5 offset:30976\n"
    "v_mul_f32 v32, v32, v116\n"
    "v_mul_f32 v33, v33, v116\n"
    "v_mfma_f32_16x16x32_bf16 v[96:99], v[48:51], v[144:147], v[96:99]\n"
    "v_add_u32 v5, 0x18000, v5\n"
    "ds_read_b64_tr_b16 v[64:65], v5 offset:0\n"
    "v_mul_f32 v34, v34, v116\n"
    "v_mul_f32 v35, v35, v116\n"
    "v_mfma_f32_16x16x32_bf16 v[104:107], v[52:55], v[144:147], v[104:107]\n"
    "ds_read_b64_tr_b16 v[66:67], v5 offset:256\n"
    "v_mul_f32 v40, v40, v116\n"
    "v_mfma_f32_16x16x32_bf16 v[108:111], v[52:55], v[148:151], v[108:111]\n"
    "ds_read_b64_tr_b16 v[68:69], v5 offset:4096\n"
    "v_mul_f32 v41, v41, v116\n"
    "v_mul_f32 v42, v42, v116\n"
    "v_mfma_f32_16x16x32_bf16 v[100:103], v[48:51], v[148:151], v[100:103]\n"
    "Lst0_4:\n"
    "s_cbranch_execz Lr0_4\n"
    "s_setprio 1\n"
    "ds_read_b64_tr_b16 v[70:71], v5 offset:4352\n"
    "v_mul_f32 v43, v43, v116\n"
    "v_cvt_pk_bf16_f32 v80, v32, v33\n"
    "s_waitcnt lgkmcnt(4)\n"
    "v_mfma_f32_16x16x32_bf16 v[96:99], v[56:59], v[152:155], v[96:99]\n"
    "ds_read_b64_tr_b16 v[48:49], v5 offset:2048\n"
    "v_cvt_pk_bf16_f32 v81, v34, v35\n"
    "v_mfma_f32_16x16x32_bf16 v[104:107], v[60:63], v[152:155], v[104:107]\n"
    "ds_read_b64_tr_b16 v[50:51], v5 offset:2304\n"
    "v_cvt_pk_bf16_f32 v82, v40, v41\n"
    "v_cvt_pk_bf16_f32 v83, v42, v43\n"
    "v_mfma_f32_16x16x32_bf16 v[108:111], v[60:63], v[156:159], v[108:111]\n"
    "s_setprio 0\n"
    "ds_read_b64_tr_b16 v[52:53], v5 offset:6144\n"
    "v_cndmask_b32 v82, v82, v118, s[86:87]\n"
    "v_cndmask_b32 v83, v83, 0, s[86:87]\n"
    "v_mfma_f32_16x16x32_bf16 v[100:103], v[56:59], v[156:159], v[100:103]\n"
    "ds_read_b64_tr_b16 v[54:55], v5 offset:6400\n"
    "v_mul_f32 v36, v36, v117\n"
    "s_waitcnt vmcnt(2)\n"
    "s_barrier\n"
    "s_setprio 3\n"
    "s_waitcnt lgkmcnt(4)\n"
    "v_mfma_f32_16x16x32_bf16 v[96:99], v[64:67], v[160:163], v[96:99]\n"
    "ds_read_b64_tr_b16 v[56:57], v5 offset:8192\n"
    "v_mul_f32 v37, v37, v117\n"
    "v_mul_f32 v38, v38, v117\n"
    "v_mfma_f32_16x16x32_bf16 v[104:107], v[68:71], v[160:163], v[104:107]\n"
    "ds_read_b64_tr_b16 v[58:59], v5 offset:8448\n"
    "v_mul_f32 v39, v39, v117\n"
    "v_mul_f32 v44, v44, v117\n"
    "v_mfma_f32_16x16x32_bf16 v[108:111], v[68:71], v[164:167], v[108:111]\n"
    "ds_read_b64_tr_b16 v[60:61], v5 offset:12288\n"
    "v_mul_f32 v45, v45, v117\n"
    "v_mfma_f32_16x16x32_bf16 v[100:103], v[64:67], v[164:167], v[100:103]\n"
    "s_waitcnt lgkmcnt(3)\n"
    "ds_read_b64_tr_b16 v[62:63], v5 offset:12544\n"
    "v_mul_f32 v46, v46, v117\n"
    "v_mul_f32 v47, v47, v117\n"
    "v_mfma_f32_16x16x32_bf16 v[96:99], v[48:51], v[168:171], v[96:99]\n"
    "ds_read_b64_tr_b16 v[64:65], v5 offset:10240\n"
    "v_cvt_pk_bf16_f32 v84, v36, v37\n"
    "v_cvt_pk_bf16_f32 v85, v38, v39\n"
    "v_mfma_f32_16x16x32_bf16 v[104:107], v[52:55], v[168:171], v[104:107]\n"
    "ds_read_b64_tr_b16 v[66:67], v5 offset:10496\n"
    "v_cvt_pk_bf16_f32 v86, v44, v45\n"
    "v_mfma_f32_16x16x32_bf16 v[108:111], v[52:55], v[172:175], v[108:111]\n"
    "ds_read_b64_tr_b16 v[68:69], v5 offset:14336\n"
    "v_cvt_pk_bf16_f32 v87, v46, v47\n"
    "v_cndmask_b32 v86, v86, v118, s[86:87]\n"
    "v_mfma_f32_16x16x32_bf16 v[100:103], v[48:51], v[172:175], v[100:103]\n"
    "s_waitcnt lgkmcnt(3)\n"
    "ds_read_b64_tr_b16 v[70:71], v5 offset:14592\n"
    "buffer_store_dwordx4 v[80:83], v10, s[56:59], s75 offen offset:0 sc1\n"
    "v_cndmask_b32 v87, v87, 0, s[86:87]\n"
    "v_mfma_f32_16x16x32_bf16 v[96:99], v[56:59], v[176:179], v[96:99]\n"
    "v_add_u32 v5, 0xffff0000, v5\n"
    "ds_read_b64_tr_b16 v[48:49], v5 offset:8192\n"
    "buffer_store_dwordx4 v[84:87], v10, s[56:59], s75 offen offset:128 sc1\n"
    "v_mfma_f32_16x16x32_bf16 v[104:107], v[60:63], v[176:179], v[104:107]\n"
    "s_and_b32 s65, s27, 3\n"
    "ds_read_b64_tr_b16 v[50:51], v5 offset:8448\n"
    "s_lshl_b32 s66, s26, 2\n"
    "s_add_u32 s65, s65, s66\n"
    "v_mfma_f32_16x16x32_bf16 v[108:111], v[60:63], v[180:183], v[108:111]\n"
    "s_lshr_b32 s97, s27, 2\n"
    "ds_read_b64_tr_b16 v[52:53], v5 offset:12288\n"
    "s_lshl_b32 s97, s97, 7\n"
    "v_mfma_f32_16x16x32_bf16 v[100:103], v[56:59], v[180:183], v[100:103]\n"
    "s_mul_i32 s99, s65, 57344\n"
    "ds_read_b64_tr_b16 v[54:55], v5 offset:12544\n"
    "s_add_u32 s99, s99, s97\n"
    "s_waitcnt lgkmcnt(4)\n"
    "v_mfma_f32_16x16x32_bf16 v[96:99], v[64:67], v[184:187], v[96:99]\n"
    "s_lshr_b32 s66, s65, 1\n"
    "ds_read_b64_tr_b16 v[56:57], v5 offset:10240\n"
    "s_lshl_b32 s66, s66, 7\n"
    "v_mfma_f32_16x16x32_bf16 v[104:107], v[68:71], v[184:187], v[104:107]\n"
    "s_add_u32 s66, s66, 4096\n"
    "ds_read_b64_tr_b16 v[58:59], v5 offset:10496\n"
    "v_and_b32 v12, 15, v0\n"
    "v_mfma_f32_16x16x32_bf16 v[108:111], v[68:71], v[188:191], v[108:111]\n"
    "s_lshr_b32 s67, s25, 1\n"
    "ds_read_b64_tr_b16 v[60:61], v5 offset:14336\n"
    "s_lshl_b32 s65, s65, 1\n"
    "v_lshl_add_u32 v75, v12, 3, s66\n"
    "v_mfma_f32_16x16x32_bf16 v[100:103], v[64:67], v[188:191], v[100:103]\n"
    "s_and_b32 s66, s25, 1\n"
    "ds_read_b64_tr_b16 v[62:63], v5 offset:14592\n"
    "s_add_u32 s65, s65, s66\n"
    "s_waitcnt lgkmcnt(4)\n"
    "v_mfma_f32_16x16x32_bf16 v[96:99], v[48:51], v[192:195], v[96:99]\n"
    "s_lshl_b32 s96, s65, 7\n"
    "v_add_u32 v5, 0xffff8000, v5\n"
    "ds_read_b64_tr_b16 v[64:65], v5 offset:0\n"
    "v_mfma_f32_16x16x32_bf16 v[104:107], v[52:55], v[192:195], v[104:107]\n"
    "s_add_u32 s96, s96, 8192\n"
    "ds_read_b64_tr_b16 v[66:67], v5 offset:256\n"
    "v_lshl_add_u32 v7, v12, 3, s96\n"
    "v_and_b32 v12, 3, v0\n"
    "v_mfma_f32_16x16x32_bf16 v[108:111], v[52:55], v[196:199], v[108:111]\n"
    "s_mul_i32 s96, s65, 114688\n"
    "ds_read_b64_tr_b16 v[68:69], v5 offset:4096\n"
    "s_mul_i32 s98, s67, 14336\n"
    "v_mfma_f32_16x16x32_bf16 v[100:103], v[48:51], v[196:199], v[100:103]\n"
    "s_add_u32 s96, s96, s98\n"
    "ds_read_b64_tr_b16 v[70:71], v5 offset:4352\n"
    "s_add_u32 s96, s96, 0x610000\n"
    "s_waitcnt lgkmcnt(4)\n"
    "v_mfma_f32_16x16x32_bf16 v[96:99], v[56:59], v[200:203], v[96:99]\n"
    "s_add_u32 s21, s96, 57344\n"
    "ds_read_b64_tr_b16 v[48:49], v5 offset:2048\n"
    "s_mul_i32 s98, s67, 3072\n"
    "s_lshl_b32 s65, s65, 15\n"
    "v_mfma_f32_16x16x32_bf16 v[104:107], v[60:63], v[200:203], v[104:107]\n"
    "s_add_u32 s98, s98, s65\n"
    "ds_read_b64_tr_b16 v[50:51], v5 offset:2304\n"
    "v_mfma_f32_16x16x32_bf16 v[108:111], v[60:63], v[204:207], v[108:111]\n"
    "s_add_u32 s98, s98, 0xd10000\n"
    "ds_read_b64_tr_b16 v[52:53], v5 offset:6144\n"
    "v_lshrrev_b32 v13, 3, v0\n"
    "v_and_b32 v14, 3, v13\n"
    "v_mfma_f32_16x16x32_bf16 v[100:103], v[56:59], v[204:207], v[100:103]\n"
    "s_waitcnt lgkmcnt(3)\n"
    "ds_read_b64_tr_b16 v[54:55], v5 offset:6400\n"
    "v_lshlrev_b32 v14, 5, v14\n"
    "v_lshl_add_u32 v14, v12, 3, v14\n"
    "v_mfma_f32_16x16x32_bf16 v[96:99], v[64:67], v[208:211], v[96:99]\n"
    "v_add_u32 v5, 0x1c000, v5\n"
    "ds_read_b64_tr_b16 v[56:57], v5 offset:0\n"
    "buffer_load_dwordx4 v3, s[40:43], s18 offen lds\n"
    "s_add_u32 m0, s63, 0x16000\n"
    "v_lshrrev_b32 v12, 2, v0\n"
    "v_lshlrev_b32 v12, 7, v12\n"
    "v_mfma_f32_16x16x32_bf16 v[104:107], v[68:71], v[208:211], v[104:107]\n"
    "s_lshl_b32 s65, s67, 12\n"
    "ds_read_b64_tr_b16 v[58:59], v5 offset:256\n"
    "buffer_load_dwordx4 v3, s[44:47], s18 offen lds\n"
    "s_add_u32 m0, s63, 0x15000\n"
    "v_add3_u32 v124, v12, v14, s65\n"
    "v_mfma_f32_16x16x32_bf16 v[108:111], v[68:71], v[212:215], v[108:111]\n"
    "s_add_u32 s18, s81, 0x8c000\n"
    "ds_read_b64_tr_b16 v[60:61], v5 offset:4096\n"
    "v_xor_b32 v125, 32, v124\n"
    "v_xor_b32 v126, 64, v124\n"
    "v_xor_b32 v127, 0x60, v124\n"
    "v_mfma_f32_16x16x32_bf16 v[100:103], v[64:67], v[212:215], v[100:103]\n"
    "s_waitcnt lgkmcnt(3)\n"
    "ds_read_b64_tr_b16 v[62:63], v5 offset:4352\n"
    "buffer_load_dwordx4 v3, s[40:43], s18 offen lds\n"
    "v_and_b32 v12, 7, v0\n"
    "v_and_b32 v14, 6, v13\n"
    "v_mfma_f32_16x16x32_bf16 v[96:99], v[48:51], v[216:219], v[96:99]\n"
    "s_add_u32 m0, s63, 0x17000\n"
    "ds_read_b64_tr_b16 v[64:65], v5 offset:2048\n"
    "buffer_load_dwordx4 v3, s[44:47], s18 offen lds\n"
    "s_add_u32 m0, s63, 0x20000\n"
    "s_add_u32 s18, s81, 0xa8000\n"
    "v_xor_b32 v14, v14, v12\n"
    "v_mul_u32_u24 v116, 896, v13\n"
    "v_mfma_f32_16x16x32_bf16 v[104:107], v[52:55], v[216:219], v[104:107]\n"
    "s_and_b32 s67, s67, 1\n"
    "ds_read_b64_tr_b16 v[66:67], v5 offset:2304\n"
    "v_lshl_add_u32 v14, v14, 4, v116\n"
    "v_mfma_f32_16x16x32_bf16 v[108:111], v[52:55], v[220:223], v[108:111]\n"
    "s_mul_i32 s65, s67, 448\n"
    "ds_read_b64_tr_b16 v[68:69], v5 offset:6144\n"
    "buffer_load_dwordx4 v3, s[40:43], s18 offen lds\n"
    "s_add_u32 m0, s63, 0x22000\n"
    "s_lshl_b32 s66, s66, 3\n"
    "v_mfma_f32_16x16x32_bf16 v[100:103], v[48:51], v[220:223], v[100:103]\n"
    "s_add_u32 s65, s65, s66\n"
    "ds_read_b64_tr_b16 v[70:71], v5 offset:6400\n"
    "buffer_load_dwordx4 v3, s[44:47], s18 offen lds\n"
    "s_add_u32 m0, s63, 0x21000\n"
    "s_add_u32 s18, s81, 0xc4000\n"
    "s_add_u32 s65, s65, s66\n"
    "s_waitcnt lgkmcnt(4)\n"
    "v_mfma_f32_16x16x32_bf16 v[96:99], v[56:59], v[224:227], v[96:99]\n"
    "s_mul_i32 s65, s65, 896\n"
    "ds_read_b64_tr_b16 v[48:49], v8 offset:49152\n"
    "v_add_u32 v2, s65, v14\n"
    "s_mul_i32 s65, s67, 112\n"
    "v_mfma_f32_16x16x32_bf16 v[104:107], v[60:63], v[224:227], v[104:107]\n"
    "s_add_u32 s65, s65, s66\n"
    "ds_read_b64_tr_b16 v[50:51], v8 offset:49664\n"
    "buffer_load_dwordx4 v3, s[40:43], s18 offen lds\n"
    "s_add_u32 m0, s63, 0x23000\n"
    "v_mfma_f32_16x16x32_bf16 v[108:111], v[60:63], v[228:231], v[108:111]\n"
    "s_mul_i32 s65, s65, 896\n"
    "ds_read_b64_tr_b16 v[52:53], v8 offset:57344\n"
    "buffer_load_dwordx4 v3, s[44:47], s18 offen lds\n"
    "s_add_u32 m0, s63, 0x24000\n"
    "s_add_u32 s18, s81, 0xe0000\n"
    "v_add_u32 v4, s65, v14\n"
    "s_mul_i32 s65, s25, 7168\n"
    "v_mfma_f32_16x16x32_bf16 v[100:103], v[56:59], v[228:231], v[100:103]\n"
    "s_waitcnt lgkmcnt(3)\n"
    "ds_read_b64_tr_b16 v[54:55], v8 offset:57856\n"
    "v_lshl_add_u32 v14, v12, 4, v116\n"
    "v_add_u32 v6, s65, v14\n"
    "v_mfma_f32_16x16x32_bf16 v[96:99], v[64:67], v[232:235], v[96:99]\n"
    "s_lshl_b32 s65, s79, 13\n"
    "ds_read_b64_tr_b16 v[56:57], v8 offset:53248\n"
    "buffer_load_dwordx4 v3, s[40:43], s18 offen lds\n"
    "s_add_u32 m0, s63, 0x26000\n"
    "v_add_u32 v78, s65, v1\n"
    "v_mfma_f32_16x16x32_bf16 v[104:107], v[68:71], v[232:235], v[104:107]\n"
    "s_and_b32 s65, s25, 3\n"
    "ds_read_b64_tr_b16 v[58:59], v8 offset:53760\n"
    "buffer_load_dwordx4 v3, s[44:47], s18 offen lds\n"
    "s_add_u32 m0, s63, 0x25000\n"
    "s_add_u32 s18, s81, 0xfc000\n"
    "v_add_u32 v73, 0xc000, v78\n"
    "v_add_u32 v74, 0xc000, v73\n"
    "v_mfma_f32_16x16x32_bf16 v[108:111], v[68:71], v[236:239], v[108:111]\n"
    "s_lshl_b32 s65, s65, 12\n"
    "ds_read_b64_tr_b16 v[60:61], v8 offset:61440\n"
    "s_add_u32 s65, s65, 0x18000\n"
    "v_mfma_f32_16x16x32_bf16 v[100:103], v[64:67], v[236:239], v[100:103]\n"
    "s_setprio 1\n"
    "ds_read_b64_tr_b16 v[62:63], v8 offset:61952\n"
    "buffer_load_dwordx4 v3, s[40:43], s18 offen lds\n"
    "s_add_u32 m0, s63, 0x27000\n"
    "v_add_u32 v76, s65, v1\n"
    "v_and_b32 v13, 1, v13\n"
    "s_waitcnt lgkmcnt(4)\n"
    "v_mfma_f32_16x16x32_bf16 v[16:19], v[48:51], v[128:131], 0\n"
    "s_and_b32 s65, s27, 1\n"
    "v_add_u32 v8, 0x10000, v8\n"
    "ds_read_b64_tr_b16 v[64:65], v8 offset:0\n"
    "buffer_load_dwordx4 v3, s[44:47], s18 offen lds\n"
    "s_add_u32 m0, s63, 0x18000\n"
    "v_xor_b32 v13, s65, v13\n"
    "v_mfma_f32_16x16x32_bf16 v[24:27], v[52:55], v[128:131], 0\n"
    "s_add_u32 s18, s81, 0x118000\n"
    "ds_read_b64_tr_b16 v[66:67], v8 offset:512\n"
    "v_lshrrev_b32 v14, 4, v0\n"
    "v_lshlrev_b32 v12, 4, v12\n"
    "v_mfma_f32_16x16x32_bf16 v[28:31], v[52:55], v[132:135], 0\n"
    "ds_read_b64_tr_b16 v[68:69], v8 offset:8192\n"
    "v_lshl_add_u32 v12, v14, 7, v12\n"
    "v_lshl_add_u32 v77, v13, 14, v12\n"
    "v_mfma_f32_16x16x32_bf16 v[20:23], v[48:51], v[132:135], 0\n"
    "s_waitcnt lgkmcnt(3)\n"
    "ds_read_b64_tr_b16 v[70:71], v8 offset:8704\n"
    "v_mul_f32 v88, v96, v112\n"
    "v_mul_f32 v89, v97, v112\n"
    "v_mfma_f32_16x16x32_bf16 v[16:19], v[56:59], v[136:139], v[16:19]\n"
    "s_setprio 0\n"
    "ds_read_b64_tr_b16 v[48:49], v8 offset:4096\n"
    "v_mul_f32 v90, v98, v112\n"
    "v_mfma_f32_16x16x32_bf16 v[24:27], v[60:63], v[136:139], v[24:27]\n"
    "ds_read_b64_tr_b16 v[50:51], v8 offset:4608\n"
    "v_mul_f32 v91, v99, v112\n"
    "v_exp_f32 v88, v88\n"
    "v_mfma_f32_16x16x32_bf16 v[28:31], v[60:63], v[140:143], v[28:31]\n"
    "ds_read_b64_tr_b16 v[52:53], v8 offset:12288\n"
    "v_mfma_f32_16x16x32_bf16 v[20:23], v[56:59], v[140:143], v[20:23]\n"
    "ds_read_b64_tr_b16 v[54:55], v8 offset:12800\n"
    "v_mul_f32 v104, v96, v104\n"
    "v_exp_f32 v89, v89\n"
    "s_waitcnt vmcnt(12)\n"
    "s_barrier\n"
    "s_setprio 3\n"
    "s_waitcnt lgkmcnt(4)\n"
    "v_mfma_f32_16x16x32_bf16 v[16:19], v[64:67], v[144:147], v[16:19]\n"
    "ds_read_b64_tr_b16 v[56:57], v9 offset:49152\n"
    "buffer_load_dwordx4 v3, s[40:43], s18 offen lds\n"
    "v_mfma_f32_16x16x32_bf16 v[24:27], v[68:71], v[144:147], v[24:27]\n"
    "s_add_u32 m0, s63, 0x1a000\n"
    "ds_read_b64_tr_b16 v[58:59], v9 offset:49664\n"
    "buffer_load_dwordx4 v3, s[44:47], s18 offen lds\n"
    "s_add_u32 m0, s63, 0x19000\n"
    "v_mul_f32 v105, v97, v105\n"
    "v_exp_f32 v90, v90\n"
    "v_mfma_f32_16x16x32_bf16 v[28:31], v[68:71], v[148:151], v[28:31]\n"
    "s_add_u32 s18, s81, 0x134000\n"
    "ds_read_b64_tr_b16 v[60:61], v9 offset:57344\n"
    "v_mfma_f32_16x16x32_bf16 v[20:23], v[64:67], v[148:151], v[20:23]\n"
    "s_waitcnt lgkmcnt(3)\n"
    "ds_read_b64_tr_b16 v[62:63], v9 offset:57856\n"
    "buffer_load_dwordx4 v3, s[40:43], s18 offen lds\n"
    "v_mul_f32 v106, v98, v106\n"
    "v_exp_f32 v91, v91\n"
    "v_mfma_f32_16x16x32_bf16 v[16:19], v[48:51], v[152:155], v[16:19]\n"
    "s_add_u32 m0, s63, 0x1b000\n"
    "ds_read_b64_tr_b16 v[64:65], v9 offset:53248\n"
    "buffer_load_dwordx4 v3, s[44:47], s18 offen lds\n"
    "s_add_u32 m0, s63, 0x1c000\n"
    "v_mfma_f32_16x16x32_bf16 v[24:27], v[52:55], v[152:155], v[24:27]\n"
    "s_add_u32 s18, s81, 0x150000\n"
    "ds_read_b64_tr_b16 v[66:67], v9 offset:53760\n"
    "v_mul_f32 v107, v99, v107\n"
    "v_mfma_f32_16x16x32_bf16 v[28:31], v[52:55], v[156:159], v[28:31]\n"
    "ds_read_b64_tr_b16 v[68:69], v9 offset:61440\n"
    "buffer_load_dwordx4 v3, s[40:43], s18 offen lds\n"
    "s_add_u32 m0, s63, 0x1e000\n"
    "v_fma_f32 v88, v88, v114, v114\n"
    "v_fma_f32 v89, v89, v114, v114\n"
    "v_mfma_f32_16x16x32_bf16 v[20:23], v[48:51], v[156:159], v[20:23]\n"
    "s_waitcnt lgkmcnt(3)\n"
    "ds_read_b64_tr_b16 v[70:71], v9 offset:61952\n"
    "buffer_load_dwordx4 v3, s[44:47], s18 offen lds\n"
    "s_add_u32 m0, s63, 0x1d000\n"
    "v_fma_f32 v90, v90, v114, v114\n"
    "v_mfma_f32_16x16x32_bf16 v[32:35], v[56:59], v[128:131], 0\n"
    "s_add_u32 s18, s81, 0x16c000\n"
    "v_add_u32 v9, 0x10000, v9\n"
    "ds_read_b64_tr_b16 v[48:49], v9 offset:0\n"
    "v_fma_f32 v91, v91, v114, v114\n"
    "v_rcp_f32 v88, v88\n"
    "v_mfma_f32_16x16x32_bf16 v[40:43], v[60:63], v[128:131], 0\n"
    "ds_read_b64_tr_b16 v[50:51], v9 offset:512\n"
    "buffer_load_dwordx4 v3, s[40:43], s18 offen lds\n"
    "v_mfma_f32_16x16x32_bf16 v[44:47], v[60:63], v[132:135], 0\n"
    "s_add_u32 m0, s63, 0x1f000\n"
    "ds_read_b64_tr_b16 v[52:53], v9 offset:8192\n"
    "buffer_load_dwordx4 v3, s[44:47], s18 offen lds\n"
    "v_rcp_f32 v89, v89\n"
    "v_mfma_f32_16x16x32_bf16 v[36:39], v[56:59], v[132:135], 0\n"
    "s_waitcnt lgkmcnt(3)\n"
    "ds_read_b64_tr_b16 v[54:55], v9 offset:8704\n"
    "v_rcp_f32 v90, v90\n"
    "v_mfma_f32_16x16x32_bf16 v[32:35], v[64:67], v[136:139], v[32:35]\n"
    "ds_read_b64_tr_b16 v[56:57], v9 offset:4096\n"
    "v_rcp_f32 v91, v91\n"
    "v_mfma_f32_16x16x32_bf16 v[40:43], v[68:71], v[136:139], v[40:43]\n"
    "s_setprio 1\n"
    "ds_read_b64_tr_b16 v[58:59], v9 offset:4608\n"
    "v_mfma_f32_16x16x32_bf16 v[44:47], v[68:71], v[140:143], v[44:47]\n"
    "s_setprio 0\n"
    "ds_read_b64_tr_b16 v[60:61], v9 offset:12288\n"
    "v_mul_f32 v104, v104, v88\n"
    "v_mfma_f32_16x16x32_bf16 v[36:39], v[64:67], v[140:143], v[36:39]\n"
    "ds_read_b64_tr_b16 v[62:63], v9 offset:12800\n"
    "v_mul_f32 v105, v105, v89\n"
    "v_mul_f32 v106, v106, v90\n"
    "s_waitcnt vmcnt(12)\n"
    "s_barrier\n"
    "s_setprio 3\n"
    "s_waitcnt lgkmcnt(4)\n"
    "v_mfma_f32_16x16x32_bf16 v[32:35], v[48:51], v[144:147], v[32:35]\n"
    "ds_read_b64_tr_b16 v[64:65], v8 offset:16384\n"
    "v_mul_f32 v107, v107, v91\n"
    "v_mfma_f32_16x16x32_bf16 v[40:43], v[52:55], v[144:147], v[40:43]\n"
    "ds_read_b64_tr_b16 v[66:67], v8 offset:16896\n"
    "v_cvt_pk_bf16_f32 v120, v104, v105\n"
    "v_cvt_pk_bf16_f32 v121, v106, v107\n"
    "v_mfma_f32_16x16x32_bf16 v[44:47], v[52:55], v[148:151], v[44:47]\n"
    "ds_read_b64_tr_b16 v[68:69], v8 offset:24576\n"
    "v_mul_f32 v92, v100, v113\n"
    "v_mfma_f32_16x16x32_bf16 v[36:39], v[48:51], v[148:151], v[36:39]\n"
    "s_waitcnt lgkmcnt(3)\n"
    "ds_read_b64_tr_b16 v[70:71], v8 offset:25088\n"
    "v_mul_f32 v93, v101, v113\n"
    "v_mul_f32 v94, v102, v113\n"
    "v_mfma_f32_16x16x32_bf16 v[32:35], v[56:59], v[152:155], v[32:35]\n"
    "ds_read_b64_tr_b16 v[48:49], v8 offset:20480\n"
    "v_mul_f32 v95, v103, v113\n"
    "v_mfma_f32_16x16x32_bf16 v[40:43], v[60:63], v[152:155], v[40:43]\n"
    "ds_read_b64_tr_b16 v[50:51], v8 offset:20992\n"
    "v_exp_f32 v92, v92\n"
    "v_mfma_f32_16x16x32_bf16 v[44:47], v[60:63], v[156:159], v[44:47]\n"
    "ds_read_b64_tr_b16 v[52:53], v8 offset:28672\n"
    "v_mul_f32 v108, v100, v108\n"
    "v_mfma_f32_16x16x32_bf16 v[36:39], v[56:59], v[156:159], v[36:39]\n"
    "s_waitcnt lgkmcnt(3)\n"
    "ds_read_b64_tr_b16 v[54:55], v8 offset:29184\n"
    "v_exp_f32 v93, v93\n"
    "v_mfma_f32_16x16x32_bf16 v[16:19], v[64:67], v[160:163], v[16:19]\n"
    "v_add_u32 v8, 0x8000, v8\n"
    "ds_read_b64_tr_b16 v[56:57], v8 offset:32768\n"
    "v_mul_f32 v109, v101, v109\n"
    "v_mfma_f32_16x16x32_bf16 v[24:27], v[68:71], v[160:163], v[24:27]\n"
    "ds_read_b64_tr_b16 v[58:59], v8 offset:33280\n"
    "v_exp_f32 v94, v94\n"
    "v_mfma_f32_16x16x32_bf16 v[28:31], v[68:71], v[164:167], v[28:31]\n"
    "ds_read_b64_tr_b16 v[60:61], v8 offset:40960\n"
    "v_mul_f32 v110, v102, v110\n"
    "v_mfma_f32_16x16x32_bf16 v[20:23], v[64:67], v[164:167], v[20:23]\n"
    "s_waitcnt lgkmcnt(3)\n"
    "ds_read_b64_tr_b16 v[62:63], v8 offset:41472\n"
    "v_exp_f32 v95, v95\n"
    "v_mfma_f32_16x16x32_bf16 v[16:19], v[48:51], v[168:171], v[16:19]\n"
    "ds_read_b64_tr_b16 v[64:65], v8 offset:36864\n"
    "v_mul_f32 v111, v103, v111\n"
    "v_mfma_f32_16x16x32_bf16 v[24:27], v[52:55], v[168:171], v[24:27]\n"
    "s_setprio 1\n"
    "ds_read_b64_tr_b16 v[66:67], v8 offset:37376\n"
    "v_fma_f32 v92, v92, v115, v115\n"
    "v_mfma_f32_16x16x32_bf16 v[28:31], v[52:55], v[172:175], v[28:31]\n"
    "s_setprio 0\n"
    "ds_read_b64_tr_b16 v[68:69], v8 offset:45056\n"
    "v_fma_f32 v93, v93, v115, v115\n"
    "v_fma_f32 v94, v94, v115, v115\n"
    "v_mfma_f32_16x16x32_bf16 v[20:23], v[48:51], v[172:175], v[20:23]\n"
    "ds_read_b64_tr_b16 v[70:71], v8 offset:45568\n"
    "v_fma_f32 v95, v95, v115, v115\n"
    "s_waitcnt vmcnt(4)\n"
    "s_barrier\n"
    "s_setprio 3\n"
    "s_waitcnt lgkmcnt(4)\n"
    "v_mfma_f32_16x16x32_bf16 v[16:19], v[56:59], v[176:179], v[16:19]\n"
    "ds_read_b64_tr_b16 v[48:49], v8 offset:49152\n"
    "v_rcp_f32 v92, v92\n"
    "v_mfma_f32_16x16x32_bf16 v[24:27], v[60:63], v[176:179], v[24:27]\n"
    "ds_read_b64_tr_b16 v[50:51], v8 offset:49664\n"
    "v_rcp_f32 v93, v93\n"
    "v_mfma_f32_16x16x32_bf16 v[28:31], v[60:63], v[180:183], v[28:31]\n"
    "ds_read_b64_tr_b16 v[52:53], v8 offset:57344\n"
    "v_mfma_f32_16x16x32_bf16 v[20:23], v[56:59], v[180:183], v[20:23]\n"
    "s_waitcnt lgkmcnt(3)\n"
    "ds_read_b64_tr_b16 v[54:55], v8 offset:57856\n"
    "v_rcp_f32 v94, v94\n"
    "v_mfma_f32_16x16x32_bf16 v[16:19], v[64:67], v[184:187], v[16:19]\n"
    "ds_read_b64_tr_b16 v[56:57], v8 offset:53248\n"
    "v_rcp_f32 v95, v95\n"
    "v_mfma_f32_16x16x32_bf16 v[24:27], v[68:71], v[184:187], v[24:27]\n"
    "ds_read_b64_tr_b16 v[58:59], v8 offset:53760\n"
    "v_mul_f32 v108, v108, v92\n"
    "v_mfma_f32_16x16x32_bf16 v[28:31], v[68:71], v[188:191], v[28:31]\n"
    "ds_read_b64_tr_b16 v[60:61], v8 offset:61440\n"
    "v_mul_f32 v109, v109, v93\n"
    "v_mfma_f32_16x16x32_bf16 v[20:23], v[64:67], v[188:191], v[20:23]\n"
    "s_waitcnt lgkmcnt(3)\n"
    "ds_read_b64_tr_b16 v[62:63], v8 offset:61952\n"
    "v_mul_f32 v110, v110, v94\n"
    "v_mul_f32 v111, v111, v95\n"
    "v_mfma_f32_16x16x32_bf16 v[16:19], v[48:51], v[192:195], v[16:19]\n"
    "ds_read_b64_tr_b16 v[64:65], v8 offset:0\n"
    "v_cvt_pk_bf16_f32 v122, v108, v109\n"
    "v_mfma_f32_16x16x32_bf16 v[24:27], v[52:55], v[192:195], v[24:27]\n"
    "ds_read_b64_tr_b16 v[66:67], v8 offset:512\n"
    "v_cvt_pk_bf16_f32 v123, v110, v111\n"
    "v_mfma_f32_16x16x32_bf16 v[28:31], v[52:55], v[196:199], v[28:31]\n"
    "ds_read_b64_tr_b16 v[68:69], v8 offset:8192\n"
    "v_mfma_f32_16x16x32_bf16 v[20:23], v[48:51], v[196:199], v[20:23]\n"
    "s_waitcnt lgkmcnt(3)\n"
    "ds_read_b64_tr_b16 v[70:71], v8 offset:8704\n"
    "ds_write_b64 v79, v[122:123] offset:49152\n"
    "v_mfma_f32_16x16x32_bf16 v[16:19], v[56:59], v[200:203], v[16:19]\n"
    "ds_read_b64_tr_b16 v[48:49], v8 offset:4096\n"
    "v_mfma_f32_16x16x32_bf16 v[24:27], v[60:63], v[200:203], v[24:27]\n"
    "s_setprio 1\n"
    "ds_read_b64_tr_b16 v[50:51], v8 offset:4608\n"
    "v_mfma_f32_16x16x32_bf16 v[28:31], v[60:63], v[204:207], v[28:31]\n"
    "s_setprio 0\n"
    "ds_read_b64_tr_b16 v[52:53], v8 offset:12288\n"
    "v_mfma_f32_16x16x32_bf16 v[20:23], v[56:59], v[204:207], v[20:23]\n"
    "ds_read_b64_tr_b16 v[54:55], v8 offset:12800\n"
    "s_waitcnt vmcnt(0)\n"
    "s_waitcnt lgkmcnt(4)\n"
    "s_barrier\n"
    "s_setprio 3\n"
    "v_mfma_f32_16x16x32_bf16 v[16:19], v[64:67], v[208:211], v[16:19]\n"
    "ds_read_b64_tr_b16 v[56:57], v8 offset:16384\n"
    "v_mfma_f32_16x16x32_bf16 v[24:27], v[68:71], v[208:211], v[24:27]\n"
    "ds_read_b64_tr_b16 v[58:59], v8 offset:16896\n"
    "ds_read_b64 v[122:123], v79 offset:49664\n"
    "v_mfma_f32_16x16x32_bf16 v[28:31], v[68:71], v[212:215], v[28:31]\n"
    "ds_read_b64_tr_b16 v[60:61], v8 offset:24576\n"
    "v_mfma_f32_16x16x32_bf16 v[20:23], v[64:67], v[212:215], v[20:23]\n"
    "s_waitcnt lgkmcnt(4)\n"
    "ds_read_b64_tr_b16 v[62:63], v8 offset:25088\n"
    "v_mfma_f32_16x16x32_bf16 v[16:19], v[48:51], v[216:219], v[16:19]\n"
    "ds_read_b64_tr_b16 v[64:65], v8 offset:20480\n"
    "v_mfma_f32_16x16x32_bf16 v[24:27], v[52:55], v[216:219], v[24:27]\n"
    "ds_read_b64_tr_b16 v[66:67], v8 offset:20992\n"
    "v_mfma_f32_16x16x32_bf16 v[28:31], v[52:55], v[220:223], v[28:31]\n"
    "ds_read_b64_tr_b16 v[68:69], v8 offset:28672\n"
    "v_mfma_f32_16x16x32_bf16 v[20:23], v[48:51], v[220:223], v[20:23]\n"
    "s_waitcnt lgkmcnt(3)\n"
    "ds_read_b64_tr_b16 v[70:71], v8 offset:29184\n"
    "v_mfma_f32_16x16x32_bf16 v[16:19], v[56:59], v[224:227], v[16:19]\n"
    "ds_read_b64_tr_b16 v[48:49], v9 offset:16384\n"
    "v_mfma_f32_16x16x32_bf16 v[24:27], v[60:63], v[224:227], v[24:27]\n"
    "ds_read_b64_tr_b16 v[50:51], v9 offset:16896\n"
    "v_mfma_f32_16x16x32_bf16 v[28:31], v[60:63], v[228:231], v[28:31]\n"
    "ds_read_b64_tr_b16 v[52:53], v9 offset:24576\n"
    "v_mfma_f32_16x16x32_bf16 v[20:23], v[56:59], v[228:231], v[20:23]\n"
    "s_waitcnt lgkmcnt(3)\n"
    "ds_read_b64_tr_b16 v[54:55], v9 offset:25088\n"
    "v_mfma_f32_16x16x32_bf16 v[16:19], v[64:67], v[232:235], v[16:19]\n"
    "ds_read_b64_tr_b16 v[56:57], v9 offset:20480\n"
    "v_mfma_f32_16x16x32_bf16 v[24:27], v[68:71], v[232:235], v[24:27]\n"
    "ds_read_b64_tr_b16 v[58:59], v9 offset:20992\n"
    "v_mfma_f32_16x16x32_bf16 v[28:31], v[68:71], v[236:239], v[28:31]\n"
    "ds_read_b64_tr_b16 v[60:61], v9 offset:28672\n"
    "v_mfma_f32_16x16x32_bf16 v[20:23], v[64:67], v[236:239], v[20:23]\n"
    "s_waitcnt lgkmcnt(3)\n"
    "ds_read_b64_tr_b16 v[62:63], v9 offset:29184\n"
    "v_mfma_f32_16x16x32_bf16 v[32:35], v[48:51], v[160:163], v[32:35]\n"
    "v_add_u32 v9, 0x8000, v9\n"
    "ds_read_b64_tr_b16 v[64:65], v9 offset:32768\n"
    "v_mfma_f32_16x16x32_bf16 v[40:43], v[52:55], v[160:163], v[40:43]\n"
    "ds_read_b64_tr_b16 v[66:67], v9 offset:33280\n"
    "v_mfma_f32_16x16x32_bf16 v[44:47], v[52:55], v[164:167], v[44:47]\n"
    "ds_read_b64_tr_b16 v[68:69], v9 offset:40960\n"
    "v_mfma_f32_16x16x32_bf16 v[36:39], v[48:51], v[164:167], v[36:39]\n"
    "s_setprio 1\n"
    "ds_read_b64_tr_b16 v[70:71], v9 offset:41472\n"
    "v_mul_f32 v88, v16, v112\n"
    "v_mul_f32 v89, v17, v112\n"
    "s_waitcnt lgkmcnt(4)\n"
    "v_mfma_f32_16x16x32_bf16 v[32:35], v[56:59], v[168:171], v[32:35]\n"
    "ds_read_b64_tr_b16 v[48:49], v9 offset:36864\n"
    "v_mul_f32 v90, v18, v112\n"
    "v_mul_f32 v91, v19, v112\n"
    "v_mfma_f32_16x16x32_bf16 v[40:43], v[60:63], v[168:171], v[40:43]\n"
    "ds_read_b64_tr_b16 v[50:51], v9 offset:37376\n"
    "v_exp_f32 v88, v88\n"
    "v_mfma_f32_16x16x32_bf16 v[44:47], v[60:63], v[172:175], v[44:47]\n"
    "s_setprio 0\n"
    "ds_read_b64_tr_b16 v[52:53], v9 offset:45056\n"
    "v_mul_f32 v24, v16, v24\n"
    "v_exp_f32 v89, v89\n"
    "v_mfma_f32_16x16x32_bf16 v[36:39], v[56:59], v[172:175], v[36:39]\n"
    "ds_read_b64_tr_b16 v[54:55], v9 offset:45568\n"
    "v_mul_f32 v25, v17, v25\n"
    "s_barrier\n"
    "s_setprio 1\n"
    "s_waitcnt lgkmcnt(4)\n"
    "v_mfma_f32_16x16x32_bf16 v[32:35], v[64:67], v[176:179], v[32:35]\n"
    "ds_read_b64_tr_b16 v[56:57], v9 offset:49152\n"
    "buffer_load_dwordx4 v[104:107], v1, s[56:59], s92 offen offset:0 sc1\n"
    "v_exp_f32 v90, v90\n"
    "v_mfma_f32_16x16x32_bf16 v[40:43], v[68:71], v[176:179], v[40:43]\n"
    "ds_read_b64_tr_b16 v[58:59], v9 offset:49664\n"
    "buffer_load_dwordx4 v[108:111], v1, s[56:59], s92 offen offset:1024 sc1\n"
    "v_mul_f32 v26, v18, v26\n"
    "v_mfma_f32_16x16x32_bf16 v[44:47], v[68:71], v[180:183], v[44:47]\n"
    "ds_read_b64_tr_b16 v[60:61], v9 offset:57344\n"
    "v_exp_f32 v91, v91\n"
    "v_mfma_f32_16x16x32_bf16 v[36:39], v[64:67], v[180:183], v[36:39]\n"
    "s_add_u32 m0, s17, 0xc000\n"
    "ds_read_b64_tr_b16 v[62:63], v9 offset:57856\n"
    "s_add_u32 s11, s90, 0x0\n"
    "buffer_load_dwordx4 v1, s[56:59], s11 offen sc1 lds\n"
    "v_mul_f32 v27, v19, v27\n"
    "v_fma_f32 v88, v88, v114, v114\n"
    "s_waitcnt lgkmcnt(4)\n"
    "v_mfma_f32_16x16x32_bf16 v[32:35], v[48:51], v[184:187], v[32:35]\n"
    "ds_read_b64_tr_b16 v[64:65], v9 offset:53248\n"
    "buffer_load_dwordx4 v1, s[56:59], s11 offen offset:1024 sc1 lds\n"
    "v_fma_f32 v89, v89, v114, v114\n"
    "v_fma_f32 v90, v90, v114, v114\n"
    "v_mfma_f32_16x16x32_bf16 v[40:43], v[52:55], v[184:187], v[40:43]\n"
    "ds_read_b64_tr_b16 v[66:67], v9 offset:53760\n"
    "v_fma_f32 v91, v91, v114, v114\n"
    "v_rcp_f32 v88, v88\n"
    "v_mfma_f32_16x16x32_bf16 v[44:47], v[52:55], v[188:191], v[44:47]\n"
    "ds_read_b64_tr_b16 v[68:69], v9 offset:61440\n"
    "buffer_load_dwordx4 v1, s[56:59], s11 offen offset:2048 sc1 lds\n"
    "v_rcp_f32 v89, v89\n"
    "v_mfma_f32_16x16x32_bf16 v[36:39], v[48:51], v[188:191], v[36:39]\n"
    "s_waitcnt lgkmcnt(3)\n"
    "ds_read_b64_tr_b16 v[70:71], v9 offset:61952\n"
    "v_mfma_f32_16x16x32_bf16 v[32:35], v[56:59], v[192:195], v[32:35]\n"
    "ds_read_b64_tr_b16 v[48:49], v9 offset:0\n"
    "buffer_load_dwordx4 v1, s[56:59], s11 offen offset:3072 sc1 lds\n"
    "v_rcp_f32 v90, v90\n"
    "v_mfma_f32_16x16x32_bf16 v[40:43], v[60:63], v[192:195], v[40:43]\n"
    "s_add_u32 m0, s17, 0x10000\n"
    "ds_read_b64_tr_b16 v[50:51], v9 offset:512\n"
    "s_add_u32 s11, s90, 0x4000\n"
    "buffer_load_dwordx4 v1, s[56:59], s11 offen sc1 lds\n"
    "v_rcp_f32 v91, v91\n"
    "v_mfma_f32_16x16x32_bf16 v[44:47], v[60:63], v[196:199], v[44:47]\n"
    "ds_read_b64_tr_b16 v[52:53], v9 offset:8192\n"
    "v_mul_f32 v24, v24, v88\n"
    "v_mul_f32 v25, v25, v89\n"
    "v_mfma_f32_16x16x32_bf16 v[36:39], v[56:59], v[196:199], v[36:39]\n"
    "s_waitcnt lgkmcnt(3)\n"
    "ds_read_b64_tr_b16 v[54:55], v9 offset:8704\n"
    "buffer_load_dwordx4 v1, s[56:59], s11 offen offset:1024 sc1 lds\n"
    "v_mul_f32 v26, v26, v90\n"
    "v_mul_f32 v27, v27, v91\n"
    "v_mfma_f32_16x16x32_bf16 v[32:35], v[64:67], v[200:203], v[32:35]\n"
    "ds_read_b64_tr_b16 v[56:57], v9 offset:4096\n"
    "buffer_load_dwordx4 v1, s[56:59], s11 offen offset:2048 sc1 lds\n"
    "v_cvt_pk_bf16_f32 v240, v24, v25\n"
    "v_cvt_pk_bf16_f32 v241, v26, v27\n"
    "v_mfma_f32_16x16x32_bf16 v[40:43], v[68:71], v[200:203], v[40:43]\n"
    "ds_read_b64_tr_b16 v[58:59], v9 offset:4608\n"
    "v_mul_f32 v92, v20, v113\n"
    "v_mfma_f32_16x16x32_bf16 v[44:47], v[68:71], v[204:207], v[44:47]\n"
    "ds_read_b64_tr_b16 v[60:61], v9 offset:12288\n"
    "buffer_load_dwordx4 v1, s[56:59], s11 offen offset:3072 sc1 lds\n"
    "v_mul_f32 v93, v21, v113\n"
    "v_mul_f32 v94, v22, v113\n"
    "v_mfma_f32_16x16x32_bf16 v[36:39], v[64:67], v[204:207], v[36:39]\n"
    "s_waitcnt lgkmcnt(3)\n"
    "ds_read_b64_tr_b16 v[62:63], v9 offset:12800\n"
    "v_mul_f32 v95, v23, v113\n"
    "v_exp_f32 v92, v92\n"
    "v_mfma_f32_16x16x32_bf16 v[32:35], v[48:51], v[208:211], v[32:35]\n"
    "s_add_u32 m0, s17, 0x14000\n"
    "ds_read_b64_tr_b16 v[64:65], v9 offset:16384\n"
    "s_add_u32 s11, s90, 0x8000\n"
    "buffer_load_dwordx4 v1, s[56:59], s11 offen sc1 lds\n"
    "v_mul_f32 v28, v20, v28\n"
    "v_mfma_f32_16x16x32_bf16 v[40:43], v[52:55], v[208:211], v[40:43]\n"
    "ds_read_b64_tr_b16 v[66:67], v9 offset:16896\n"
    "buffer_load_dwordx4 v1, s[56:59], s11 offen offset:1024 sc1 lds\n"
    "Lst0_5:\n"
    "s_cbranch_execz Lr0_5\n"
    "v_exp_f32 v93, v93\n"
    "v_mfma_f32_16x16x32_bf16 v[44:47], v[52:55], v[212:215], v[44:47]\n"
    "ds_read_b64_tr_b16 v[68:69], v9 offset:24576\n"
    "v_mul_f32 v29, v21, v29\n"
    "v_exp_f32 v94, v94\n"
    "v_mfma_f32_16x16x32_bf16 v[36:39], v[48:51], v[212:215], v[36:39]\n"
    "s_waitcnt lgkmcnt(3)\n"
    "ds_read_b64_tr_b16 v[70:71], v9 offset:25088\n"
    "buffer_load_dwordx4 v1, s[56:59], s11 offen offset:2048 sc1 lds\n"
    "v_mfma_f32_16x16x32_bf16 v[32:35], v[56:59], v[216:219], v[32:35]\n"
    "ds_read_b64_tr_b16 v[48:49], v9 offset:20480\n"
    "buffer_load_dwordx4 v1, s[56:59], s11 offen offset:3072 sc1 lds\n"
    "v_mul_f32 v30, v22, v30\n"
    "v_exp_f32 v95, v95\n"
    "v_mfma_f32_16x16x32_bf16 v[40:43], v[60:63], v[216:219], v[40:43]\n"
    "ds_read_b64_tr_b16 v[50:51], v9 offset:20992\n"
    "v_mul_f32 v31, v23, v31\n"
    "v_mfma_f32_16x16x32_bf16 v[44:47], v[60:63], v[220:223], v[44:47]\n"
    "ds_read_b64_tr_b16 v[52:53], v9 offset:28672\n"
    "buffer_store_dwordx4 v[120:123], v1, s[56:59], s82 offen sc1\n"
    "v_fma_f32 v92, v92, v115, v115\n"
    "v_fma_f32 v93, v93, v115, v115\n"
    "v_mfma_f32_16x16x32_bf16 v[36:39], v[56:59], v[220:223], v[36:39]\n"
    "s_waitcnt lgkmcnt(3)\n"
    "ds_read_b64_tr_b16 v[54:55], v9 offset:29184\n"
    "v_fma_f32 v94, v94, v115, v115\n"
    "v_fma_f32 v95, v95, v115, v115\n"
    "v_mfma_f32_16x16x32_bf16 v[32:35], v[64:67], v[224:227], v[32:35]\n"
    "v_rcp_f32 v92, v92\n"
    "v_mfma_f32_16x16x32_bf16 v[40:43], v[68:71], v[224:227], v[40:43]\n"
    "v_rcp_f32 v93, v93\n"
    "v_mfma_f32_16x16x32_bf16 v[44:47], v[68:71], v[228:231], v[44:47]\n"
    "v_rcp_f32 v94, v94\n"
    "v_mfma_f32_16x16x32_bf16 v[36:39], v[64:67], v[228:231], v[36:39]\n"
    "s_waitcnt lgkmcnt(0)\n"
    "v_rcp_f32 v95, v95\n"
    "v_mfma_f32_16x16x32_bf16 v[32:35], v[48:51], v[232:235], v[32:35]\n"
    "v_mul_f32 v28, v28, v92\n"
    "v_mfma_f32_16x16x32_bf16 v[40:43], v[52:55], v[232:235], v[40:43]\n"
    "v_mul_f32 v29, v29, v93\n"
    "v_mul_f32 v30, v30, v94\n"
    "v_mfma_f32_16x16x32_bf16 v[44:47], v[52:55], v[236:239], v[44:47]\n"
    "v_mul_f32 v31, v31, v95\n"
    "v_cvt_pk_bf16_f32 v244, v28, v29\n"
    "v_mfma_f32_16x16x32_bf16 v[36:39], v[48:51], v[236:239], v[36:39]\n"
    "v_cvt_pk_bf16_f32 v245, v30, v31\n"
    "Lattn_h0:\n"
    "Lst0_6:\n"
    "s_cbranch_execz Lr0_6\n"
    "s_waitcnt vmcnt(8)\n"
    "s_barrier\n"
    "s_mov_b64 s[84:85], s[56:57]\n"
    "s_mov_b32 s87, 0x20000\n"
    "s_cmp_ge_u32 s27, 28\n"
    "s_cselect_b32 s50, 0, s50\n"
    "s_cselect_b32 s86, 0, s58\n"
    "s_setprio 3\n"
    "ds_read_b128 v[64:67], v1 offset:49152\n"
    "ds_read_b128 v[68:71], v1 offset:50176\n"
    "ds_read_b128 v[80:83], v73 offset:0\n"
    "ds_read_b128 v[84:87], v73 offset:1024\n"
    "ds_read_b128 v[96:99], v73 offset:2048\n"
    "ds_read_b128 v[120:123], v73 offset:3072\n"
    "ds_read_b128 v[100:103], v73 offset:6144\n"
    "ds_read_b128 v[8:11], v73 offset:7168\n"
    "v_mul_f32 v88, v32, v112\n"
    "v_mul_f32 v89, v33, v112\n"
    "v_mul_f32 v90, v34, v112\n"
    "v_mul_f32 v91, v35, v112\n"
    "v_exp_f32 v88, v88\n"
    "v_mul_f32 v40, v32, v40\n"
    "v_exp_f32 v89, v89\n"
    "v_mul_f32 v41, v33, v41\n"
    "v_exp_f32 v90, v90\n"
    "v_mul_f32 v42, v34, v42\n"
    "v_exp_f32 v91, v91\n"
    "v_mul_f32 v43, v35, v43\n"
    "s_waitcnt lgkmcnt(7)\n"
    "v_mfma_f32_32x32x16_bf16 v[16:31], v[64:67], v[104:107], 0\n"
    "s_waitcnt lgkmcnt(6)\n"
    "v_fma_f32 v88, v88, v114, v114\n"
    "v_fma_f32 v89, v89, v114, v114\n"
    "v_fma_f32 v90, v90, v114, v114\n"
    "v_mfma_f32_32x32x16_bf16 v[16:31], v[68:71], v[108:111], v[16:31]\n"
    "s_waitcnt lgkmcnt(5)\n"
    "v_fma_f32 v91, v91, v114, v114\n"
    "v_rcp_f32 v88, v88\n"
    "v_rcp_f32 v89, v89\n"
    "v_mfma_f32_32x32x16_bf16 v[48:63], v[80:83], v[104:107], 0\n"
    "s_waitcnt lgkmcnt(4)\n"
    "ds_read_b128 v[80:83], v73 offset:4096\n"
    "v_rcp_f32 v90, v90\n"
    "v_rcp_f32 v91, v91\n"
    "v_mul_f32 v40, v40, v88\n"
    "v_mul_f32 v41, v41, v89\n"
    "v_mul_f32 v42, v42, v90\n"
    "v_mul_f32 v43, v43, v91\n"
    "v_cvt_pk_bf16_f32 v242, v40, v41\n"
    "v_cvt_pk_bf16_f32 v243, v42, v43\n"
    "buffer_store_dwordx4 v[240:243], v1, s[56:59], s83 offen sc1\n"
    "v_mul_f32 v92, v36, v113\n"
    "v_max3_f32 v116, v16, v17, v18\n"
    "v_max3_f32 v117, v19, v20, v21\n"
    "v_max3_f32 v116, v116, v22, v23\n"
    "v_max3_f32 v117, v117, v24, v25\n"
    "v_max3_f32 v116, v116, v26, v27\n"
    "v_max3_f32 v117, v117, v28, v29\n"
    "v_max3_f32 v116, v116, v30, v31\n"
    "v_cmp_lt_u32 vcc, 31, v0\n"
    "v_max_f32 v116, v116, v117\n"
    "v_mov_b32 v117, v116\n"
    "v_mul_f32 v93, v37, v113\n"
    "v_mul_f32 v94, v38, v113\n"
    "v_permlane32_swap_b32 v116, v117\n"
    "v_max_f32 v116, v116, v117\n"
    "v_sub_f32 v116, 0xc2cc0000, v116\n"
    "v_and_b32 v117, 0xffff0000, v116\n"
    "v_sub_f32 v116, v116, v117\n"
    "v_cvt_pk_bf16_f32 v116, v116, v117\n"
    "v_cndmask_b32 v110, v110, v116, vcc\n"
    "v_mov_b32 v72, 0xdddddddd\n"
    "v_mov_b32 v64, 0\n"
    "v_mov_b32 v65, 0\n"
    "v_mov_b32 v66, 0\n"
    "v_mov_b32 v67, 0\n"
    "v_add_u32 v3, 6144, v2\n"
    "v_mfma_f32_32x32x16_bf16 v[48:63], v[84:87], v[108:111], v[48:63]\n"
    "ds_read_b128 v[84:87], v73 offset:5120\n"
    "v_mfma_f32_32x32x16_bf16 v[16:31], v[64:67], v[64:67], 0\n"
    "s_setprio 2\n"
    "v_mul_f32 v95, v39, v113\n"
    "v_exp_f32 v92, v92\n"
    "v_mul_f32 v44, v36, v44\n"
    "v_exp_f32 v93, v93\n"
    "v_mul_f32 v45, v37, v45\n"
    "v_exp_f32 v94, v94\n"
    "v_mul_f32 v46, v38, v46\n"
    "v_exp_f32 v95, v95\n"
    "v_mul_f32 v47, v39, v47\n"
    "v_fma_f32 v92, v92, v115, v115\n"
    "v_fma_f32 v93, v93, v115, v115\n"
    "v_fma_f32 v94, v94, v115, v115\n"
    "v_fma_f32 v95, v95, v115, v115\n"
    "v_rcp_f32 v92, v92\n"
    "s_setprio 1\n"
    "s_add_u32 s83, s83, s76\n"
    "v_rcp_f32 v93, v93\n"
    "v_rcp_f32 v94, v94\n"
    "v_rcp_f32 v95, v95\n"
    "v_mul_f32 v44, v44, v92\n"
    "v_mul_f32 v45, v45, v93\n"
    "v_mul_f32 v46, v46, v94\n"
    "v_mul_f32 v47, v47, v95\n"
    "v_cvt_pk_bf16_f32 v246, v44, v45\n"
    "v_cvt_pk_bf16_f32 v247, v46, v47\n"
    "buffer_store_dwordx4 v[244:247], v1, s[56:59], s83 offen sc1\n"
    "v_exp_f32 v48, v48\n"
    "s_setprio 0\n"
    "v_exp_f32 v49, v49\n"
    "s_waitcnt vmcnt(2) lgkmcnt(0)\n"
    "s_barrier\n"
    "s_setprio 3\n"
    "ds_read_b128 v[88:91], v73 offset:19456\n"
    "v_exp_f32 v50, v50\n"
    "v_exp_f32 v51, v51\n"
    "v_exp_f32 v52, v52\n"
    "v_mfma_f32_32x32x16_bf16 v[32:47], v[80:83], v[104:107], 0\n"
    "s_add_u32 m0, s17, 0x18000\n"
    "ds_read_b128 v[80:83], v73 offset:16384\n"
    "s_add_u32 s11, s90, 0xc000\n"
    "buffer_load_dwordx4 v1, s[56:59], s11 offen sc1 lds\n"
    "buffer_load_dwordx4 v1, s[56:59], s11 offen offset:1024 sc1 lds\n"
    "buffer_load_dwordx4 v1, s[56:59], s11 offen offset:2048 sc1 lds\n"
    "buffer_load_dwordx4 v1, s[56:59], s11 offen offset:3072 sc1 lds\n"
    "v_exp_f32 v53, v53\n"
    "v_exp_f32 v54, v54\n"
    "v_exp_f32 v55, v55\n"
    "v_exp_f32 v56, v56\n"
    "v_mfma_f32_32x32x16_bf16 v[32:47], v[84:87], v[108:111], v[32:47]\n"
    "s_add_u32 m0, s17, 0x1c000\n"
    "ds_read_b128 v[84:87], v73 offset:17408\n"
    "s_add_u32 s11, s90, 0x10000\n"
    "buffer_load_dwordx4 v1, s[56:59], s11 offen sc1 lds\n"
    "buffer_load_dwordx4 v1, s[56:59], s11 offen offset:1024 sc1 lds\n"
    "buffer_load_dwordx4 v1, s[56:59], s11 offen offset:2048 sc1 lds\n"
    "buffer_load_dwordx4 v1, s[56:59], s11 offen offset:3072 sc1 lds\n"
    "v_exp_f32 v57, v57\n"
    "v_exp_f32 v58, v58\n"
    "v_exp_f32 v59, v59\n"
    "v_smfmac_f32_32x32x32_bf16 v[16:31], v[96:99], v[48:55], v72\n"
    "s_add_u32 m0, s17, 0x20000\n"
    "s_add_u32 s11, s90, 0x14000\n"
    "ds_read_b128 v[96:99], v73 offset:18432\n"
    "buffer_load_dwordx4 v1, s[56:59], s11 offen sc1 lds\n"
    "buffer_load_dwordx4 v1, s[56:59], s11 offen offset:1024 sc1 lds\n"
    "buffer_load_dwordx4 v1, s[56:59], s11 offen offset:2048 sc1 lds\n"
    "buffer_load_dwordx4 v1, s[56:59], s11 offen offset:3072 sc1 lds\n"
    "v_exp_f32 v60, v60\n"
    "v_exp_f32 v61, v61\n"
    "v_exp_f32 v62, v62\n"
    "v_exp_f32 v63, v63\n"
    "v_exp_f32 v32, v32\n"
    "v_exp_f32 v33, v33\n"
    "v_smfmac_f32_32x32x32_bf16 v[16:31], v[120:123], v[56:63], v72\n"
    "s_waitcnt lgkmcnt(1)\n"
    "ds_read_b128 v[120:123], v73 offset:23552\n"
    "v_exp_f32 v34, v34\n"
    "v_exp_f32 v35, v35\n"
    "v_exp_f32 v36, v36\n"
    "v_mfma_f32_32x32x16_bf16 v[48:63], v[80:83], v[104:107], 0\n"
    "ds_read_b128 v[80:83], v73 offset:20480\n"
    "v_exp_f32 v37, v37\n"
    "v_exp_f32 v38, v38\n"
    "v_exp_f32 v39, v39\n"
    "v_exp_f32 v40, v40\n"
    "v_mfma_f32_32x32x16_bf16 v[48:63], v[84:87], v[108:111], v[48:63]\n"
    "ds_read_b128 v[84:87], v73 offset:21504\n"
    "v_exp_f32 v41, v41\n"
    "v_exp_f32 v42, v42\n"
    "v_exp_f32 v43, v43\n"
    "v_smfmac_f32_32x32x32_bf16 v[16:31], v[100:103], v[32:39], v72\n"
    "ds_read_b128 v[100:103], v73 offset:22528\n"
    "v_exp_f32 v44, v44\n"
    "v_exp_f32 v45, v45\n"
    "v_exp_f32 v46, v46\n"
    "v_exp_f32 v47, v47\n"
    "s_nop 1\n"
    "v_exp_f32 v48, v48\n"
    "v_exp_f32 v49, v49\n"
    "v_smfmac_f32_32x32x32_bf16 v[16:31], v[8:11], v[40:47], v72\n"
    "s_waitcnt lgkmcnt(1)\n"
    "ds_read_b128 v[8:11], v73 offset:35840\n"
    "v_exp_f32 v50, v50\n"
    "v_exp_f32 v51, v51\n"
    "v_exp_f32 v52, v52\n"
    "v_mfma_f32_32x32x16_bf16 v[32:47], v[80:83], v[104:107], 0\n"
    "ds_read_b128 v[80:83], v73 offset:32768\n"
    "v_exp_f32 v53, v53\n"
    "v_exp_f32 v54, v54\n"
    "v_exp_f32 v55, v55\n"
    "v_exp_f32 v56, v56\n"
    "v_mfma_f32_32x32x16_bf16 v[32:47], v[84:87], v[108:111], v[32:47]\n"
    "ds_read_b128 v[84:87], v73 offset:33792\n"
    "v_exp_f32 v57, v57\n"
    "v_exp_f32 v58, v58\n"
    "v_exp_f32 v59, v59\n"
    "v_smfmac_f32_32x32x32_bf16 v[16:31], v[96:99], v[48:55], v72\n"
    "s_setprio 2\n"
    "ds_read_b128 v[96:99], v73 offset:34816\n"
    "v_exp_f32 v60, v60\n"
    "v_exp_f32 v61, v61\n"
    "v_exp_f32 v62, v62\n"
    "v_exp_f32 v63, v63\n"
    "s_nop 0\n"
    "v_exp_f32 v32, v32\n"
    "v_exp_f32 v33, v33\n"
    "v_smfmac_f32_32x32x32_bf16 v[16:31], v[88:91], v[56:63], v72\n"
    "s_waitcnt lgkmcnt(1)\n"
    "ds_read_b128 v[88:91], v73 offset:39936\n"
    "v_exp_f32 v34, v34\n"
    "v_exp_f32 v35, v35\n"
    "v_exp_f32 v36, v36\n"
    "v_mfma_f32_32x32x16_bf16 v[48:63], v[80:83], v[104:107], 0\n"
    "s_setprio 1\n"
    "ds_read_b128 v[80:83], v73 offset:36864\n"
    "v_exp_f32 v37, v37\n"
    "v_exp_f32 v38, v38\n"
    "v_exp_f32 v39, v39\n"
    "v_exp_f32 v40, v40\n"
    "v_mfma_f32_32x32x16_bf16 v[48:63], v[84:87], v[108:111], v[48:63]\n"
    "ds_read_b128 v[84:87], v73 offset:37888\n"
    "v_exp_f32 v41, v41\n"
    "v_exp_f32 v42, v42\n"
    "v_exp_f32 v43, v43\n"
    "v_smfmac_f32_32x32x32_bf16 v[16:31], v[100:103], v[32:39], v72\n"
    "s_setprio 0\n"
    "ds_read_b128 v[100:103], v73 offset:38912\n"
    "buffer_load_dwordx4 v[244:247], v6, s[28:31], s99 offen\n"
    "v_exp_f32 v44, v44\n"
    "v_exp_f32 v45, v45\n"
    "v_exp_f32 v46, v46\n"
    "v_exp_f32 v47, v47\n"
    "v_exp_f32 v48, v48\n"
    "v_exp_f32 v49, v49\n"
    "v_smfmac_f32_32x32x32_bf16 v[16:31], v[120:123], v[40:47], v72\n"
    "s_waitcnt vmcnt(1) lgkmcnt(0)\n"
    "s_barrier\n"
    "s_setprio 3\n"
    "ds_read_b128 v[120:123], v74 offset:3072\n"
    "v_exp_f32 v50, v50\n"
    "v_exp_f32 v51, v51\n"
    "v_exp_f32 v52, v52\n"
    "v_mfma_f32_32x32x16_bf16 v[32:47], v[80:83], v[104:107], 0\n"
    "s_add_u32 m0, s17, 0xc000\n"
    "ds_read_b128 v[80:83], v74 offset:0\n"
    "s_add_u32 s11, s90, 0x18000\n"
    "buffer_load_dwordx4 v1, s[56:59], s11 offen sc1 lds\n"
    "v_exp_f32 v53, v53\n"
    "v_exp_f32 v54, v54\n"
    "v_exp_f32 v55, v55\n"
    "v_exp_f32 v56, v56\n"
    "v_mfma_f32_32x32x16_bf16 v[32:47], v[84:87], v[108:111], v[32:47]\n"
    "ds_read_b128 v[84:87], v74 offset:1024\n"
    "buffer_load_dwordx4 v1, s[56:59], s11 offen offset:1024 sc1 lds\n"
    "v_exp_f32 v57, v57\n"
    "v_exp_f32 v58, v58\n"
    "v_exp_f32 v59, v59\n"
    "v_smfmac_f32_32x32x32_bf16 v[16:31], v[96:99], v[48:55], v72\n"
    "s_add_u32 s18, s97, 0x0\n"
    "ds_read_b128 v[96:99], v74 offset:2048\n"
    "buffer_load_dwordx4 v1, s[56:59], s11 offen offset:2048 sc1 lds\n"
    "s_add_u32 m0, s16, 0x0\n"
    "s_nop 0\n"
    "buffer_load_dwordx4 v2, s[48:51], s18 offen lds\n"
    "v_exp_f32 v60, v60\n"
    "v_exp_f32 v61, v61\n"
    "v_exp_f32 v62, v62\n"
    "v_exp_f32 v63, v63\n"
    "v_exp_f32 v32, v32\n"
    "v_exp_f32 v33, v33\n"
    "v_smfmac_f32_32x32x32_bf16 v[16:31], v[8:11], v[56:63], v72\n"
    "s_waitcnt lgkmcnt(1)\n"
    "s_add_u32 m0, s17, 0xc000\n"
    "ds_read_b128 v[8:11], v74 offset:7168\n"
    "buffer_load_dwordx4 v1, s[56:59], s11 offen offset:3072 sc1 lds\n"
    "v_exp_f32 v34, v34\n"
    "v_exp_f32 v35, v35\n"
    "v_exp_f32 v36, v36\n"
    "v_mfma_f32_32x32x16_bf16 v[48:63], v[80:83], v[104:107], 0\n"
    "s_add_u32 m0, s17, 0x10000\n"
    "ds_read_b128 v[80:83], v74 offset:4096\n"
    "s_add_u32 s11, s90, 0x1c000\n"
    "buffer_load_dwordx4 v1, s[56:59], s11 offen sc1 lds\n"
    "v_exp_f32 v37, v37\n"
    "v_exp_f32 v38, v38\n"
    "v_exp_f32 v39, v39\n"
    "v_exp_f32 v40, v40\n"
    "v_mfma_f32_32x32x16_bf16 v[48:63], v[84:87], v[108:111], v[48:63]\n"
    "ds_read_b128 v[84:87], v74 offset:5120\n"
    "buffer_load_dwordx4 v1, s[56:59], s11 offen offset:1024 sc1 lds\n"
    "v_exp_f32 v41, v41\n"
    "v_exp_f32 v42, v42\n"
    "v_exp_f32 v43, v43\n"
    "v_smfmac_f32_32x32x32_bf16 v[16:31], v[100:103], v[32:39], v72\n"
    "s_add_u32 m0, s16, 0x0\n"
    "ds_read_b128 v[100:103], v74 offset:6144\n"
    "buffer_load_dwordx4 v3, s[48:51], s18 offen offset:1024 lds\n"
    "v_exp_f32 v44, v44\n"
    "v_exp_f32 v45, v45\n"
    "v_exp_f32 v46, v46\n"
    "v_exp_f32 v47, v47\n"
    "v_exp_f32 v48, v48\n"
    "v_exp_f32 v49, v49\n"
    "v_smfmac_f32_32x32x32_bf16 v[16:31], v[88:91], v[40:47], v72\n"
    "s_waitcnt lgkmcnt(1)\n"
    "s_add_u32 m0, s17, 0x10000\n"
    "ds_read_b128 v[88:91], v74 offset:19456\n"
    "buffer_load_dwordx4 v1, s[56:59], s11 offen offset:2048 sc1 lds\n"
    "v_exp_f32 v50, v50\n"
    "v_exp_f32 v51, v51\n"
    "v_exp_f32 v52, v52\n"
    "v_mfma_f32_32x32x16_bf16 v[32:47], v[80:83], v[104:107], 0\n"
    "ds_read_b128 v[80:83], v74 offset:16384\n"
    "buffer_load_dwordx4 v1, s[56:59], s11 offen offset:3072 sc1 lds\n"
    "v_exp_f32 v53, v53\n"
    "v_exp_f32 v54, v54\n"
    "v_exp_f32 v55, v55\n"
    "v_exp_f32 v56, v56\n"
    "v_mfma_f32_32x32x16_bf16 v[32:47], v[84:87], v[108:111], v[32:47]\n"
    "s_add_u32 m0, s17, 0x14000\n"
    "ds_read_b128 v[84:87], v74 offset:17408\n"
    "s_add_u32 s11, s90, 0x20000\n"
    "buffer_load_dwordx4 v1, s[56:59], s11 offen sc1 lds\n"
    "v_exp_f32 v57, v57\n"
    "v_exp_f32 v58, v58\n"
    "v_exp_f32 v59, v59\n"
    "v_smfmac_f32_32x32x32_bf16 v[16:31], v[96:99], v[48:55], v72\n"
    "s_add_u32 m0, s16, 0x4000\n"
    "s_add_u32 s18, s97, 0x7000\n"
    "ds_read_b128 v[96:99], v74 offset:18432\n"
    "buffer_load_dwordx4 v2, s[48:51], s18 offen lds\n"
    "v_exp_f32 v60, v60\n"
    "v_exp_f32 v61, v61\n"
    "v_exp_f32 v62, v62\n"
    "v_exp_f32 v63, v63\n"
    "v_exp_f32 v32, v32\n"
    "v_exp_f32 v33, v33\n"
    "v_smfmac_f32_32x32x32_bf16 v[16:31], v[120:123], v[56:63], v72\n"
    "s_waitcnt lgkmcnt(1)\n"
    "s_add_u32 m0, s17, 0x14000\n"
    "ds_read_b128 v[120:123], v74 offset:23552\n"
    "buffer_load_dwordx4 v1, s[56:59], s11 offen offset:1024 sc1 lds\n"
    "v_exp_f32 v34, v34\n"
    "v_exp_f32 v35, v35\n"
    "v_exp_f32 v36, v36\n"
    "v_mfma_f32_32x32x16_bf16 v[48:63], v[80:83], v[104:107], 0\n"
    "ds_read_b128 v[80:83], v74 offset:20480\n"
    "buffer_load_dwordx4 v1, s[56:59], s11 offen offset:2048 sc1 lds\n"
    "v_exp_f32 v37, v37\n"
    "v_exp_f32 v38, v38\n"
    "v_exp_f32 v39, v39\n"
    "v_exp_f32 v40, v40\n"
    "v_mfma_f32_32x32x16_bf16 v[48:63], v[84:87], v[108:111], v[48:63]\n"
    "ds_read_b128 v[84:87], v74 offset:21504\n"
    "buffer_load_dwordx4 v1, s[56:59], s11 offen offset:3072 sc1 lds\n"
    "v_exp_f32 v41, v41\n"
    "v_exp_f32 v42, v42\n"
    "v_exp_f32 v43, v43\n"
    "v_smfmac_f32_32x32x32_bf16 v[16:31], v[100:103], v[32:39], v72\n"
    "s_add_u32 m0, s16, 0x4000\n"
    "ds_read_b128 v[100:103], v74 offset:22528\n"
    "buffer_load_dwordx4 v3, s[48:51], s18 offen offset:1024 lds\n"
    "v_exp_f32 v44, v44\n"
    "v_exp_f32 v45, v45\n"
    "v_exp_f32 v46, v46\n"
    "v_exp_f32 v47, v47\n"
    "v_exp_f32 v48, v48\n"
    "v_exp_f32 v49, v49\n"
    "v_smfmac_f32_32x32x32_bf16 v[16:31], v[8:11], v[40:47], v72\n"
    "s_waitcnt lgkmcnt(1)\n"
    "ds_read_b128 v[8:11], v74 offset:35840\n"
    "v_exp_f32 v50, v50\n"
    "v_exp_f32 v51, v51\n"
    "v_exp_f32 v52, v52\n"
    "v_mfma_f32_32x32x16_bf16 v[32:47], v[80:83], v[104:107], 0\n"
    "s_setprio 2\n"
    "ds_read_b128 v[80:83], v74 offset:32768\n"
    "v_exp_f32 v53, v53\n"
    "v_exp_f32 v54, v54\n"
    "v_exp_f32 v55, v55\n"
    "v_exp_f32 v56, v56\n"
    "v_mfma_f32_32x32x16_bf16 v[32:47], v[84:87], v[108:111], v[32:47]\n"
    "ds_read_b128 v[84:87], v74 offset:33792\n"
    "v_exp_f32 v57, v57\n"
    "v_exp_f32 v58, v58\n"
    "v_exp_f32 v59, v59\n"
    "v_smfmac_f32_32x32x32_bf16 v[16:31], v[96:99], v[48:55], v72\n"
    "s_add_u32 m0, s16, 0x8000\n"
    "s_add_u32 s18, s97, 0xe000\n"
    "ds_read_b128 v[96:99], v74 offset:34816\n"
    "buffer_load_dwordx4 v2, s[48:51], s18 offen lds\n"
    "v_exp_f32 v60, v60\n"
    "v_exp_f32 v61, v61\n"
    "v_exp_f32 v62, v62\n"
    "v_exp_f32 v63, v63\n"
    "v_exp_f32 v32, v32\n"
    "v_exp_f32 v33, v33\n"
    "v_smfmac_f32_32x32x32_bf16 v[16:31], v[88:91], v[56:63], v72\n"
    "s_setprio 1\n"
    "s_waitcnt lgkmcnt(1)\n"
    "ds_read_b128 v[88:91], v74 offset:39936\n"
    "v_exp_f32 v34, v34\n"
    "v_exp_f32 v35, v35\n"
    "v_exp_f32 v36, v36\n"
    "v_mfma_f32_32x32x16_bf16 v[48:63], v[80:83], v[104:107], 0\n"
    "ds_read_b128 v[80:83], v74 offset:36864\n"
    "buffer_load_dwordx4 v3, s[48:51], s18 offen offset:1024 lds\n"
    "v_exp_f32 v37, v37\n"
    "v_exp_f32 v38, v38\n"
    "v_exp_f32 v39, v39\n"
    "v_exp_f32 v40, v40\n"
    "v_mfma_f32_32x32x16_bf16 v[48:63], v[84:87], v[108:111], v[48:63]\n"
    "ds_read_b128 v[84:87], v74 offset:37888\n"
    "v_exp_f32 v41, v41\n"
    "v_exp_f32 v42, v42\n"
    "v_exp_f32 v43, v43\n"
    "v_smfmac_f32_32x32x32_bf16 v[16:31], v[100:103], v[32:39], v72\n"
    "s_setprio 0\n"
    "ds_read_b128 v[100:103], v74 offset:38912\n"
    "buffer_load_dwordx2 v[250:251], v75, s[56:59], 0 offen sc1\n"
    "v_exp_f32 v44, v44\n"
    "v_exp_f32 v45, v45\n"
    "v_exp_f32 v46, v46\n"
    "v_exp_f32 v47, v47\n"
    "v_exp_f32 v48, v48\n"
    "v_exp_f32 v49, v49\n"
    "v_smfmac_f32_32x32x32_bf16 v[16:31], v[120:123], v[40:47], v72\n"
    "s_waitcnt vmcnt(4) lgkmcnt(0)\n"
    "s_barrier\n"
    "s_setprio 3\n"
    "ds_read_b128 v[120:123], v73 offset:3072\n"
    "v_exp_f32 v50, v50\n"
    "v_exp_f32 v51, v51\n"
    "v_exp_f32 v52, v52\n"
    "v_mfma_f32_32x32x16_bf16 v[32:47], v[80:83], v[104:107], 0\n"
    "s_add_u32 m0, s17, 0x18000\n"
    "ds_read_b128 v[80:83], v73 offset:0\n"
    "s_add_u32 s11, s90, 0x24000\n"
    "buffer_load_dwordx4 v1, s[56:59], s11 offen sc1 lds\n"
    "v_exp_f32 v53, v53\n"
    "v_exp_f32 v54, v54\n"
    "v_exp_f32 v55, v55\n"
    "v_exp_f32 v56, v56\n"
    "v_mfma_f32_32x32x16_bf16 v[32:47], v[84:87], v[108:111], v[32:47]\n"
    "ds_read_b128 v[84:87], v73 offset:1024\n"
    "buffer_load_dwordx4 v1, s[56:59], s11 offen offset:1024 sc1 lds\n"
    "v_exp_f32 v57, v57\n"
    "v_exp_f32 v58, v58\n"
    "v_exp_f32 v59, v59\n"
    "v_smfmac_f32_32x32x32_bf16 v[16:31], v[96:99], v[48:55], v72\n"
    "s_add_u32 s18, s97, 0x15000\n"
    "ds_read_b128 v[96:99], v73 offset:2048\n"
    "buffer_load_dwordx4 v1, s[56:59], s11 offen offset:2048 sc1 lds\n"
    "s_add_u32 m0, s16, 0x24000\n"
    "s_nop 0\n"
    "buffer_load_dwordx4 v2, s[48:51], s18 offen lds\n"
    "v_exp_f32 v60, v60\n"
    "v_exp_f32 v61, v61\n"
    "v_exp_f32 v62, v62\n"
    "v_exp_f32 v63, v63\n"
    "v_exp_f32 v32, v32\n"
    "v_exp_f32 v33, v33\n"
    "v_smfmac_f32_32x32x32_bf16 v[16:31], v[8:11], v[56:63], v72\n"
    "s_waitcnt lgkmcnt(1)\n"
    "s_add_u32 m0, s17, 0x18000\n"
    "ds_read_b128 v[8:11], v73 offset:7168\n"
    "buffer_load_dwordx4 v1, s[56:59], s11 offen offset:3072 sc1 lds\n"
    "v_exp_f32 v34, v34\n"
    "v_exp_f32 v35, v35\n"
    "v_exp_f32 v36, v36\n"
    "v_mfma_f32_32x32x16_bf16 v[48:63], v[80:83], v[104:107], 0\n"
    "s_add_u32 m0, s17, 0x1c000\n"
    "ds_read_b128 v[80:83], v73 offset:4096\n"
    "s_add_u32 s11, s90, 0x28000\n"
    "buffer_load_dwordx4 v1, s[56:59], s11 offen sc1 lds\n"
    "v_exp_f32 v37, v37\n"
    "v_exp_f32 v38, v38\n"
    "v_exp_f32 v39, v39\n"
    "v_exp_f32 v40, v40\n"
    "v_mfma_f32_32x32x16_bf16 v[48:63], v[84:87], v[108:111], v[48:63]\n"
    "ds_read_b128 v[84:87], v73 offset:5120\n"
    "buffer_load_dwordx4 v1, s[56:59], s11 offen offset:1024 sc1 lds\n"
    "v_exp_f32 v41, v41\n"
    "v_exp_f32 v42, v42\n"
    "v_exp_f32 v43, v43\n"
    "v_smfmac_f32_32x32x32_bf16 v[16:31], v[100:103], v[32:39], v72\n"
    "s_add_u32 m0, s16, 0x24000\n"
    "ds_read_b128 v[100:103], v73 offset:6144\n"
    "buffer_load_dwordx4 v3, s[48:51], s18 offen offset:1024 lds\n"
    "v_exp_f32 v44, v44\n"
    "v_exp_f32 v45, v45\n"
    "v_exp_f32 v46, v46\n"
    "v_exp_f32 v47, v47\n"
    "v_exp_f32 v48, v48\n"
    "v_exp_f32 v49, v49\n"
    "v_smfmac_f32_32x32x32_bf16 v[16:31], v[88:91], v[40:47], v72\n"
    "s_waitcnt lgkmcnt(1)\n"
    "ds_read_b128 v[88:91], v73 offset:19456\n"
    "v_exp_f32 v50, v50\n"
    "v_exp_f32 v51, v51\n"
    "v_exp_f32 v52, v52\n"
    "v_mfma_f32_32x32x16_bf16 v[32:47], v[80:83], v[104:107], 0\n"
    "s_add_u32 m0, s17, 0x1c000\n"
    "ds_read_b128 v[80:83], v73 offset:16384\n"
    "buffer_load_dwordx4 v1, s[56:59], s11 offen offset:2048 sc1 lds\n"
    "v_exp_f32 v53, v53\n"
    "v_exp_f32 v54, v54\n"
    "v_exp_f32 v55, v55\n"
    "v_exp_f32 v56, v56\n"
    "v_mfma_f32_32x32x16_bf16 v[32:47], v[84:87], v[108:111], v[32:47]\n"
    "ds_read_b128 v[84:87], v73 offset:17408\n"
    "buffer_load_dwordx4 v1, s[56:59], s11 offen offset:3072 sc1 lds\n"
    "v_exp_f32 v57, v57\n"
    "v_exp_f32 v58, v58\n"
    "v_exp_f32 v59, v59\n"
    "v_smfmac_f32_32x32x32_bf16 v[16:31], v[96:99], v[48:55], v72\n"
    "s_add_u32 m0, s17, 0x20000\n"
    "s_add_u32 s11, s90, 0x2c000\n"
    "ds_read_b128 v[96:99], v73 offset:18432\n"
    "buffer_load_dwordx4 v1, s[56:59], s11 offen sc1 lds\n"
    "v_exp_f32 v60, v60\n"
    "v_exp_f32 v61, v61\n"
    "v_exp_f32 v62, v62\n"
    "v_exp_f32 v63, v63\n"
    "s_waitcnt vmcnt(11)\n"
    "v_cmp_ne_u64 vcc, s[60:61], v[250:251]\n"
    "s_nop 0\n"
    "s_cbranch_vccz Lok_13\n"
    "s_mov_b32 s10, 0x8000\n"
    "Lspin_14:\n"
    "buffer_load_dwordx2 v[250:251], v75, s[56:59], 0 offen sc1\n"
    "s_sub_u32 s10, s10, 1\n"
    "s_waitcnt vmcnt(0)\n"
    "s_cbranch_scc1 Lok_13\n"
    "v_cmp_ne_u64 vcc, s[60:61], v[250:251]\n"
    "s_nop 0\n"
    "s_cbranch_vccnz Lspin_14\n"
    "Lok_13:\n"
    "v_exp_f32 v32, v32\n"
    "v_exp_f32 v33, v33\n"
    "v_smfmac_f32_32x32x32_bf16 v[16:31], v[120:123], v[56:63], v72\n"
    "s_waitcnt lgkmcnt(1)\n"
    "s_add_u32 m0, s17, 0x20000\n"
    "ds_read_b128 v[120:123], v73 offset:23552\n"
    "s_add_u32 s11, s90, 0x2c000\n"
    "buffer_load_dwordx4 v1, s[56:59], s11 offen offset:1024 sc1 lds\n"
    "v_exp_f32 v34, v34\n"
    "v_exp_f32 v35, v35\n"
    "v_exp_f32 v36, v36\n"
    "v_mfma_f32_32x32x16_bf16 v[48:63], v[80:83], v[104:107], 0\n"
    "ds_read_b128 v[80:83], v73 offset:20480\n"
    "buffer_load_dwordx4 v1, s[56:59], s11 offen offset:2048 sc1 lds\n"
    "v_exp_f32 v37, v37\n"
    "v_exp_f32 v38, v38\n"
    "v_exp_f32 v39, v39\n"
    "v_exp_f32 v40, v40\n"
    "v_mfma_f32_32x32x16_bf16 v[48:63], v[84:87], v[108:111], v[48:63]\n"
    "ds_read_b128 v[84:87], v73 offset:21504\n"
    "buffer_load_dwordx4 v1, s[56:59], s11 offen offset:3072 sc1 lds\n"
    "v_exp_f32 v41, v41\n"
    "v_exp_f32 v42, v42\n"
    "v_exp_f32 v43, v43\n"
    "v_smfmac_f32_32x32x32_bf16 v[16:31], v[100:103], v[32:39], v72\n"
    "s_add_u32 s19, s96, 0\n"
    "ds_read_b128 v[100:103], v73 offset:22528\n"
    "buffer_load_dwordx4 v[128:131], v1, s[84:87], s19 offen offset:0 sc1\n"
    "v_exp_f32 v44, v44\n"
    "v_exp_f32 v45, v45\n"
    "v_exp_f32 v46, v46\n"
    "v_exp_f32 v47, v47\n"
    "v_exp_f32 v48, v48\n"
    "v_exp_f32 v49, v49\n"
    "v_smfmac_f32_32x32x32_bf16 v[16:31], v[8:11], v[40:47], v72\n"
    "s_waitcnt lgkmcnt(1)\n"
    "ds_read_b128 v[8:11], v73 offset:35840\n"
    "v_exp_f32 v50, v50\n"
    "v_exp_f32 v51, v51\n"
    "v_exp_f32 v52, v52\n"
    "v_mfma_f32_32x32x16_bf16 v[32:47], v[80:83], v[104:107], 0\n"
    "s_add_u32 s20, s21, 0\n"
    "ds_read_b128 v[80:83], v73 offset:32768\n"
    "buffer_load_dwordx4 v[132:135], v1, s[84:87], s20 offen offset:0 sc1\n"
    "v_exp_f32 v53, v53\n"
    "v_exp_f32 v54, v54\n"
    "v_exp_f32 v55, v55\n"
    "v_exp_f32 v56, v56\n"
    "v_mfma_f32_32x32x16_bf16 v[32:47], v[84:87], v[108:111], v[32:47]\n"
    "s_setprio 2\n"
    "ds_read_b128 v[84:87], v73 offset:33792\n"
    "buffer_load_dwordx4 v[136:139], v1, s[84:87], s19 offen offset:1024 sc1\n"
    "v_exp_f32 v57, v57\n"
    "v_exp_f32 v58, v58\n"
    "v_exp_f32 v59, v59\n"
    "v_smfmac_f32_32x32x32_bf16 v[16:31], v[96:99], v[48:55], v72\n"
    "s_setprio 1\n"
    "ds_read_b128 v[96:99], v73 offset:34816\n"
    "buffer_load_dwordx4 v[140:143], v1, s[84:87], s20 offen offset:1024 sc1\n"
    "v_exp_f32 v60, v60\n"
    "v_exp_f32 v61, v61\n"
    "v_exp_f32 v62, v62\n"
    "v_exp_f32 v63, v63\n"
    "v_exp_f32 v32, v32\n"
    "v_exp_f32 v33, v33\n"
    "v_smfmac_f32_32x32x32_bf16 v[16:31], v[88:91], v[56:63], v72\n"
    "s_waitcnt lgkmcnt(1)\n"
    "ds_read_b128 v[88:91], v73 offset:39936\n"
    "v_exp_f32 v34, v34\n"
    "v_exp_f32 v35, v35\n"
    "v_exp_f32 v36, v36\n"
    "v_mfma_f32_32x32x16_bf16 v[48:63], v[80:83], v[104:107], 0\n"
    "ds_read_b128 v[80:83], v73 offset:36864\n"
    "buffer_load_dwordx4 v[144:147], v1, s[84:87], s19 offen offset:2048 sc1\n"
    "v_exp_f32 v37, v37\n"
    "v_exp_f32 v38, v38\n"
    "v_exp_f32 v39, v39\n"
    "v_exp_f32 v40, v40\n"
    "v_mfma_f32_32x32x16_bf16 v[48:63], v[84:87], v[108:111], v[48:63]\n"
    "ds_read_b128 v[84:87], v73 offset:37888\n"
    "buffer_load_dwordx4 v[148:151], v1, s[84:87], s20 offen offset:2048 sc1\n"
    "v_exp_f32 v41, v41\n"
    "v_exp_f32 v42, v42\n"
    "v_exp_f32 v43, v43\n"
    "v_smfmac_f32_32x32x32_bf16 v[16:31], v[100:103], v[32:39], v72\n"
    "s_setprio 0\n"
    "ds_read_b128 v[100:103], v73 offset:38912\n"
    "buffer_load_dwordx4 v[152:155], v1, s[84:87], s19 offen offset:3072 sc1\n"
    "v_exp_f32 v44, v44\n"
    "v_exp_f32 v45, v45\n"
    "v_exp_f32 v46, v46\n"
    "v_exp_f32 v47, v47\n"
    "v_exp_f32 v48, v48\n"
    "v_exp_f32 v49, v49\n"
    "v_smfmac_f32_32x32x32_bf16 v[16:31], v[120:123], v[40:47], v72\n"
    "s_waitcnt vmcnt(7) lgkmcnt(0)\n"
    "s_barrier\n"
    "s_setprio 3\n"
    "ds_read_b128 v[120:123], v74 offset:3072\n"
    "v_exp_f32 v50, v50\n"
    "v_exp_f32 v51, v51\n"
    "v_exp_f32 v52, v52\n"
    "v_mfma_f32_32x32x16_bf16 v[32:47], v[80:83], v[104:107], 0\n"
    "s_add_u32 m0, s17, 0xc000\n"
    "ds_read_b128 v[80:83], v74 offset:0\n"
    "s_add_u32 s11, s90, 0x30000\n"
    "buffer_load_dwordx4 v1, s[56:59], s11 offen sc1 lds\n"
    "v_exp_f32 v53, v53\n"
    "v_exp_f32 v54, v54\n"
    "v_exp_f32 v55, v55\n"
    "v_exp_f32 v56, v56\n"
    "v_mfma_f32_32x32x16_bf16 v[32:47], v[84:87], v[108:111], v[32:47]\n"
    "ds_read_b128 v[84:87], v74 offset:1024\n"
    "buffer_load_dwordx4 v1, s[56:59], s11 offen offset:1024 sc1 lds\n"
    "v_exp_f32 v57, v57\n"
    "v_exp_f32 v58, v58\n"
    "v_exp_f32 v59, v59\n"
    "v_smfmac_f32_32x32x32_bf16 v[16:31], v[96:99], v[48:55], v72\n"
    "ds_read_b128 v[96:99], v74 offset:2048\n"
    "buffer_load_dwordx4 v1, s[56:59], s11 offen offset:2048 sc1 lds\n"
    "buffer_load_dwordx4 v[156:159], v1, s[84:87], s20 offen offset:3072 sc1\n"
    "v_exp_f32 v60, v60\n"
    "v_exp_f32 v61, v61\n"
    "v_exp_f32 v62, v62\n"
    "v_exp_f32 v63, v63\n"
    "v_exp_f32 v32, v32\n"
    "v_exp_f32 v33, v33\n"
    "v_smfmac_f32_32x32x32_bf16 v[16:31], v[8:11], v[56:63], v72\n"
    "s_waitcnt lgkmcnt(1)\n"
    "ds_read_b128 v[8:11], v74 offset:7168\n"
    "buffer_load_dwordx4 v1, s[56:59], s11 offen offset:3072 sc1 lds\n"
    "v_exp_f32 v34, v34\n"
    "v_exp_f32 v35, v35\n"
    "v_exp_f32 v36, v36\n"
    "v_mfma_f32_32x32x16_bf16 v[48:63], v[80:83], v[104:107], 0\n"
    "Lst0_7:\n"
    "s_cbranch_execz Lr0_7\n"
    "s_add_u32 m0, s17, 0x10000\n"
    "ds_read_b128 v[80:83], v74 offset:4096\n"
    "s_add_u32 s11, s90, 0x34000\n"
    "buffer_load_dwordx4 v1, s[56:59], s11 offen sc1 lds\n"
    "v_exp_f32 v37, v37\n"
    "v_exp_f32 v38, v38\n"
    "v_exp_f32 v39, v39\n"
    "v_exp_f32 v40, v40\n"
    "v_mfma_f32_32x32x16_bf16 v[48:63], v[84:87], v[108:111], v[48:63]\n"
    "ds_read_b128 v[84:87], v74 offset:5120\n"
    "buffer_load_dwordx4 v1, s[56:59], s11 offen offset:1024 sc1 lds\n"
    "v_exp_f32 v41, v41\n"
    "v_exp_f32 v42, v42\n"
    "v_exp_f32 v43, v43\n"
    "v_smfmac_f32_32x32x32_bf16 v[16:31], v[100:103], v[32:39], v72\n"
    "s_add_u32 s19, s96, 4096\n"
    "ds_read_b128 v[100:103], v74 offset:6144\n"
    "buffer_load_dwordx4 v[160:163], v1, s[84:87], s19 offen offset:0 sc1\n"
    "v_exp_f32 v44, v44\n"
    "v_exp_f32 v45, v45\n"
    "v_exp_f32 v46, v46\n"
    "v_exp_f32 v47, v47\n"
    "v_exp_f32 v48, v48\n"
    "v_exp_f32 v49, v49\n"
    "v_smfmac_f32_32x32x32_bf16 v[16:31], v[88:91], v[40:47], v72\n"
    "s_waitcnt lgkmcnt(1)\n"
    "ds_read_b128 v[88:91], v74 offset:19456\n"
    "buffer_load_dwordx4 v1, s[56:59], s11 offen offset:2048 sc1 lds\n"
    "v_exp_f32 v50, v50\n"
    "v_exp_f32 v51, v51\n"
    "v_exp_f32 v52, v52\n"
    "v_mfma_f32_32x32x16_bf16 v[32:47], v[80:83], v[104:107], 0\n"
    "ds_read_b128 v[80:83], v74 offset:16384\n"
    "buffer_load_dwordx4 v1, s[56:59], s11 offen offset:3072 sc1 lds\n"
    "v_exp_f32 v53, v53\n"
    "v_exp_f32 v54, v54\n"
    "v_exp_f32 v55, v55\n"
    "v_exp_f32 v56, v56\n"
    "v_mfma_f32_32x32x16_bf16 v[32:47], v[84:87], v[108:111], v[32:47]\n"
    "s_add_u32 m0, s17, 0x14000\n"
    "ds_read_b128 v[84:87], v74 offset:17408\n"
    "s_add_u32 s11, s90, 0x38000\n"
    "buffer_load_dwordx4 v1, s[56:59], s11 offen sc1 lds\n"
    "v_exp_f32 v57, v57\n"
    "v_exp_f32 v58, v58\n"
    "v_exp_f32 v59, v59\n"
    "v_smfmac_f32_32x32x32_bf16 v[16:31], v[96:99], v[48:55], v72\n"
    "s_add_u32 s20, s21, 4096\n"
    "ds_read_b128 v[96:99], v74 offset:18432\n"
    "buffer_load_dwordx4 v[164:167], v1, s[84:87], s20 offen offset:0 sc1\n"
    "v_exp_f32 v60, v60\n"
    "v_exp_f32 v61, v61\n"
    "v_exp_f32 v62, v62\n"
    "v_exp_f32 v63, v63\n"
    "v_exp_f32 v32, v32\n"
    "v_exp_f32 v33, v33\n"
    "v_smfmac_f32_32x32x32_bf16 v[16:31], v[120:123], v[56:63], v72\n"
    "s_waitcnt lgkmcnt(1)\n"
    "ds_read_b128 v[120:123], v74 offset:23552\n"
    "buffer_load_dwordx4 v1, s[56:59], s11 offen offset:1024 sc1 lds\n"
    "v_exp_f32 v34, v34\n"
    "v_exp_f32 v35, v35\n"
    "v_exp_f32 v36, v36\n"
    "v_mfma_f32_32x32x16_bf16 v[48:63], v[80:83], v[104:107], 0\n"
    "ds_read_b128 v[80:83], v74 offset:20480\n"
    "buffer_load_dwordx4 v1, s[56:59], s11 offen offset:2048 sc1 lds\n"
    "v_exp_f32 v37, v37\n"
    "v_exp_f32 v38, v38\n"
    "v_exp_f32 v39, v39\n"
    "v_exp_f32 v40, v40\n"
    "v_mfma_f32_32x32x16_bf16 v[48:63], v[84:87], v[108:111], v[48:63]\n"
    "ds_read_b128 v[84:87], v74 offset:21504\n"
    "buffer_load_dwordx4 v1, s[56:59], s11 offen offset:3072 sc1 lds\n"
    "v_exp_f32 v41, v41\n"
    "v_exp_f32 v42, v42\n"
    "v_exp_f32 v43, v43\n"
    "v_smfmac_f32_32x32x32_bf16 v[16:31], v[100:103], v[32:39], v72\n"
    "ds_read_b128 v[100:103], v74 offset:22528\n"
    "buffer_load_dwordx4 v[168:171], v1, s[84:87], s19 offen offset:1024 sc1\n"
    "v_exp_f32 v44, v44\n"
    "v_exp_f32 v45, v45\n"
    "v_exp_f32 v46, v46\n"
    "v_exp_f32 v47, v47\n"
    "v_exp_f32 v48, v48\n"
    "v_exp_f32 v49, v49\n"
    "v_smfmac_f32_32x32x32_bf16 v[16:31], v[8:11], v[40:47], v72\n"
    "s_waitcnt lgkmcnt(1)\n"
    "ds_read_b128 v[8:11], v74 offset:35840\n"
    "v_exp_f32 v50, v50\n"
    "v_exp_f32 v51, v51\n"
    "v_exp_f32 v52, v52\n"
    "v_mfma_f32_32x32x16_bf16 v[32:47], v[80:83], v[104:107], 0\n"
    "s_setprio 2\n"
    "ds_read_b128 v[80:83], v74 offset:32768\n"
    "buffer_load_dwordx4 v[172:175], v1, s[84:87], s20 offen offset:1024 sc1\n"
    "v_exp_f32 v53, v53\n"
    "v_exp_f32 v54, v54\n"
    "v_exp_f32 v55, v55\n"
    "v_exp_f32 v56, v56\n"
    "v_mfma_f32_32x32x16_bf16 v[32:47], v[84:87], v[108:111], v[32:47]\n"
    "ds_read_b128 v[84:87], v74 offset:33792\n"
    "buffer_load_dwordx4 v[176:179], v1, s[84:87], s19 offen offset:2048 sc1\n"
    "v_exp_f32 v57, v57\n"
    "v_exp_f32 v58, v58\n"
    "v_exp_f32 v59, v59\n"
    "v_smfmac_f32_32x32x32_bf16 v[16:31], v[96:99], v[48:55], v72\n"
    "s_setprio 1\n"
    "ds_read_b128 v[96:99], v74 offset:34816\n"
    "buffer_load_dwordx4 v[180:183], v1, s[84:87], s20 offen offset:2048 sc1\n"
    "v_exp_f32 v60, v60\n"
    "v_exp_f32 v61, v61\n"
    "v_exp_f32 v62, v62\n"
    "v_exp_f32 v63, v63\n"
    "v_exp_f32 v32, v32\n"
    "v_exp_f32 v33, v33\n"
    "v_smfmac_f32_32x32x32_bf16 v[16:31], v[88:91], v[56:63], v72\n"
    "s_waitcnt lgkmcnt(1)\n"
    "ds_read_b128 v[88:91], v74 offset:39936\n"
    "v_exp_f32 v34, v34\n"
    "v_exp_f32 v35, v35\n"
    "v_exp_f32 v36, v36\n"
    "v_mfma_f32_32x32x16_bf16 v[48:63], v[80:83], v[104:107], 0\n"
    "ds_read_b128 v[80:83], v74 offset:36864\n"
    "buffer_load_dwordx4 v[184:187], v1, s[84:87], s19 offen offset:3072 sc1\n"
    "v_exp_f32 v37, v37\n"
    "v_exp_f32 v38, v38\n"
    "v_exp_f32 v39, v39\n"
    "v_exp_f32 v40, v40\n"
    "v_mfma_f32_32x32x16_bf16 v[48:63], v[84:87], v[108:111], v[48:63]\n"
    "ds_read_b128 v[84:87], v74 offset:37888\n"
    "v_exp_f32 v41, v41\n"
    "v_exp_f32 v42, v42\n"
    "v_exp_f32 v43, v43\n"
    "v_smfmac_f32_32x32x32_bf16 v[16:31], v[100:103], v[32:39], v72\n"
    "s_setprio 0\n"
    "ds_read_b128 v[100:103], v74 offset:38912\n"
    "buffer_load_dwordx4 v[188:191], v1, s[84:87], s20 offen offset:3072 sc1\n"
    "v_exp_f32 v44, v44\n"
    "v_exp_f32 v45, v45\n"
    "v_exp_f32 v46, v46\n"
    "v_exp_f32 v47, v47\n"
    "v_exp_f32 v48, v48\n"
    "v_exp_f32 v49, v49\n"
    "v_smfmac_f32_32x32x32_bf16 v[16:31], v[120:123], v[40:47], v72\n"
    "s_waitcnt vmcnt(6) lgkmcnt(0)\n"
    "s_barrier\n"
    "s_setprio 3\n"
    "ds_read_b128 v[120:123], v73 offset:3072\n"
    "v_exp_f32 v50, v50\n"
    "v_exp_f32 v51, v51\n"
    "v_exp_f32 v52, v52\n"
    "v_mfma_f32_32x32x16_bf16 v[32:47], v[80:83], v[104:107], 0\n"
    "s_add_u32 m0, s17, 0x18000\n"
    "ds_read_b128 v[80:83], v73 offset:0\n"
    "s_add_u32 s11, s90, 0x3c000\n"
    "buffer_load_dwordx4 v1, s[56:59], s11 offen sc1 lds\n"
    "v_exp_f32 v53, v53\n"
    "v_exp_f32 v54, v54\n"
    "v_exp_f32 v55, v55\n"
    "v_exp_f32 v56, v56\n"
    "v_mfma_f32_32x32x16_bf16 v[32:47], v[84:87], v[108:111], v[32:47]\n"
    "ds_read_b128 v[84:87], v73 offset:1024\n"
    "buffer_load_dwordx4 v1, s[56:59], s11 offen offset:1024 sc1 lds\n"
    "v_exp_f32 v57, v57\n"
    "v_exp_f32 v58, v58\n"
    "v_exp_f32 v59, v59\n"
    "v_smfmac_f32_32x32x32_bf16 v[16:31], v[96:99], v[48:55], v72\n"
    "s_add_u32 m0, s16, 0x1c000\n"
    "s_add_u32 s18, s97, 0x1c000\n"
    "ds_read_b128 v[96:99], v73 offset:2048\n"
    "buffer_load_dwordx4 v2, s[48:51], s18 offen lds\n"
    "v_exp_f32 v60, v60\n"
    "v_exp_f32 v61, v61\n"
    "v_exp_f32 v62, v62\n"
    "v_exp_f32 v63, v63\n"
    "v_exp_f32 v32, v32\n"
    "v_exp_f32 v33, v33\n"
    "v_smfmac_f32_32x32x32_bf16 v[16:31], v[8:11], v[56:63], v72\n"
    "s_waitcnt lgkmcnt(1)\n"
    "s_add_u32 m0, s17, 0x18000\n"
    "ds_read_b128 v[8:11], v73 offset:7168\n"
    "buffer_load_dwordx4 v1, s[56:59], s11 offen offset:2048 sc1 lds\n"
    "v_exp_f32 v34, v34\n"
    "v_exp_f32 v35, v35\n"
    "v_exp_f32 v36, v36\n"
    "v_mfma_f32_32x32x16_bf16 v[48:63], v[80:83], v[104:107], 0\n"
    "ds_read_b128 v[80:83], v73 offset:4096\n"
    "buffer_load_dwordx4 v1, s[56:59], s11 offen offset:3072 sc1 lds\n"
    "v_exp_f32 v37, v37\n"
    "v_exp_f32 v38, v38\n"
    "v_exp_f32 v39, v39\n"
    "v_exp_f32 v40, v40\n"
    "v_mfma_f32_32x32x16_bf16 v[48:63], v[84:87], v[108:111], v[48:63]\n"
    "s_add_u32 m0, s16, 0x1c000\n"
    "ds_read_b128 v[84:87], v73 offset:5120\n"
    "buffer_load_dwordx4 v3, s[48:51], s18 offen offset:1024 lds\n"
    "v_exp_f32 v41, v41\n"
    "v_exp_f32 v42, v42\n"
    "v_exp_f32 v43, v43\n"
    "v_smfmac_f32_32x32x32_bf16 v[16:31], v[100:103], v[32:39], v72\n"
    "s_add_u32 s19, s96, 8192\n"
    "ds_read_b128 v[100:103], v73 offset:6144\n"
    "buffer_load_dwordx4 v[192:195], v1, s[84:87], s19 offen offset:0 sc1\n"
    "v_exp_f32 v44, v44\n"
    "v_exp_f32 v45, v45\n"
    "v_exp_f32 v46, v46\n"
    "v_exp_f32 v47, v47\n"
    "v_exp_f32 v48, v48\n"
    "v_exp_f32 v49, v49\n"
    "v_smfmac_f32_32x32x32_bf16 v[16:31], v[88:91], v[40:47], v72\n"
    "s_waitcnt lgkmcnt(1)\n"
    "ds_read_b128 v[88:91], v73 offset:19456\n"
    "v_exp_f32 v50, v50\n"
    "v_exp_f32 v51, v51\n"
    "v_exp_f32 v52, v52\n"
    "v_mfma_f32_32x32x16_bf16 v[32:47], v[80:83], v[104:107], 0\n"
    "s_add_u32 s20, s21, 8192\n"
    "ds_read_b128 v[80:83], v73 offset:16384\n"
    "buffer_load_dwordx4 v[196:199], v1, s[84:87], s20 offen offset:0 sc1\n"
    "v_exp_f32 v53, v53\n"
    "v_exp_f32 v54, v54\n"
    "v_exp_f32 v55, v55\n"
    "v_exp_f32 v56, v56\n"
    "v_mfma_f32_32x32x16_bf16 v[32:47], v[84:87], v[108:111], v[32:47]\n"
    "ds_read_b128 v[84:87], v73 offset:17408\n"
    "v_exp_f32 v57, v57\n"
    "v_exp_f32 v58, v58\n"
    "v_exp_f32 v59, v59\n"
    "v_smfmac_f32_32x32x32_bf16 v[16:31], v[96:99], v[48:55], v72\n"
    "ds_read_b128 v[96:99], v73 offset:18432\n"
    "buffer_load_dwordx4 v[200:203], v1, s[84:87], s19 offen offset:1024 sc1\n"
    "v_exp_f32 v60, v60\n"
    "v_exp_f32 v61, v61\n"
    "v_exp_f32 v62, v62\n"
    "v_exp_f32 v63, v63\n"
    "s_nop 0\n"
    "v_exp_f32 v32, v32\n"
    "v_exp_f32 v33, v33\n"
    "v_smfmac_f32_32x32x32_bf16 v[16:31], v[120:123], v[56:63], v72\n"
    "s_waitcnt lgkmcnt(1)\n"
    "ds_read_b128 v[120:123], v73 offset:23552\n"
    "v_exp_f32 v34, v34\n"
    "v_exp_f32 v35, v35\n"
    "v_exp_f32 v36, v36\n"
    "v_mfma_f32_32x32x16_bf16 v[48:63], v[80:83], v[104:107], 0\n"
    "s_add_u32 m0, s16, 0x20000\n"
    "ds_read_b128 v[80:83], v73 offset:20480\n"
    "s_add_u32 s18, s97, 0x23000\n"
    "buffer_load_dwordx4 v2, s[48:51], s18 offen lds\n"
    "v_exp_f32 v37, v37\n"
    "v_exp_f32 v38, v38\n"
    "v_exp_f32 v39, v39\n"
    "v_exp_f32 v40, v40\n"
    "v_mfma_f32_32x32x16_bf16 v[48:63], v[84:87], v[108:111], v[48:63]\n"
    "ds_read_b128 v[84:87], v73 offset:21504\n"
    "v_exp_f32 v41, v41\n"
    "v_exp_f32 v42, v42\n"
    "v_exp_f32 v43, v43\n"
    "v_smfmac_f32_32x32x32_bf16 v[16:31], v[100:103], v[32:39], v72\n"
    "ds_read_b128 v[100:103], v73 offset:22528\n"
    "buffer_load_dwordx4 v[204:207], v1, s[84:87], s20 offen offset:1024 sc1\n"
    "v_exp_f32 v44, v44\n"
    "v_exp_f32 v45, v45\n"
    "v_exp_f32 v46, v46\n"
    "v_exp_f32 v47, v47\n"
    "s_nop 0\n"
    "v_exp_f32 v48, v48\n"
    "v_exp_f32 v49, v49\n"
    "v_smfmac_f32_32x32x32_bf16 v[16:31], v[8:11], v[40:47], v72\n"
    "s_waitcnt lgkmcnt(1)\n"
    "ds_read_b128 v[8:11], v73 offset:35840\n"
    "v_exp_f32 v50, v50\n"
    "v_exp_f32 v51, v51\n"
    "v_exp_f32 v52, v52\n"
    "v_mfma_f32_32x32x16_bf16 v[32:47], v[80:83], v[104:107], 0\n"
    "s_setprio 2\n"
    "ds_read_b128 v[80:83], v73 offset:32768\n"
    "buffer_load_dwordx4 v3, s[48:51], s18 offen offset:1024 lds\n"
    "v_exp_f32 v53, v53\n"
    "v_exp_f32 v54, v54\n"
    "v_exp_f32 v55, v55\n"
    "v_exp_f32 v56, v56\n"
    "v_mfma_f32_32x32x16_bf16 v[32:47], v[84:87], v[108:111], v[32:47]\n"
    "ds_read_b128 v[84:87], v73 offset:33792\n"
    "buffer_load_dwordx4 v[208:211], v1, s[84:87], s19 offen offset:2048 sc1\n"
    "v_exp_f32 v57, v57\n"
    "v_exp_f32 v58, v58\n"
    "v_exp_f32 v59, v59\n"
    "v_smfmac_f32_32x32x32_bf16 v[16:31], v[96:99], v[48:55], v72\n"
    "s_setprio 1\n"
    "ds_read_b128 v[96:99], v73 offset:34816\n"
    "buffer_load_dwordx4 v[212:215], v1, s[84:87], s20 offen offset:2048 sc1\n"
    "v_exp_f32 v60, v60\n"
    "v_exp_f32 v61, v61\n"
    "v_exp_f32 v62, v62\n"
    "v_exp_f32 v63, v63\n"
    "v_exp_f32 v32, v32\n"
    "v_exp_f32 v33, v33\n"
    "v_smfmac_f32_32x32x32_bf16 v[16:31], v[88:91], v[56:63], v72\n"
    "s_waitcnt lgkmcnt(1)\n"
    "ds_read_b128 v[88:91], v73 offset:39936\n"
    "v_exp_f32 v34, v34\n"
    "v_exp_f32 v35, v35\n"
    "v_exp_f32 v36, v36\n"
    "v_mfma_f32_32x32x16_bf16 v[48:63], v[80:83], v[104:107], 0\n"
    "ds_read_b128 v[80:83], v73 offset:36864\n"
    "buffer_load_dwordx4 v[216:219], v1, s[84:87], s19 offen offset:3072 sc1\n"
    "v_exp_f32 v37, v37\n"
    "v_exp_f32 v38, v38\n"
    "v_exp_f32 v39, v39\n"
    "v_exp_f32 v40, v40\n"
    "v_mfma_f32_32x32x16_bf16 v[48:63], v[84:87], v[108:111], v[48:63]\n"
    "ds_read_b128 v[84:87], v73 offset:37888\n"
    "v_exp_f32 v41, v41\n"
    "v_exp_f32 v42, v42\n"
    "v_exp_f32 v43, v43\n"
    "v_smfmac_f32_32x32x32_bf16 v[16:31], v[100:103], v[32:39], v72\n"
    "s_setprio 0\n"
    "ds_read_b128 v[100:103], v73 offset:38912\n"
    "buffer_load_dwordx4 v[220:223], v1, s[84:87], s20 offen offset:3072 sc1\n"
    "v_exp_f32 v44, v44\n"
    "v_exp_f32 v45, v45\n"
    "v_exp_f32 v46, v46\n"
    "v_exp_f32 v47, v47\n"
    "v_exp_f32 v48, v48\n"
    "v_exp_f32 v49, v49\n"
    "v_smfmac_f32_32x32x32_bf16 v[16:31], v[120:123], v[40:47], v72\n"
    "s_waitcnt vmcnt(11) lgkmcnt(0)\n"
    "s_barrier\n"
    "s_setprio 3\n"
    "ds_read_b128 v[120:123], v74 offset:3072\n"
    "v_exp_f32 v50, v50\n"
    "v_exp_f32 v51, v51\n"
    "v_exp_f32 v52, v52\n"
    "v_mfma_f32_32x32x16_bf16 v[32:47], v[80:83], v[104:107], 0\n"
    "ds_read_b128 v[80:83], v74 offset:0\n"
    "v_exp_f32 v53, v53\n"
    "v_exp_f32 v54, v54\n"
    "v_exp_f32 v55, v55\n"
    "v_exp_f32 v56, v56\n"
    "v_mfma_f32_32x32x16_bf16 v[32:47], v[84:87], v[108:111], v[32:47]\n"
    "s_add_u32 m0, s16, 0xc000\n"
    "ds_read_b128 v[84:87], v74 offset:1024\n"
    "s_add_u32 s18, s97, 0x2a000\n"
    "buffer_load_dwordx4 v2, s[48:51], s18 offen lds\n"
    "v_exp_f32 v57, v57\n"
    "v_exp_f32 v58, v58\n"
    "v_exp_f32 v59, v59\n"
    "v_smfmac_f32_32x32x32_bf16 v[16:31], v[96:99], v[48:55], v72\n"
    "s_add_u32 s19, s96, 12288\n"
    "ds_read_b128 v[96:99], v74 offset:2048\n"
    "buffer_load_dwordx4 v[224:227], v1, s[84:87], s19 offen offset:0 sc1\n"
    "v_exp_f32 v60, v60\n"
    "v_exp_f32 v61, v61\n"
    "v_exp_f32 v62, v62\n"
    "v_exp_f32 v63, v63\n"
    "v_exp_f32 v32, v32\n"
    "v_exp_f32 v33, v33\n"
    "v_smfmac_f32_32x32x32_bf16 v[16:31], v[8:11], v[56:63], v72\n"
    "s_waitcnt lgkmcnt(1)\n"
    "ds_read_b128 v[8:11], v74 offset:7168\n"
    "v_exp_f32 v34, v34\n"
    "v_exp_f32 v35, v35\n"
    "v_exp_f32 v36, v36\n"
    "v_mfma_f32_32x32x16_bf16 v[48:63], v[80:83], v[104:107], 0\n"
    "ds_read_b128 v[80:83], v74 offset:4096\n"
    "buffer_load_dwordx4 v3, s[48:51], s18 offen offset:1024 lds\n"
    "v_exp_f32 v37, v37\n"
    "v_exp_f32 v38, v38\n"
    "v_exp_f32 v39, v39\n"
    "v_exp_f32 v40, v40\n"
    "v_mfma_f32_32x32x16_bf16 v[48:63], v[84:87], v[108:111], v[48:63]\n"
    "s_setprio 2\n"
    "ds_read_b128 v[84:87], v74 offset:5120\n"
    "v_exp_f32 v41, v41\n"
    "v_exp_f32 v42, v42\n"
    "v_exp_f32 v43, v43\n"
    "v_smfmac_f32_32x32x32_bf16 v[16:31], v[100:103], v[32:39], v72\n"
    "s_setprio 1\n"
    "s_add_u32 s20, s21, 12288\n"
    "ds_read_b128 v[100:103], v74 offset:6144\n"
    "buffer_load_dwordx4 v[228:231], v1, s[84:87], s20 offen offset:0 sc1\n"
    "v_exp_f32 v44, v44\n"
    "v_exp_f32 v45, v45\n"
    "v_exp_f32 v46, v46\n"
    "v_exp_f32 v47, v47\n"
    "v_exp_f32 v48, v48\n"
    "v_exp_f32 v49, v49\n"
    "v_smfmac_f32_32x32x32_bf16 v[16:31], v[88:91], v[40:47], v72\n"
    "s_setprio 0\n"
    "s_waitcnt lgkmcnt(0)\n"
    "s_barrier\n"
    "s_setprio 1\n"
    "v_exp_f32 v50, v50\n"
    "v_exp_f32 v51, v51\n"
    "v_exp_f32 v52, v52\n"
    "v_mfma_f32_32x32x16_bf16 v[32:47], v[80:83], v[104:107], 0\n"
    "v_exp_f32 v53, v53\n"
    "v_exp_f32 v54, v54\n"
    "v_exp_f32 v55, v55\n"
    "v_exp_f32 v56, v56\n"
    "v_mfma_f32_32x32x16_bf16 v[32:47], v[84:87], v[108:111], v[32:47]\n"
    "s_add_u32 m0, s16, 0x10000\n"
    "s_add_u32 s18, s97, 0x31000\n"
    "buffer_load_dwordx4 v2, s[48:51], s18 offen lds\n"
    "v_exp_f32 v57, v57\n"
    "v_exp_f32 v58, v58\n"
    "v_exp_f32 v59, v59\n"
    "v_smfmac_f32_32x32x32_bf16 v[16:31], v[96:99], v[48:55], v72\n"
    "buffer_load_dwordx4 v[232:235], v1, s[84:87], s19 offen offset:1024 sc1\n"
    "v_exp_f32 v60, v60\n"
    "v_exp_f32 v61, v61\n"
    "v_exp_f32 v62, v62\n"
    "v_exp_f32 v63, v63\n"
    "v_exp_f32 v32, v32\n"
    "v_exp_f32 v33, v33\n"
    "v_smfmac_f32_32x32x32_bf16 v[16:31], v[120:123], v[56:63], v72\n"
    "buffer_load_dwordx4 v3, s[48:51], s18 offen offset:1024 lds\n"
    "v_exp_f32 v34, v34\n"
    "v_exp_f32 v35, v35\n"
    "v_exp_f32 v36, v36\n"
    "v_exp_f32 v37, v37\n"
    "v_exp_f32 v38, v38\n"
    "v_exp_f32 v39, v39\n"
    "v_exp_f32 v40, v40\n"
    "v_exp_f32 v41, v41\n"
    "v_exp_f32 v42, v42\n"
    "v_exp_f32 v43, v43\n"
    "v_smfmac_f32_32x32x32_bf16 v[16:31], v[100:103], v[32:39], v72\n"
    "buffer_load_dwordx4 v[236:239], v1, s[84:87], s20 offen offset:1024 sc1\n"
    "v_exp_f32 v44, v44\n"
    "v_exp_f32 v45, v45\n"
    "v_exp_f32 v46, v46\n"
    "v_exp_f32 v47, v47\n"
    "s_nop 1\n"
    "v_smfmac_f32_32x32x32_bf16 v[16:31], v[8:11], v[40:47], v72\n"
    "Lc_h0:\n"
    "s_nop 11\n"
    "ds_write_b128 v76, v[16:19] offset:0\n"
    "ds_write_b128 v76, v[20:23] offset:1024\n"
    "ds_write_b128 v76, v[24:27] offset:2048\n"
    "ds_write_b128 v76, v[28:31] offset:3072\n"
    "s_waitcnt vmcnt(18) lgkmcnt(0)\n"
    "s_barrier\n"
    "s_cmp_ge_u32 s27, 28\n"
    "s_cbranch_scc1 Lidle_h0\n"
    "ds_read_b64_tr_b16 v[48:49], v124 offset:0\n"
    "ds_read_b64_tr_b16 v[50:51], v124 offset:2048\n"
    "ds_read_b64_tr_b16 v[52:53], v125 offset:0\n"
    "ds_read_b64_tr_b16 v[54:55], v125 offset:2048\n"
    "ds_read_b64_tr_b16 v[56:57], v126 offset:0\n"
    "ds_read_b64_tr_b16 v[58:59], v126 offset:2048\n"
    "ds_read_b64_tr_b16 v[60:61], v127 offset:0\n"
    "ds_read_b64_tr_b16 v[62:63], v127 offset:2048\n"
    "s_setprio 3\n"
    "s_waitcnt lgkmcnt(6)\n"
    "v_mfma_f32_16x16x32_bf16 v[16:19], v[48:51], v[128:131], 0\n"
    "s_add_u32 m0, s16, 0x14000\n"
    "ds_read_b64_tr_b16 v[64:65], v124 offset:16384\n"
    "s_add_u32 s18, s97, 0x38000\n"
    "buffer_load_dwordx4 v2, s[48:51], s18 offen lds\n"
    "v_mfma_f32_16x16x32_bf16 v[20:23], v[48:51], v[132:135], 0\n"
    "s_waitcnt lgkmcnt(5)\n"
    "ds_read_b64_tr_b16 v[66:67], v124 offset:18432\n"
    "v_mfma_f32_16x16x32_bf16 v[28:31], v[52:55], v[132:135], 0\n"
    "ds_read_b64_tr_b16 v[68:69], v125 offset:16384\n"
    "buffer_load_dwordx4 v3, s[48:51], s18 offen offset:1024 lds\n"
    "v_mfma_f32_16x16x32_bf16 v[24:27], v[52:55], v[128:131], 0\n"
    "s_waitcnt lgkmcnt(5)\n"
    "ds_read_b64_tr_b16 v[70:71], v125 offset:18432\n"
    "v_mfma_f32_16x16x32_bf16 v[32:35], v[56:59], v[128:131], 0\n"
    "s_add_u32 m0, s16, 0x16000\n"
    "ds_read_b64_tr_b16 v[72:73], v126 offset:16384\n"
    "s_add_u32 s18, s97, 0xfc000\n"
    "buffer_load_dwordx4 v2, s[48:51], s18 offen lds\n"
    "v_mfma_f32_16x16x32_bf16 v[36:39], v[56:59], v[132:135], 0\n"
    "s_waitcnt lgkmcnt(5)\n"
    "ds_read_b64_tr_b16 v[74:75], v126 offset:18432\n"
    "v_mfma_f32_16x16x32_bf16 v[44:47], v[60:63], v[132:135], 0\n"
    "ds_read_b64_tr_b16 v[76:77], v127 offset:16384\n"
    "buffer_load_dwordx4 v3, s[48:51], s18 offen offset:1024 lds\n"
    "v_mfma_f32_16x16x32_bf16 v[40:43], v[60:63], v[128:131], 0\n"
    "s_waitcnt lgkmcnt(5)\n"
    "ds_read_b64_tr_b16 v[78:79], v127 offset:18432\n"
    "v_mfma_f32_16x16x32_bf16 v[16:19], v[64:67], v[136:139], v[16:19]\n"
    "ds_read_b64_tr_b16 v[48:49], v124 offset:32768\n"
    "v_mfma_f32_16x16x32_bf16 v[20:23], v[64:67], v[140:143], v[20:23]\n"
    "s_waitcnt lgkmcnt(5)\n"
    "ds_read_b64_tr_b16 v[50:51], v124 offset:34816\n"
    "v_mfma_f32_16x16x32_bf16 v[28:31], v[68:71], v[140:143], v[28:31]\n"
    "ds_read_b64_tr_b16 v[52:53], v125 offset:32768\n"
    "v_mfma_f32_16x16x32_bf16 v[24:27], v[68:71], v[136:139], v[24:27]\n"
    "s_waitcnt lgkmcnt(5)\n"
    "ds_read_b64_tr_b16 v[54:55], v125 offset:34816\n"
    "v_mfma_f32_16x16x32_bf16 v[32:35], v[72:75], v[136:139], v[32:35]\n"
    "ds_read_b64_tr_b16 v[56:57], v126 offset:32768\n"
    "v_mfma_f32_16x16x32_bf16 v[36:39], v[72:75], v[140:143], v[36:39]\n"
    "s_waitcnt lgkmcnt(5)\n"
    "ds_read_b64_tr_b16 v[58:59], v126 offset:34816\n"
    "v_mfma_f32_16x16x32_bf16 v[44:47], v[76:79], v[140:143], v[44:47]\n"
    "ds_read_b64_tr_b16 v[60:61], v127 offset:32768\n"
    "v_mfma_f32_16x16x32_bf16 v[40:43], v[76:79], v[136:139], v[40:43]\n"
    "s_waitcnt lgkmcnt(5)\n"
    "ds_read_b64_tr_b16 v[62:63], v127 offset:34816\n"
    "v_mfma_f32_16x16x32_bf16 v[16:19], v[48:51], v[144:147], v[16:19]\n"
    "v_add_u32 v124, 0x18000, v124\n"
    "ds_read_b64_tr_b16 v[64:65], v124 offset:49152\n"
    "v_mfma_f32_16x16x32_bf16 v[20:23], v[48:51], v[148:151], v[20:23]\n"
    "s_waitcnt lgkmcnt(5)\n"
    "ds_read_b64_tr_b16 v[66:67], v124 offset:51200\n"
    "v_mfma_f32_16x16x32_bf16 v[28:31], v[52:55], v[148:151], v[28:31]\n"
    "v_add_u32 v125, 0x18000, v125\n"
    "ds_read_b64_tr_b16 v[68:69], v125 offset:49152\n"
    "v_mfma_f32_16x16x32_bf16 v[24:27], v[52:55], v[144:147], v[24:27]\n"
    "s_waitcnt lgkmcnt(5)\n"
    "ds_read_b64_tr_b16 v[70:71], v125 offset:51200\n"
    "v_mfma_f32_16x16x32_bf16 v[32:35], v[56:59], v[144:147], v[32:35]\n"
    "v_add_u32 v126, 0x18000, v126\n"
    "ds_read_b64_tr_b16 v[72:73], v126 offset:49152\n"
    "v_mfma_f32_16x16x32_bf16 v[36:39], v[56:59], v[148:151], v[36:39]\n"
    "s_setprio 2\n"
    "ds_read_b64_tr_b16 v[74:75], v126 offset:51200\n"
    "s_waitcnt lgkmcnt(6)\n"
    "v_mfma_f32_16x16x32_bf16 v[44:47], v[60:63], v[148:151], v[44:47]\n"
    "v_add_u32 v127, 0x18000, v127\n"
    "ds_read_b64_tr_b16 v[76:77], v127 offset:49152\n"
    "v_mfma_f32_16x16x32_bf16 v[40:43], v[60:63], v[144:147], v[40:43]\n"
    "s_waitcnt lgkmcnt(5)\n"
    "ds_read_b64_tr_b16 v[78:79], v127 offset:51200\n"
    "v_mfma_f32_16x16x32_bf16 v[16:19], v[64:67], v[152:155], v[16:19]\n"
    "ds_read_b64_tr_b16 v[48:49], v124 offset:16384\n"
    "v_mfma_f32_16x16x32_bf16 v[20:23], v[64:67], v[156:159], v[20:23]\n"
    "s_waitcnt lgkmcnt(5)\n"
    "ds_read_b64_tr_b16 v[50:51], v124 offset:18432\n"
    "v_mfma_f32_16x16x32_bf16 v[28:31], v[68:71], v[156:159], v[28:31]\n"
    "s_setprio 1\n"
    "ds_read_b64_tr_b16 v[52:53], v125 offset:16384\n"
    "v_mfma_f32_16x16x32_bf16 v[24:27], v[68:71], v[152:155], v[24:27]\n"
    "s_waitcnt lgkmcnt(5)\n"
    "ds_read_b64_tr_b16 v[54:55], v125 offset:18432\n"
    "v_mfma_f32_16x16x32_bf16 v[32:35], v[72:75], v[152:155], v[32:35]\n"
    "ds_read_b64_tr_b16 v[56:57], v126 offset:16384\n"
    "v_mfma_f32_16x16x32_bf16 v[36:39], v[72:75], v[156:159], v[36:39]\n"
    "s_waitcnt lgkmcnt(5)\n"
    "ds_read_b64_tr_b16 v[58:59], v126 offset:18432\n"
    "v_mfma_f32_16x16x32_bf16 v[44:47], v[76:79], v[156:159], v[44:47]\n"
    "s_setprio 0\n"
    "ds_read_b64_tr_b16 v[60:61], v127 offset:16384\n"
    "v_mfma_f32_16x16x32_bf16 v[40:43], v[76:79], v[152:155], v[40:43]\n"
    "ds_read_b64_tr_b16 v[62:63], v127 offset:18432\n"
    "s_barrier\n"
    "s_add_u32 m0, s16, 0x18000\n"
    "s_add_u32 s18, s97, 0x3f000\n"
    "buffer_load_dwordx4 v2, s[48:51], s18 offen lds\n"
    "buffer_load_dwordx4 v3, s[48:51], s18 offen offset:1024 lds\n"
    "s_add_u32 m0, s16, 0x1a000\n"
    "s_add_u32 s18, s97, 0x103000\n"
    "buffer_load_dwordx4 v2, s[48:51], s18 offen lds\n"
    "buffer_load_dwordx4 v3, s[48:51], s18 offen offset:1024 lds\n"
    "s_add_u32 m0, s16, 0x0\n"
    "s_add_u32 s18, s97, 0x46000\n"
    "buffer_load_dwordx4 v2, s[48:51], s18 offen lds\n"
    "buffer_load_dwordx4 v3, s[48:51], s18 offen offset:1024 lds\n"
    "s_add_u32 m0, s16, 0x4000\n"
    "s_add_u32 s18, s97, 0x4d000\n"
    "buffer_load_dwordx4 v2, s[48:51], s18 offen lds\n"
    "buffer_load_dwordx4 v3, s[48:51], s18 offen offset:1024 lds\n"
    "s_waitcnt vmcnt(8)\n"
    "s_barrier\n"
    "s_setprio 3\n"
    "s_waitcnt lgkmcnt(6)\n"
    "v_mfma_f32_16x16x32_bf16 v[16:19], v[48:51], v[160:163], v[16:19]\n"
    "s_add_u32 m0, s16, 0x2000\n"
    "ds_read_b64_tr_b16 v[64:65], v124 offset:32768\n"
    "s_add_u32 s18, s97, 0x10a000\n"
    "buffer_load_dwordx4 v2, s[48:51], s18 offen lds\n"
    "v_mfma_f32_16x16x32_bf16 v[20:23], v[48:51], v[164:167], v[20:23]\n"
    "s_waitcnt lgkmcnt(5)\n"
    "ds_read_b64_tr_b16 v[66:67], v124 offset:34816\n"
    "v_mfma_f32_16x16x32_bf16 v[28:31], v[52:55], v[164:167], v[28:31]\n"
    "ds_read_b64_tr_b16 v[68:69], v125 offset:32768\n"
    "buffer_load_dwordx4 v3, s[48:51], s18 offen offset:1024 lds\n"
    "v_mfma_f32_16x16x32_bf16 v[24:27], v[52:55], v[160:163], v[24:27]\n"
    "s_waitcnt lgkmcnt(5)\n"
    "ds_read_b64_tr_b16 v[70:71], v125 offset:34816\n"
    "v_mfma_f32_16x16x32_bf16 v[32:35], v[56:59], v[160:163], v[32:35]\n"
    "s_add_u32 m0, s16, 0x6000\n"
    "ds_read_b64_tr_b16 v[72:73], v126 offset:32768\n"
    "s_add_u32 s18, s97, 0x111000\n"
    "buffer_load_dwordx4 v2, s[48:51], s18 offen lds\n"
    "v_mfma_f32_16x16x32_bf16 v[36:39], v[56:59], v[164:167], v[36:39]\n"
    "s_waitcnt lgkmcnt(5)\n"
    "ds_read_b64_tr_b16 v[74:75], v126 offset:34816\n"
    "v_mfma_f32_16x16x32_bf16 v[44:47], v[60:63], v[164:167], v[44:47]\n"
    "ds_read_b64_tr_b16 v[76:77], v127 offset:32768\n"
    "buffer_load_dwordx4 v3, s[48:51], s18 offen offset:1024 lds\n"
    "v_mfma_f32_16x16x32_bf16 v[40:43], v[60:63], v[160:163], v[40:43]\n"
    "s_waitcnt lgkmcnt(5)\n"
    "ds_read_b64_tr_b16 v[78:79], v127 offset:34816\n"
    "v_mfma_f32_16x16x32_bf16 v[16:19], v[64:67], v[168:171], v[16:19]\n"
    "s_add_u32 m0, s16, 0x8000\n"
    "v_add_u32 v124, 0xffff4000, v124\n"
    "ds_read_b64_tr_b16 v[48:49], v124 offset:0\n"
    "s_add_u32 s18, s97, 0x54000\n"
    "buffer_load_dwordx4 v2, s[48:51], s18 offen lds\n"
    "v_mfma_f32_16x16x32_bf16 v[20:23], v[64:67], v[172:175], v[20:23]\n"
    "s_waitcnt lgkmcnt(5)\n"
    "ds_read_b64_tr_b16 v[50:51], v124 offset:2048\n"
    "v_mfma_f32_16x16x32_bf16 v[28:31], v[68:71], v[172:175], v[28:31]\n"
    "v_add_u32 v125, 0xffff4000, v125\n"
    "ds_read_b64_tr_b16 v[52:53], v125 offset:0\n"
    "buffer_load_dwordx4 v3, s[48:51], s18 offen offset:1024 lds\n"
    "v_lshrrev_b32 v13, 5, v0\n"
    "v_mul_u32_u24 v14, 7168, v13\n"
    "v_mfma_f32_16x16x32_bf16 v[24:27], v[68:71], v[168:171], v[24:27]\n"
    "s_waitcnt lgkmcnt(5)\n"
    "ds_read_b64_tr_b16 v[54:55], v125 offset:2048\n"
    "v_add_u32 v5, v14, v4\n"
    "v_mfma_f32_16x16x32_bf16 v[32:35], v[72:75], v[168:171], v[32:35]\n"
    "s_add_u32 m0, s16, 0xa000\n"
    "v_add_u32 v126, 0xffff4000, v126\n"
    "ds_read_b64_tr_b16 v[56:57], v126 offset:0\n"
    "s_add_u32 s18, s97, 0x118000\n"
    "buffer_load_dwordx4 v2, s[48:51], s18 offen lds\n"
    "v_mul_u32_u24 v14, 21504, v13\n"
    "v_mfma_f32_16x16x32_bf16 v[36:39], v[72:75], v[172:175], v[36:39]\n"
    "s_bfe_u32 s65, s25, 0x10001\n"
    "ds_read_b64_tr_b16 v[58:59], v126 offset:2048\n"
    "s_waitcnt lgkmcnt(6)\n"
    "v_mfma_f32_16x16x32_bf16 v[44:47], v[76:79], v[172:175], v[44:47]\n"
    "s_mul_i32 s66, s65, 100352\n"
    "v_add_u32 v127, 0xffff4000, v127\n"
    "ds_read_b64_tr_b16 v[60:61], v127 offset:0\n"
    "s_and_b32 s67, s25, 1\n"
    "buffer_load_dwordx4 v3, s[48:51], s18 offen offset:1024 lds\n"
    "v_mfma_f32_16x16x32_bf16 v[40:43], v[76:79], v[168:171], v[40:43]\n"
    "s_mul_i32 s67, s67, 93184\n"
    "ds_read_b64_tr_b16 v[62:63], v127 offset:2048\n"
    "s_waitcnt lgkmcnt(6)\n"
    "v_mfma_f32_16x16x32_bf16 v[16:19], v[48:51], v[176:179], v[16:19]\n"
    "s_add_u32 s66, s66, s67\n"
    "ds_read_b64_tr_b16 v[64:65], v124 offset:16384\n"
    "s_add_u32 m0, s16, 0x24000\n"
    "s_add_u32 s18, s97, 0x5b000\n"
    "buffer_load_dwordx4 v2, s[48:51], s18 offen lds\n"
    "v_mfma_f32_16x16x32_bf16 v[20:23], v[48:51], v[180:183], v[20:23]\n"
    "s_waitcnt lgkmcnt(5)\n"
    "ds_read_b64_tr_b16 v[66:67], v124 offset:18432\n"
    "v_add3_u32 v12, v14, s66, v4\n"
    "v_mfma_f32_16x16x32_bf16 v[28:31], v[52:55], v[180:183], v[28:31]\n"
    "s_lshl_b32 s65, s65, 10\n"
    "ds_read_b64_tr_b16 v[68:69], v125 offset:16384\n"
    "s_sub_u32 s7, s98, s65\n"
    "buffer_load_dwordx4 v3, s[48:51], s18 offen offset:1024 lds\n"
    "v_mfma_f32_16x16x32_bf16 v[24:27], v[52:55], v[176:179], v[24:27]\n"
    "s_add_u32 s7, s7, 12288\n"
    "ds_read_b64_tr_b16 v[70:71], v125 offset:18432\n"
    "s_waitcnt lgkmcnt(6)\n"
    "v_mfma_f32_16x16x32_bf16 v[32:35], v[56:59], v[176:179], v[32:35]\n"
    "s_add_u32 m0, s16, 0x26000\n"
    "ds_read_b64_tr_b16 v[72:73], v126 offset:16384\n"
    "s_add_u32 s18, s97, 0x11f000\n"
    "buffer_load_dwordx4 v2, s[48:51], s18 offen lds\n"
    "v_and_b32 v106, 15, v0\n"
    "v_mfma_f32_16x16x32_bf16 v[36:39], v[56:59], v[180:183], v[36:39]\n"
    "s_setprio 2\n"
    "ds_read_b64_tr_b16 v[74:75], v126 offset:18432\n"
    "v_and_b32 v107, 7, v0\n"
    "s_waitcnt lgkmcnt(6)\n"
    "v_mfma_f32_16x16x32_bf16 v[44:47], v[60:63], v[180:183], v[44:47]\n"
    "ds_read_b64_tr_b16 v[76:77], v127 offset:16384\n"
    "Lst0_8:\n"
    "s_cbranch_execz Lr0_8\n"
    "buffer_load_dwordx4 v3, s[48:51], s18 offen offset:1024 lds\n"
    "v_lshrrev_b32 v108, 5, v0\n"
    "v_xor_b32 v108, v107, v108\n"
    "v_mfma_f32_16x16x32_bf16 v[40:43], v[60:63], v[176:179], v[40:43]\n"
    "s_waitcnt lgkmcnt(5)\n"
    "ds_read_b64_tr_b16 v[78:79], v127 offset:18432\n"
    "v_lshlrev_b32 v108, 4, v108\n"
    "v_mfma_f32_16x16x32_bf16 v[16:19], v[64:67], v[184:187], v[16:19]\n"
    "ds_read_b64_tr_b16 v[48:49], v124 offset:32768\n"
    "v_lshl_add_u32 v108, v106, 7, v108\n"
    "v_mfma_f32_16x16x32_bf16 v[20:23], v[64:67], v[188:191], v[20:23]\n"
    "s_waitcnt lgkmcnt(5)\n"
    "ds_read_b64_tr_b16 v[50:51], v124 offset:34816\n"
    "v_bfe_u32 v106, v0, 4, 1\n"
    "v_mfma_f32_16x16x32_bf16 v[28:31], v[68:71], v[188:191], v[28:31]\n"
    "s_setprio 1\n"
    "ds_read_b64_tr_b16 v[52:53], v125 offset:32768\n"
    "s_lshl_b32 s65, s25, 12\n"
    "v_lshl_add_u32 v108, v106, 3, v108\n"
    "v_mfma_f32_16x16x32_bf16 v[24:27], v[68:71], v[184:187], v[24:27]\n"
    "s_waitcnt lgkmcnt(5)\n"
    "ds_read_b64_tr_b16 v[54:55], v125 offset:34816\n"
    "v_add_u32 v100, s65, v108\n"
    "v_mfma_f32_16x16x32_bf16 v[32:35], v[72:75], v[184:187], v[32:35]\n"
    "ds_read_b64_tr_b16 v[56:57], v126 offset:32768\n"
    "v_xor_b32 v101, 32, v100\n"
    "v_mfma_f32_16x16x32_bf16 v[36:39], v[72:75], v[188:191], v[36:39]\n"
    "s_waitcnt lgkmcnt(5)\n"
    "ds_read_b64_tr_b16 v[58:59], v126 offset:34816\n"
    "v_xor_b32 v102, 64, v100\n"
    "v_mfma_f32_16x16x32_bf16 v[44:47], v[76:79], v[188:191], v[44:47]\n"
    "s_setprio 0\n"
    "ds_read_b64_tr_b16 v[60:61], v127 offset:32768\n"
    "v_xor_b32 v103, 0x60, v100\n"
    "v_add_u32 v96, 0x14000, v100\n"
    "v_mfma_f32_16x16x32_bf16 v[40:43], v[76:79], v[184:187], v[40:43]\n"
    "ds_read_b64_tr_b16 v[62:63], v127 offset:34816\n"
    "v_add_u32 v97, 0x14000, v101\n"
    "s_waitcnt vmcnt(10)\n"
    "s_barrier\n"
    "s_setprio 3\n"
    "s_waitcnt lgkmcnt(6)\n"
    "v_mfma_f32_16x16x32_bf16 v[16:19], v[48:51], v[192:195], v[16:19]\n"
    "s_add_u32 m0, s16, 0x1c000\n"
    "ds_read_b64_tr_b16 v[64:65], v124 offset:49152\n"
    "s_add_u32 s18, s97, 0x0\n"
    "buffer_load_dwordx4 v4, s[36:39], s18 offen lds\n"
    "v_add_u32 v98, 0x14000, v102\n"
    "v_mfma_f32_16x16x32_bf16 v[20:23], v[48:51], v[196:199], v[20:23]\n"
    "s_waitcnt lgkmcnt(5)\n"
    "ds_read_b64_tr_b16 v[66:67], v124 offset:51200\n"
    "v_add_u32 v99, 0x14000, v103\n"
    "v_mfma_f32_16x16x32_bf16 v[28:31], v[52:55], v[196:199], v[28:31]\n"
    "s_add_u32 s18, s97, 0x3400\n"
    "ds_read_b64_tr_b16 v[68:69], v125 offset:49152\n"
    "buffer_load_dwordx4 v5, s[36:39], s18 offen offset:1024 lds\n"
    "v_lshrrev_b32 v106, 3, v0\n"
    "v_mfma_f32_16x16x32_bf16 v[24:27], v[52:55], v[192:195], v[24:27]\n"
    "s_waitcnt lgkmcnt(5)\n"
    "ds_read_b64_tr_b16 v[70:71], v125 offset:51200\n"
    "v_xor_b32 v107, v107, v106\n"
    "v_lshlrev_b32 v107, 4, v107\n"
    "v_mfma_f32_16x16x32_bf16 v[32:35], v[56:59], v[192:195], v[32:35]\n"
    "s_add_u32 m0, s16, 0x1e000\n"
    "ds_read_b64_tr_b16 v[72:73], v126 offset:49152\n"
    "s_add_u32 s18, s97, 0x31000\n"
    "buffer_load_dwordx4 v4, s[36:39], s18 offen lds\n"
    "v_lshl_add_u32 v107, v106, 7, v107\n"
    "v_mfma_f32_16x16x32_bf16 v[36:39], v[56:59], v[196:199], v[36:39]\n"
    "s_waitcnt lgkmcnt(5)\n"
    "ds_read_b64_tr_b16 v[74:75], v126 offset:51200\n"
    "v_add_u32 v105, s62, v107\n"
    "v_mfma_f32_16x16x32_bf16 v[44:47], v[60:63], v[196:199], v[44:47]\n"
    "s_add_u32 s18, s97, 0x34400\n"
    "ds_read_b64_tr_b16 v[76:77], v127 offset:49152\n"
    "buffer_load_dwordx4 v5, s[36:39], s18 offen offset:1024 lds\n"
    "v_add_u32 v104, 0x14000, v105\n"
    "v_mfma_f32_16x16x32_bf16 v[40:43], v[60:63], v[192:195], v[40:43]\n"
    "s_waitcnt lgkmcnt(5)\n"
    "ds_read_b64_tr_b16 v[78:79], v127 offset:51200\n"
    "v_and_b32 v106, 1, v0\n"
    "v_lshlrev_b32 v106, 4, v106\n"
    "v_mfma_f32_16x16x32_bf16 v[16:19], v[64:67], v[200:203], v[16:19]\n"
    "s_add_u32 m0, s16, 0x20000\n"
    "v_add_u32 v124, 0xffff4000, v124\n"
    "ds_read_b64_tr_b16 v[48:49], v124 offset:0\n"
    "s_add_u32 s18, s97, 0x7000\n"
    "buffer_load_dwordx4 v5, s[36:39], s18 offen lds\n"
    "v_mov_b32 v107, 0x3f80\n"
    "v_mfma_f32_16x16x32_bf16 v[20:23], v[64:67], v[204:207], v[20:23]\n"
    "s_waitcnt lgkmcnt(5)\n"
    "ds_read_b64_tr_b16 v[50:51], v124 offset:2048\n"
    "v_lshlrev_b32 v106, v106, v107\n"
    "v_mfma_f32_16x16x32_bf16 v[28:31], v[68:71], v[204:207], v[28:31]\n"
    "s_setprio 2\n"
    "v_add_u32 v125, 0xffff4000, v125\n"
    "ds_read_b64_tr_b16 v[52:53], v125 offset:0\n"
    "s_mov_b32 s8, 0x00300003\n"
    "s_add_u32 s18, s97, 0xc000\n"
    "buffer_load_dwordx4 v4, s[36:39], s18 offen offset:1024 lds\n"
    "v_mfma_f32_16x16x32_bf16 v[24:27], v[68:71], v[200:203], v[24:27]\n"
    "s_mov_b32 s9, 0x30000300\n"
    "ds_read_b64_tr_b16 v[54:55], v125 offset:2048\n"
    "v_cndmask_b32 v108, 0, v106, s[8:9]\n"
    "s_waitcnt lgkmcnt(6)\n"
    "v_mfma_f32_16x16x32_bf16 v[32:35], v[72:75], v[200:203], v[32:35]\n"
    "s_lshl_b64 s[8:9], s[8:9], 2\n"
    "v_add_u32 v126, 0xffff4000, v126\n"
    "ds_read_b64_tr_b16 v[56:57], v126 offset:0\n"
    "buffer_load_dwordx2 v[250:251], v7, s[56:59], 0 offen sc1\n"
    "s_add_u32 m0, s16, 0x22000\n"
    "s_add_u32 s18, s97, 0x38000\n"
    "buffer_load_dwordx4 v5, s[36:39], s18 offen lds\n"
    "v_mfma_f32_16x16x32_bf16 v[36:39], v[72:75], v[204:207], v[36:39]\n"
    "s_setprio 1\n"
    "ds_read_b64_tr_b16 v[58:59], v126 offset:2048\n"
    "v_cndmask_b32 v109, 0, v106, s[8:9]\n"
    "s_waitcnt lgkmcnt(6)\n"
    "v_mfma_f32_16x16x32_bf16 v[44:47], v[76:79], v[204:207], v[44:47]\n"
    "s_add_u32 s18, s97, 0x3d000\n"
    "v_add_u32 v127, 0xffff4000, v127\n"
    "ds_read_b64_tr_b16 v[60:61], v127 offset:0\n"
    "buffer_load_dwordx4 v4, s[36:39], s18 offen offset:1024 lds\n"
    "v_mov_b32 v106, 0\n"
    "v_mfma_f32_16x16x32_bf16 v[40:43], v[76:79], v[200:203], v[40:43]\n"
    "s_setprio 0\n"
    "ds_read_b64_tr_b16 v[62:63], v127 offset:2048\n"
    "v_mov_b32 v107, 0\n"
    "v_mov_b32 v110, 0\n"
    "s_waitcnt vmcnt(9)\n"
    "s_barrier\n"
    "s_setprio 3\n"
    "s_waitcnt lgkmcnt(6)\n"
    "v_mfma_f32_16x16x32_bf16 v[16:19], v[48:51], v[208:211], v[16:19]\n"
    "s_add_u32 m0, s16, 0xc000\n"
    "ds_read_b64_tr_b16 v[64:65], v124 offset:16384\n"
    "s_add_u32 s18, s97, 0xfc00\n"
    "buffer_load_dwordx4 v5, s[36:39], s18 offen lds\n"
    "v_mov_b32 v111, 0\n"
    "v_mfma_f32_16x16x32_bf16 v[20:23], v[48:51], v[212:215], v[20:23]\n"
    "s_waitcnt lgkmcnt(5)\n"
    "ds_read_b64_tr_b16 v[66:67], v124 offset:18432\n"
    "v_lshlrev_b32 v112, 16, v244\n"
    "v_mfma_f32_16x16x32_bf16 v[28:31], v[52:55], v[212:215], v[28:31]\n"
    "s_add_u32 s18, s97, 0x13000\n"
    "ds_read_b64_tr_b16 v[68:69], v125 offset:16384\n"
    "buffer_load_dwordx4 v5, s[36:39], s18 offen offset:1024 lds\n"
    "v_and_b32 v113, 0xffff0000, v244\n"
    "v_mfma_f32_16x16x32_bf16 v[24:27], v[52:55], v[208:211], v[24:27]\n"
    "s_waitcnt lgkmcnt(5)\n"
    "ds_read_b64_tr_b16 v[70:71], v125 offset:18432\n"
    "v_lshlrev_b32 v114, 16, v245\n"
    "v_and_b32 v115, 0xffff0000, v245\n"
    "v_mfma_f32_16x16x32_bf16 v[32:35], v[56:59], v[208:211], v[32:35]\n"
    "s_add_u32 m0, s16, 0xe000\n"
    "ds_read_b64_tr_b16 v[72:73], v126 offset:16384\n"
    "s_add_u32 s18, s97, 0x40c00\n"
    "buffer_load_dwordx4 v5, s[36:39], s18 offen lds\n"
    "v_lshlrev_b32 v116, 16, v246\n"
    "v_mfma_f32_16x16x32_bf16 v[36:39], v[56:59], v[212:215], v[36:39]\n"
    "s_waitcnt lgkmcnt(5)\n"
    "ds_read_b64_tr_b16 v[74:75], v126 offset:18432\n"
    "v_and_b32 v117, 0xffff0000, v246\n"
    "v_mfma_f32_16x16x32_bf16 v[44:47], v[60:63], v[212:215], v[44:47]\n"
    "s_add_u32 s18, s97, 0x44000\n"
    "ds_read_b64_tr_b16 v[76:77], v127 offset:16384\n"
    "buffer_load_dwordx4 v5, s[36:39], s18 offen offset:1024 lds\n"
    "v_lshlrev_b32 v118, 16, v247\n"
    "v_mfma_f32_16x16x32_bf16 v[40:43], v[60:63], v[208:211], v[40:43]\n"
    "s_waitcnt lgkmcnt(5)\n"
    "ds_read_b64_tr_b16 v[78:79], v127 offset:18432\n"
    "v_and_b32 v119, 0xffff0000, v247\n"
    "v_mfma_f32_16x16x32_bf16 v[16:19], v[64:67], v[216:219], v[16:19]\n"
    "s_add_u32 m0, s16, 0x10000\n"
    "ds_read_b64_tr_b16 v[48:49], v124 offset:32768\n"
    "s_add_u32 s18, s97, 0x4600\n"
    "buffer_load_dwordx4 v12, s[36:39], s18 offen lds\n"
    "v_mfma_f32_16x16x32_bf16 v[20:23], v[64:67], v[220:223], v[20:23]\n"
    "s_waitcnt lgkmcnt(5)\n"
    "ds_read_b64_tr_b16 v[50:51], v124 offset:34816\n"
    "v_mfma_f32_16x16x32_bf16 v[28:31], v[68:71], v[220:223], v[28:31]\n"
    "s_add_u32 s18, s97, 0x10600\n"
    "ds_read_b64_tr_b16 v[52:53], v125 offset:32768\n"
    "buffer_load_dwordx4 v12, s[36:39], s18 offen offset:1024 lds\n"
    "v_mfma_f32_16x16x32_bf16 v[24:27], v[68:71], v[216:219], v[24:27]\n"
    "s_waitcnt lgkmcnt(5)\n"
    "ds_read_b64_tr_b16 v[54:55], v125 offset:34816\n"
    "v_mfma_f32_16x16x32_bf16 v[32:35], v[72:75], v[216:219], v[32:35]\n"
    "ds_read_b64_tr_b16 v[56:57], v126 offset:32768\n"
    "s_waitcnt vmcnt(8)\n"
    "v_cmp_ne_u64 vcc, s[60:61], v[250:251]\n"
    "s_nop 0\n"
    "s_cbranch_vccz Lok_15\n"
    "s_mov_b32 s10, 0x8000\n"
    "Lspin_16:\n"
    "buffer_load_dwordx2 v[250:251], v7, s[56:59], 0 offen sc1\n"
    "s_sub_u32 s10, s10, 1\n"
    "s_waitcnt vmcnt(0)\n"
    "s_cbranch_scc1 Lok_15\n"
    "v_cmp_ne_u64 vcc, s[60:61], v[250:251]\n"
    "s_nop 0\n"
    "s_cbranch_vccnz Lspin_16\n"
    "Lok_15:\n"
    "v_mfma_f32_16x16x32_bf16 v[36:39], v[72:75], v[220:223], v[36:39]\n"
    "s_waitcnt lgkmcnt(5)\n"
    "ds_read_b64_tr_b16 v[58:59], v126 offset:34816\n"
    "v_mfma_f32_16x16x32_bf16 v[44:47], v[76:79], v[220:223], v[44:47]\n"
    "s_add_u32 s94, s98, 16384\n"
    "ds_read_b64_tr_b16 v[60:61], v127 offset:32768\n"
    "v_mfma_f32_16x16x32_bf16 v[40:43], v[76:79], v[216:219], v[40:43]\n"
    "s_setprio 2\n"
    "ds_read_b64_tr_b16 v[62:63], v127 offset:34816\n"
    "s_waitcnt lgkmcnt(6)\n"
    "v_mfma_f32_16x16x32_bf16 v[16:19], v[48:51], v[224:227], v[16:19]\n"
    "v_add_u32 v124, 0x18000, v124\n"
    "ds_read_b64_tr_b16 v[64:65], v124 offset:49152\n"
    "buffer_load_dwordx4 v[128:131], v1, s[56:59], s98 offen offset:0 sc1\n"
    "v_mfma_f32_16x16x32_bf16 v[20:23], v[48:51], v[228:231], v[20:23]\n"
    "s_waitcnt lgkmcnt(5)\n"
    "ds_read_b64_tr_b16 v[66:67], v124 offset:51200\n"
    "v_mfma_f32_16x16x32_bf16 v[28:31], v[52:55], v[228:231], v[28:31]\n"
    "v_add_u32 v125, 0x18000, v125\n"
    "ds_read_b64_tr_b16 v[68:69], v125 offset:49152\n"
    "buffer_load_dwordx4 v[132:135], v1, s[56:59], s94 offen offset:0 sc1\n"
    "v_mfma_f32_16x16x32_bf16 v[24:27], v[52:55], v[224:227], v[24:27]\n"
    "s_waitcnt lgkmcnt(5)\n"
    "ds_read_b64_tr_b16 v[70:71], v125 offset:51200\n"
    "v_mfma_f32_16x16x32_bf16 v[32:35], v[56:59], v[224:227], v[32:35]\n"
    "s_setprio 1\n"
    "v_add_u32 v126, 0x18000, v126\n"
    "ds_read_b64_tr_b16 v[72:73], v126 offset:49152\n"
    "buffer_load_dwordx4 v[136:139], v1, s[56:59], s98 offen offset:1024 sc1\n"
    "v_mfma_f32_16x16x32_bf16 v[36:39], v[56:59], v[228:231], v[36:39]\n"
    "s_waitcnt lgkmcnt(5)\n"
    "ds_read_b64_tr_b16 v[74:75], v126 offset:51200\n"
    "v_mfma_f32_16x16x32_bf16 v[44:47], v[60:63], v[228:231], v[44:47]\n"
    "v_add_u32 v127, 0x18000, v127\n"
    "ds_read_b64_tr_b16 v[76:77], v127 offset:49152\n"
    "buffer_load_dwordx4 v[140:143], v1, s[56:59], s94 offen offset:1024 sc1\n"
    "v_mfma_f32_16x16x32_bf16 v[40:43], v[60:63], v[224:227], v[40:43]\n"
    "s_setprio 0\n"
    "ds_read_b64_tr_b16 v[78:79], v127 offset:51200\n"
    "s_waitcnt vmcnt(4)\n"
    "s_barrier\n"
    "s_setprio 3\n"
    "s_waitcnt lgkmcnt(6)\n"
    "v_mfma_f32_16x16x32_bf16 v[16:19], v[64:67], v[232:235], v[16:19]\n"
    "ds_read_b64_tr_b16 v[48:49], v124 offset:16384\n"
    "buffer_load_dwordx4 v[144:147], v1, s[56:59], s98 offen offset:2048 sc1\n"
    "v_mfma_f32_16x16x32_bf16 v[20:23], v[64:67], v[236:239], v[20:23]\n"
    "s_waitcnt lgkmcnt(5)\n"
    "ds_read_b64_tr_b16 v[50:51], v124 offset:18432\n"
    "v_mfma_f32_16x16x32_bf16 v[28:31], v[68:71], v[236:239], v[28:31]\n"
    "ds_read_b64_tr_b16 v[52:53], v125 offset:16384\n"
    "buffer_load_dwordx4 v[148:151], v1, s[56:59], s94 offen offset:2048 sc1\n"
    "v_mfma_f32_16x16x32_bf16 v[24:27], v[68:71], v[232:235], v[24:27]\n"
    "s_waitcnt lgkmcnt(5)\n"
    "ds_read_b64_tr_b16 v[54:55], v125 offset:18432\n"
    "v_mfma_f32_16x16x32_bf16 v[32:35], v[72:75], v[232:235], v[32:35]\n"
    "ds_read_b64_tr_b16 v[56:57], v126 offset:16384\n"
    "buffer_load_dwordx4 v[152:155], v1, s[56:59], s7 offen offset:0 sc1\n"
    "v_mfma_f32_16x16x32_bf16 v[36:39], v[72:75], v[236:239], v[36:39]\n"
    "s_waitcnt lgkmcnt(5)\n"
    "ds_read_b64_tr_b16 v[58:59], v126 offset:18432\n"
    "v_mfma_f32_16x16x32_bf16 v[44:47], v[76:79], v[236:239], v[44:47]\n"
    "ds_read_b64_tr_b16 v[60:61], v127 offset:16384\n"
    "buffer_load_dwordx4 v[156:159], v1, s[56:59], s7 offen offset:1024 sc1\n"
    "v_mfma_f32_16x16x32_bf16 v[40:43], v[76:79], v[232:235], v[40:43]\n"
    "ds_read_b64_tr_b16 v[62:63], v127 offset:18432\n"
    "v_cvt_pk_bf16_f32 v16, v16, v17\n"
    "v_cvt_pk_bf16_f32 v17, v18, v19\n"
    "ds_write_b64 v96, v[16:17] offset:0\n"
    "v_cvt_pk_bf16_f32 v20, v20, v21\n"
    "v_cvt_pk_bf16_f32 v21, v22, v23\n"
    "ds_write_b64 v96, v[20:21] offset:2048\n"
    "s_waitcnt vmcnt(7) lgkmcnt(8)\n"
    "v_mfma_f32_16x16x32_bf16 v[160:163], v[48:51], v[128:131], 0\n"
    "ds_read_b64_tr_b16 v[64:65], v124 offset:32768\n"
    "s_waitcnt vmcnt(6)\n"
    "v_mfma_f32_16x16x32_bf16 v[164:167], v[48:51], v[132:135], 0\n"
    "s_waitcnt lgkmcnt(7)\n"
    "ds_read_b64_tr_b16 v[66:67], v124 offset:34816\n"
    "v_cvt_pk_bf16_f32 v28, v28, v29\n"
    "v_mfma_f32_16x16x32_bf16 v[172:175], v[52:55], v[132:135], 0\n"
    "ds_read_b64_tr_b16 v[68:69], v125 offset:32768\n"
    "v_cvt_pk_bf16_f32 v29, v30, v31\n"
    "v_mfma_f32_16x16x32_bf16 v[168:171], v[52:55], v[128:131], 0\n"
    "s_waitcnt lgkmcnt(7)\n"
    "ds_read_b64_tr_b16 v[70:71], v125 offset:34816\n"
    "ds_write_b64 v97, v[28:29] offset:2048\n"
    "v_mfma_f32_16x16x32_bf16 v[176:179], v[56:59], v[128:131], 0\n"
    "ds_read_b64_tr_b16 v[72:73], v126 offset:32768\n"
    "v_mfma_f32_16x16x32_bf16 v[180:183], v[56:59], v[132:135], 0\n"
    "s_waitcnt lgkmcnt(8)\n"
    "ds_read_b64_tr_b16 v[74:75], v126 offset:34816\n"
    "v_cvt_pk_bf16_f32 v24, v24, v25\n"
    "v_mfma_f32_16x16x32_bf16 v[188:191], v[60:63], v[132:135], 0\n"
    "ds_read_b64_tr_b16 v[76:77], v127 offset:32768\n"
    "v_cvt_pk_bf16_f32 v25, v26, v27\n"
    "v_mfma_f32_16x16x32_bf16 v[184:187], v[60:63], v[128:131], 0\n"
    "ds_read_b64_tr_b16 v[78:79], v127 offset:34816\n"
    "ds_write_b64 v97, v[24:25] offset:0\n"
    "s_waitcnt vmcnt(5) lgkmcnt(8)\n"
    "v_mfma_f32_16x16x32_bf16 v[160:163], v[64:67], v[136:139], v[160:163]\n"
    "v_add_u32 v124, 0xffff4000, v124\n"
    "ds_read_b64_tr_b16 v[48:49], v124 offset:0\n"
    "s_waitcnt vmcnt(4)\n"
    "v_mfma_f32_16x16x32_bf16 v[164:167], v[64:67], v[140:143], v[164:167]\n"
    "s_waitcnt lgkmcnt(7)\n"
    "ds_read_b64_tr_b16 v[50:51], v124 offset:2048\n"
    "v_cvt_pk_bf16_f32 v32, v32, v33\n"
    "v_mfma_f32_16x16x32_bf16 v[172:175], v[68:71], v[140:143], v[172:175]\n"
    "v_add_u32 v125, 0xffff4000, v125\n"
    "ds_read_b64_tr_b16 v[52:53], v125 offset:0\n"
    "v_cvt_pk_bf16_f32 v33, v34, v35\n"
    "v_mfma_f32_16x16x32_bf16 v[168:171], v[68:71], v[136:139], v[168:171]\n"
    "s_waitcnt lgkmcnt(6)\n"
    "ds_read_b64_tr_b16 v[54:55], v125 offset:2048\n"
    "v_mfma_f32_16x16x32_bf16 v[176:179], v[72:75], v[136:139], v[176:179]\n"
    "v_add_u32 v126, 0xffff4000, v126\n"
    "ds_read_b64_tr_b16 v[56:57], v126 offset:0\n"
    "ds_write_b64 v98, v[32:33] offset:0\n"
    "v_mfma_f32_16x16x32_bf16 v[180:183], v[72:75], v[140:143], v[180:183]\n"
    "s_waitcnt lgkmcnt(7)\n"
    "ds_read_b64_tr_b16 v[58:59], v126 offset:2048\n"
    "v_cvt_pk_bf16_f32 v36, v36, v37\n"
    "v_mfma_f32_16x16x32_bf16 v[188:191], v[76:79], v[140:143], v[188:191]\n"
    "v_add_u32 v127, 0xffff4000, v127\n"
    "ds_read_b64_tr_b16 v[60:61], v127 offset:0\n"
    "v_cvt_pk_bf16_f32 v37, v38, v39\n"
    "v_mfma_f32_16x16x32_bf16 v[184:187], v[76:79], v[136:139], v[184:187]\n"
    "ds_read_b64_tr_b16 v[62:63], v127 offset:2048\n"
    "s_waitcnt vmcnt(3) lgkmcnt(7)\n"
    "v_mfma_f32_16x16x32_bf16 v[160:163], v[48:51], v[144:147], v[160:163]\n"
    "ds_read_b64_tr_b16 v[64:65], v124 offset:16384\n"
    "ds_write_b64 v98, v[36:37] offset:2048\n"
    "s_waitcnt vmcnt(2)\n"
    "v_mfma_f32_16x16x32_bf16 v[164:167], v[48:51], v[148:151], v[164:167]\n"
    "s_waitcnt lgkmcnt(7)\n"
    "ds_read_b64_tr_b16 v[66:67], v124 offset:18432\n"
    "v_cvt_pk_bf16_f32 v44, v44, v45\n"
    "v_mfma_f32_16x16x32_bf16 v[172:175], v[52:55], v[148:151], v[172:175]\n"
    "s_setprio 2\n"
    "ds_read_b64_tr_b16 v[68:69], v125 offset:16384\n"
    "v_mfma_f32_16x16x32_bf16 v[168:171], v[52:55], v[144:147], v[168:171]\n"
    "s_waitcnt lgkmcnt(6)\n"
    "ds_read_b64_tr_b16 v[70:71], v125 offset:18432\n"
    "v_cvt_pk_bf16_f32 v45, v46, v47\n"
    "v_mfma_f32_16x16x32_bf16 v[176:179], v[56:59], v[144:147], v[176:179]\n"
    "ds_read_b64_tr_b16 v[72:73], v126 offset:16384\n"
    "s_waitcnt vmcnt(0)\n"
    "v_swap_b32 v154, v156\n"
    "ds_write_b64 v99, v[44:45] offset:2048\n"
    "v_mfma_f32_16x16x32_bf16 v[180:183], v[56:59], v[148:151], v[180:183]\n"
    "s_waitcnt lgkmcnt(7)\n"
    "ds_read_b64_tr_b16 v[74:75], v126 offset:18432\n"
    "v_swap_b32 v155, v157\n"
    "v_cvt_pk_bf16_f32 v40, v40, v41\n"
    "v_mfma_f32_16x16x32_bf16 v[188:191], v[60:63], v[148:151], v[188:191]\n"
    "ds_read_b64_tr_b16 v[76:77], v127 offset:16384\n"
    "v_mfma_f32_16x16x32_bf16 v[184:187], v[60:63], v[144:147], v[184:187]\n"
    "s_waitcnt lgkmcnt(6)\n"
    "ds_read_b64_tr_b16 v[78:79], v127 offset:18432\n"
    "v_cvt_pk_bf16_f32 v41, v42, v43\n"
    "v_mfma_f32_16x16x32_bf16 v[160:163], v[64:67], v[152:155], v[160:163]\n"
    "ds_write_b64 v99, v[40:41] offset:0\n"
    "v_mfma_f32_16x16x32_bf16 v[164:167], v[64:67], v[156:159], v[164:167]\n"
    "s_setprio 1\n"
    "s_waitcnt lgkmcnt(6)\n"
    "v_mfma_f32_16x16x32_bf16 v[172:175], v[68:71], v[156:159], v[172:175]\n"
    "v_mfma_f32_16x16x32_bf16 v[168:171], v[68:71], v[152:155], v[168:171]\n"
    "s_waitcnt lgkmcnt(3)\n"
    "s_nop 0\n"
    "v_cvt_pk_bf16_f32 v160, v160, v161\n"
    "v_cvt_pk_bf16_f32 v161, v162, v163\n"
    "ds_write_b64 v100, v[160:161] offset:0\n"
    "v_mfma_f32_16x16x32_bf16 v[176:179], v[72:75], v[152:155], v[176:179]\n"
    "v_cvt_pk_bf16_f32 v164, v164, v165\n"
    "v_cvt_pk_bf16_f32 v165, v166, v167\n"
    "ds_write_b64 v100, v[164:165] offset:2048\n"
    "v_mfma_f32_16x16x32_bf16 v[180:183], v[72:75], v[156:159], v[180:183]\n"
    "s_waitcnt lgkmcnt(3)\n"
    "v_cvt_pk_bf16_f32 v172, v172, v173\n"
    "v_cvt_pk_bf16_f32 v173, v174, v175\n"
    "ds_write_b64 v101, v[172:173] offset:2048\n"
    "v_mfma_f32_16x16x32_bf16 v[188:191], v[76:79], v[156:159], v[188:191]\n"
    "s_setprio 0\n"
    "v_cvt_pk_bf16_f32 v168, v168, v169\n"
    "v_cvt_pk_bf16_f32 v169, v170, v171\n"
    "ds_write_b64 v101, v[168:169] offset:0\n"
    "v_mfma_f32_16x16x32_bf16 v[184:187], v[76:79], v[152:155], v[184:187]\n"
    "v_cvt_pk_bf16_f32 v176, v176, v177\n"
    "v_cvt_pk_bf16_f32 v177, v178, v179\n"
    "ds_write_b64 v102, v[176:177] offset:0\n"
    "v_cvt_pk_bf16_f32 v180, v180, v181\n"
    "v_cvt_pk_bf16_f32 v181, v182, v183\n"
    "ds_write_b64 v102, v[180:181] offset:2048\n"
    "v_cvt_pk_bf16_f32 v188, v188, v189\n"
    "v_cvt_pk_bf16_f32 v189, v190, v191\n"
    "ds_write_b64 v103, v[188:189] offset:2048\n"
    "v_cvt_pk_bf16_f32 v184, v184, v185\n"
    "v_cvt_pk_bf16_f32 v185, v186, v187\n"
    "ds_write_b64 v103, v[184:185] offset:0\n"
    "s_waitcnt lgkmcnt(0)\n"
    "s_barrier\n"
    "ds_read_b128 v[16:19], v104 offset:0\n"
    "ds_read_b128 v[20:23], v104 offset:8192\n"
    "ds_read_b128 v[24:27], v104 offset:16384\n"
    "ds_read_b128 v[28:31], v104 offset:24576\n"
    "ds_read_b128 v[32:35], v105 offset:0\n"
    "ds_read_b128 v[36:39], v105 offset:8192\n"
    "ds_read_b128 v[40:43], v105 offset:16384\n"
    "ds_read_b128 v[44:47], v105 offset:24576\n"
    "s_waitcnt lgkmcnt(7)\n"
    "v_mfma_f32_16x16x32_bf16 v[80:83], v[108:111], v[16:19], 0\n"
    "v_mfma_f32_16x16x32_bf16 v[84:87], v[106:109], v[16:19], 0\n"
    "s_waitcnt lgkmcnt(6)\n"
    "v_mfma_f32_16x16x32_bf16 v[80:83], v[108:111], v[20:23], v[80:83]\n"
    "v_mfma_f32_16x16x32_bf16 v[84:87], v[106:109], v[20:23], v[84:87]\n"
    "s_waitcnt lgkmcnt(5)\n"
    "v_mfma_f32_16x16x32_bf16 v[80:83], v[108:111], v[24:27], v[80:83]\n"
    "v_mfma_f32_16x16x32_bf16 v[84:87], v[106:109], v[24:27], v[84:87]\n"
    "s_waitcnt lgkmcnt(4)\n"
    "v_mfma_f32_16x16x32_bf16 v[80:83], v[108:111], v[28:31], v[80:83]\n"
    "v_mfma_f32_16x16x32_bf16 v[84:87], v[106:109], v[28:31], v[84:87]\n"
    "s_waitcnt lgkmcnt(3)\n"
    "s_nop 5\n"
    "v_mul_f32 v88, 0xbfb8aa3b, v80\n"
    "v_mul_f32 v89, 0xbfb8aa3b, v81\n"
    "v_mul_f32 v90, 0xbfb8aa3b, v82\n"
    "v_mul_f32 v91, 0xbfb8aa3b, v83\n"
    "v_mul_f32 v92, 0xbfb8aa3b, v84\n"
    "v_mul_f32 v93, 0xbfb8aa3b, v85\n"
    "v_mul_f32 v94, 0xbfb8aa3b, v86\n"
    "v_mul_f32 v95, 0xbfb8aa3b, v87\n"
    "v_exp_f32 v88, v88\n"
    "v_mfma_f32_16x16x32_bf16 v[48:51], v[108:111], v[32:35], 0\n"
    "v_exp_f32 v89, v89\n"
    "v_mfma_f32_16x16x32_bf16 v[52:55], v[106:109], v[32:35], 0\n"
    "s_waitcnt lgkmcnt(2)\n"
    "v_exp_f32 v90, v90\n"
    "v_mfma_f32_16x16x32_bf16 v[48:51], v[108:111], v[36:39], v[48:51]\n"
    "v_exp_f32 v91, v91\n"
    "v_mfma_f32_16x16x32_bf16 v[52:55], v[106:109], v[36:39], v[52:55]\n"
    "s_waitcnt lgkmcnt(1)\n"
    "v_exp_f32 v92, v92\n"
    "v_mfma_f32_16x16x32_bf16 v[48:51], v[108:111], v[40:43], v[48:51]\n"
    "v_exp_f32 v93, v93\n"
    "v_mfma_f32_16x16x32_bf16 v[52:55], v[106:109], v[40:43], v[52:55]\n"
    "s_waitcnt lgkmcnt(0)\n"
    "v_exp_f32 v94, v94\n"
    "v_mfma_f32_16x16x32_bf16 v[48:51], v[108:111], v[44:47], v[48:51]\n"
    "v_exp_f32 v95, v95\n"
    "v_mfma_f32_16x16x32_bf16 v[52:55], v[106:109], v[44:47], v[52:55]\n"
    "v_add_f32 v88, 1.0, v88\n"
    "v_add_f32 v89, 1.0, v89\n"
    "v_add_f32 v90, 1.0, v90\n"
    "v_add_f32 v91, 1.0, v91\n"
    "v_add_f32 v92, 1.0, v92\n"
    "v_add_f32 v93, 1.0, v93\n"
    "v_add_f32 v94, 1.0, v94\n"
    "v_add_f32 v95, 1.0, v95\n"
    "v_rcp_f32 v88, v88\n"
    "v_rcp_f32 v89, v89\n"
    "v_rcp_f32 v90, v90\n"
    "v_rcp_f32 v91, v91\n"
    "v_rcp_f32 v92, v92\n"
    "v_rcp_f32 v93, v93\n"
    "v_rcp_f32 v94, v94\n"
    "v_rcp_f32 v95, v95\n"
    "v_mul_f32 v80, v80, v88\n"
    "v_mul_f32 v81, v81, v89\n"
    "v_mul_f32 v82, v82, v90\n"
    "v_mul_f32 v83, v83, v91\n"
    "v_mul_f32 v84, v84, v92\n"
    "v_mul_f32 v85, v85, v93\n"
    "v_mul_f32 v86, v86, v94\n"
    "v_mul_f32 v87, v87, v95\n"
    "v_fma_f32 v48, v48, v80, v112\n"
    "v_fma_f32 v49, v49, v81, v113\n"
    "v_fma_f32 v50, v50, v82, v114\n"
    "v_fma_f32 v51, v51, v83, v115\n"
    "v_fma_f32 v52, v52, v84, v116\n"
    "v_fma_f32 v53, v53, v85, v117\n"
    "v_fma_f32 v54, v54, v86, v118\n"
    "v_fma_f32 v55, v55, v87, v119\n"
    "v_cvt_pk_bf16_f32 v56, v48, v49\n"
    "v_cvt_pk_bf16_f32 v57, v50, v51\n"
    "v_cvt_pk_bf16_f32 v58, v52, v53\n"
    "v_cvt_pk_bf16_f32 v59, v54, v55\n"
    "buffer_store_dwordx4 v[56:59], v6, s[52:55], s99 offen\n"
    "s_endpgm\n"
    "Lfin_h0:\n"
    ".p2align 12\n"
    "Lhalf1:\n"
    "s_load_dwordx2 s[36:37], %1, 0x10\n"
    "s_load_dwordx2 s[48:49], %1, 0x28\n"
    "s_load_dwordx2 s[52:53], %1, 0x30\n"
    "s_load_dwordx2 s[56:57], %1, 0x38\n"
    "v_add_u32 v83, 7168, v82\n"
    "v_lshrrev_b32 v84, 4, v0\n"
    "s_mov_b32 s24, %2\n"
    "s_and_b32 s26, s24, 7\n"
    "s_add_u32 s72, s71, 112\n"
    "s_mov_b32 s38, 0x62000\n"
    "s_mov_b32 s39, 0x20000\n"
    "s_mov_b32 s42, 0x188000\n"
    "s_mov_b32 s43, 0x20000\n"
    "s_mov_b32 s46, 0x188000\n"
    "s_mov_b32 s47, 0x20000\n"
    "s_mov_b32 s50, 0x188000\n"
    "s_mov_b32 s51, 0x20000\n"
    "s_mov_b32 s54, 0x1c0000\n"
    "s_mov_b32 s55, 0x20000\n"
    "s_mov_b32 s58, 0xf10000\n"
    "s_mov_b32 s59, 0x20000\n"
    "v_lshlrev_b32 v1, 4, v0\n"
    "s_lshl_b32 s62, s25, 10\n"
    "s_lshl_b32 s16, s66, 11\n"
    "s_lshl_b32 s17, s66, 12\n"
    "s_waitcnt lgkmcnt(0)\n"
    "buffer_load_dwordx4 v[132:135], v82, s[28:31], s99 offen offset:0\n"
    "buffer_load_dwordx4 v[140:143], v82, s[28:31], s99 offen offset:128\n"
    "buffer_load_dwordx4 v[148:151], v82, s[28:31], s99 offen offset:256\n"
    "v_and_b32 v85, 15, v0\n"
    "v_bfe_u32 v86, v0, 2, 2\n"
    "v_mov_b32 v88, 0x3f803f80\n"
    "v_cmp_eq_u32 vcc, v84, v86\n"
    "v_mov_b32 v52, 0\n"
    "v_mov_b32 v53, 0\n"
    "v_cndmask_b32 v48, 0, v88, vcc\n"
    "v_cndmask_b32 v49, 0, v88, vcc\n"
    "v_cndmask_b32 v50, 0, v88, vcc\n"
    "v_cndmask_b32 v51, 0, v88, vcc\n"
    "v_mov_b32 v56, 0\n"
    "v_mov_b32 v57, 0\n"
    "v_and_b32 v87, 1, v0\n"
    "v_lshlrev_b32 v87, 4, v87\n"
    "v_mov_b32 v88, 0x3f80\n"
    "v_lshlrev_b32 v87, v87, v88\n"
    "s_mov_b32 s8, 0x00300003\n"
    "s_mov_b32 s9, 0x30000300\n"
    "v_cndmask_b32 v54, 0, v87, s[8:9]\n"
    "s_lshl_b64 s[8:9], s[8:9], 2\n"
    "v_cndmask_b32 v55, 0, v87, s[8:9]\n"
    "s_add_u32 s64, s73, 0xc000\n"
    "v_xor_b32 v87, v85, v84\n"
    "v_lshlrev_b32 v87, 4, v87\n"
    "v_lshl_add_u32 v87, v84, 8, v87\n"
    "v_add_u32 v94, s64, v87\n"
    "v_xor_b32 v95, 64, v94\n"
    "v_xor_b32 v4, 0x400, v94\n"
    "v_xor_b32 v12, 0x400, v95\n"
    "v_xor_b32 v87, v80, v81\n"
    "v_lshlrev_b32 v87, 4, v87\n"
    "v_and_b32 v88, 3, v81\n"
    "v_lshl_add_u32 v87, v88, 8, v87\n"
    "v_lshrrev_b32 v88, 2, v81\n"
    "v_lshl_add_u32 v87, v88, 13, v87\n"
    "v_add_u32 v60, s64, v87\n"
    "s_lshl_b32 s64, s66, 3\n"
    "v_add_u32 v87, s64, v80\n"
    "s_and_b32 s64, s25, 1\n"
    "s_lshl_b32 s64, s64, 2\n"
    "v_and_b32 v88, 2, v80\n"
    "v_or_b32 v88, s64, v88\n"
    "v_xor_b32 v88, v88, v81\n"
    "v_min_u32 v89, 6, v88\n"
    "v_mul_u32_u24 v2, 2688, v87\n"
    "v_lshl_add_u32 v2, v89, 4, v2\n"
    "v_mul_u32_u24 v3, 3584, v87\n"
    "v_lshl_add_u32 v3, v88, 4, v3\n"
    "s_cmp_lt_u32 s70, 2\n"
    "s_cselect_b32 s5, 0, 112\n"
    "s_cselect_b32 s4, 172032, s5\n"
    "s_sub_u32 s5, 172032, s5\n"
    "s_mov_b32 s6, s71\n"
    "s_lshl_b32 s63, s66, 10\n"
    "v_and_b32 v87, 3, v80\n"
    "v_lshlrev_b32 v87, 5, v87\n"
    "v_lshl_add_u32 v88, v84, 3, v86\n"
    "v_lshlrev_b32 v91, 6, v88\n"
    "v_lshlrev_b32 v88, 7, v88\n"
    "v_and_b32 v90, 3, v0\n"
    "v_lshlrev_b32 v90, 3, v90\n"
    "s_mul_i32 s64, s79, 56\n"
    "v_add_u32 v89, s64, v90\n"
    "v_xor_b32 v92, v89, v87\n"
    "v_add_u32 v6, v88, v92\n"
    "v_add_u32 v89, 32, v89\n"
    "v_xor_b32 v89, v89, v87\n"
    "v_add_u32 v7, v88, v89\n"
    "s_lshl_b32 s64, s79, 6\n"
    "v_add_u32 v89, s64, v90\n"
    "v_xor_b32 v89, v89, v87\n"
    "v_add_u32 v8, v88, v89\n"
    "v_xor_b32 v9, 32, v8\n"
    "v_and_b32 v89, 1, v84\n"
    "v_xor_b32 v89, s79, v89\n"
    "v_lshl_add_u32 v89, v89, 5, v90\n"
    "v_add_u32 v5, v91, v89\n"
    "s_lshr_b32 s64, s73, 4\n"
    "v_lshl_add_u32 v74, v85, 2, s64\n"
    "v_xor_b32 v75, 64, v74\n"
    "v_lshl_add_u32 v76, v84, 4, s64\n"
    "v_xor_b32 v77, 64, v76\n"
    "v_lshl_add_u32 v79, v0, 3, s63\n"
    "v_lshl_add_u32 v73, v80, 2, s64\n"
    "s_nop 0\n"
    "s_mul_i32 s64, s69, 7\n"
    "s_mul_i32 s65, s70, 5\n"
    "s_add_u32 s67, s65, 1\n"
    "s_lshl_b32 s65, s65, 1\n"
    "s_add_u32 s80, s70, 4\n"
    "s_lshl_b32 s81, s70, 1\n"
    "s_sub_u32 s81, s81, 2\n"
    "s_cmp_lt_u32 s70, 2\n"
    "s_cselect_b32 s80, s65, s80\n"
    "s_cselect_b32 s81, s67, s81\n"
    "s_add_u32 s81, s81, s64\n"
    "s_lshl_b32 s81, s81, 7\n"
    "s_lshl_b32 s64, s64, 1\n"
    "s_add_u32 s80, s80, s64\n"
    "s_lshl_b32 s80, s80, 6\n"
    "s_add_u32 s6, s6, s4\n"
    "buffer_load_dwordx4 v[156:159], v82, s[28:31], s99 offen offset:384\n"
    "buffer_load_dwordx4 v[164:167], v82, s[28:31], s99 offen offset:512\n"
    "buffer_load_dwordx4 v[172:175], v82, s[28:31], s99 offen offset:640\n"
    "buffer_load_dwordx4 v[180:183], v82, s[28:31], s99 offen offset:768\n"
    "s_cmp_ge_u32 s70, 2\n"
    "s_cbranch_scc1 Ls1_h1\n"
    "s_and_b32 s66, s25, 3\n"
    "s_lshl_b32 s67, s68, 2\n"
    "s_add_u32 s67, s67, s66\n"
    "s_and_b32 s65, s77, 7\n"
    "s_lshl_b32 s65, s65, 1\n"
    "s_add_u32 s65, s65, s79\n"
    "s_lshl_b32 s65, s65, 6\n"
    "s_add_u32 s65, s65, s67\n"
    "s_lshl_b32 s74, s65, 12\n"
    "s_add_u32 s74, s74, 0x210000\n"
    "s_lshl_b32 s77, s77, 4\n"
    "s_add_u32 s77, s77, s68\n"
    "s_lshl_b32 s77, s77, 3\n"
    "s_add_u32 s77, s77, 256\n"
    "s_lshl_b32 s67, s67, 1\n"
    "s_add_u32 s67, s67, s79\n"
    "s_mul_i32 s67, s67, 57344\n"
    "s_add_u32 s67, s67, 0x610000\n"
    "s_mul_i32 s76, s79, 0xfffe4000\n"
    "s_add_u32 s76, s76, 57344\n"
    "s_lshl_b32 s82, s80, 4\n"
    "s_add_u32 s82, s82, s67\n"
    "s_lshl_b32 s83, s81, 4\n"
    "s_add_u32 s83, s83, s67\n"
    "s_lshl_b32 s66, s79, 10\n"
    "s_add_u32 s83, s83, s66\n"
    "s_add_u32 s82, s82, s66\n"
    "s_lshl_b32 s95, s68, 4\n"
    "s_lshl_b32 s66, s69, 2\n"
    "s_add_u32 s66, s66, s70\n"
    "s_add_u32 s95, s95, s66\n"
    "s_lshl_b32 s95, s95, 3\n"
    "s_add_u32 s95, s95, 4096\n"
    "v_lshrrev_b32 v90, 4, v0\n"
    "s_mov_b32 s88, 0xf000f000\n"
    "s_mov_b32 s89, 0xf000f000\n"
    "v_and_b32 v91, 15, v0\n"
    "v_lshlrev_b32 v92, 4, v91\n"
    "v_lshl_add_u32 v11, v90, 9, v92\n"
    "v_mov_b32 v92, 0x3f803f80\n"
    "v_cmp_eq_u32 vcc, 12, v91\n"
    "s_nop 1\n"
    "v_cndmask_b32 v78, 0, v92, vcc\n"
    "s_branch Ls2_h1\n"
    "Ls1_h1:\n"
    "s_and_b32 s66, s25, 3\n"
    "s_lshl_b32 s67, s68, 2\n"
    "s_add_u32 s67, s67, s66\n"
    "s_and_b32 s65, s77, 7\n"
    "s_lshl_b32 s65, s65, 1\n"
    "s_add_u32 s65, s65, s79\n"
    "s_lshl_b32 s65, s65, 6\n"
    "s_add_u32 s65, s65, s67\n"
    "s_lshl_b32 s74, s65, 12\n"
    "s_add_u32 s74, s74, 0x210000\n"
    "s_lshl_b32 s77, s77, 4\n"
    "s_add_u32 s77, s77, s68\n"
    "s_lshl_b32 s77, s77, 3\n"
    "s_add_u32 s77, s77, 256\n"
    "s_lshl_b32 s67, s67, 1\n"
    "s_add_u32 s67, s67, s79\n"
    "s_mul_i32 s67, s67, 57344\n"
    "s_add_u32 s67, s67, 0x610000\n"
    "s_mul_i32 s76, s79, 0xfffe4000\n"
    "s_add_u32 s76, s76, 57344\n"
    "s_lshl_b32 s82, s80, 4\n"
    "s_add_u32 s82, s82, s67\n"
    "s_lshl_b32 s83, s81, 4\n"
    "s_add_u32 s83, s83, s67\n"
    "s_lshl_b32 s66, s79, 10\n"
    "s_add_u32 s83, s83, s66\n"
    "s_add_u32 s82, s82, 0\n"
    "s_lshl_b32 s95, s68, 4\n"
    "s_lshl_b32 s66, s69, 2\n"
    "s_add_u32 s66, s66, s70\n"
    "s_add_u32 s95, s95, s66\n"
    "s_lshl_b32 s95, s95, 3\n"
    "s_add_u32 s95, s95, 4096\n"
    "v_lshrrev_b32 v90, 4, v0\n"
    "s_lshl_b32 s65, s65, 11\n"
    "s_add_u32 s65, s65, 0x10000\n"
    "s_cmp_lt_u32 s69, 2\n"
    "s_cselect_b32 s74, s65, s74\n"
    "s_cselect_b32 s65, 1, 2\n"
    "s_cselect_b32 s84, 0x3e8b97f6, 1.0\n"
    "s_cselect_b32 s85, 0, 0x3f803f80\n"
    "s_lshl_b32 s65, s65, 18\n"
    "s_add_u32 s75, s74, s65\n"
    "s_add_u32 s78, s77, 128\n"
    "s_mov_b32 s86, 0\n"
    "s_mov_b32 s87, 0xffff0000\n"
    "v_mov_b32 v118, s85\n"
    "v_and_b32 v91, 8, v0\n"
    "v_and_b32 v92, 7, v0\n"
    "v_lshl_add_u32 v91, v91, 1, v92\n"
    "v_lshlrev_b32 v91, 4, v91\n"
    "v_lshl_add_u32 v10, v90, 9, v91\n"
    "Ls2_h1:\n"
    "Lhop_h1:\n"
    "s_cmp_lt_u32 s70, 2\n"
    "s_movk_i32 s64, 0xfc\n"
    "s_cmovk_i32 s64, 0xe3\n"
    "s_and_b32 s65, s25, 3\n"
    "s_lshl_b32 s65, 0x11111111, s65\n"
    "s_and_b32 s64, s64, s65\n"
    "s_mov_b64 exec, 0\n"
    "s_bitcmp1_b32 s64, 0\n"
    "s_cbranch_scc1 Lst1_0\n"
    "Lr1_0:\n"
    "s_bitcmp1_b32 s64, 1\n"
    "s_cbranch_scc1 Lst1_1\n"
    "Lr1_1:\n"
    "s_bitcmp1_b32 s64, 2\n"
    "s_cbranch_scc1 Lst1_2\n"
    "Lr1_2:\n"
    "s_bitcmp1_b32 s64, 3\n"
    "s_cbranch_scc1 Lst1_3\n"
    "Lr1_3:\n"
    "s_bitcmp1_b32 s64, 4\n"
    "s_cbranch_scc1 Lst1_4\n"
    "Lr1_4:\n"
    "s_bitcmp1_b32 s64, 5\n"
    "s_cbranch_scc1 Lst1_5\n"
    "Lr1_5:\n"
    "s_bitcmp1_b32 s64, 6\n"
    "s_cbranch_scc1 Lst1_6\n"
    "Lr1_6:\n"
    "s_bitcmp1_b32 s64, 7\n"
    "s_cbranch_scc1 Lst1_7\n"
    "Lr1_7:\n"
    "s_bitcmp1_b32 s64, 8\n"
    "s_cbranch_scc1 Lr1_8\n"
    "Lr1_8:\n"
    "s_bitcmp1_b32 s64, 9\n"
    "s_cbranch_scc1 Lr1_9\n"
    "Lr1_9:\n"
    "s_mov_b64 exec, -1\n"
    "s_waitcnt lgkmcnt(0)\n"
    "s_waitcnt vmcnt(6)\n"
    "v_mfma_f32_16x16x32_bf16 v[16:19], v[48:51], v[132:135], 0\n"
    "s_waitcnt vmcnt(5)\n"
    "v_mfma_f32_16x16x32_bf16 v[16:19], v[48:51], v[140:143], v[16:19]\n"
    "s_waitcnt vmcnt(4)\n"
    "v_mfma_f32_16x16x32_bf16 v[16:19], v[48:51], v[148:151], v[16:19]\n"
    "s_waitcnt vmcnt(3)\n"
    "v_mfma_f32_16x16x32_bf16 v[16:19], v[48:51], v[156:159], v[16:19]\n"
    "s_waitcnt vmcnt(2)\n"
    "v_mfma_f32_16x16x32_bf16 v[16:19], v[48:51], v[164:167], v[16:19]\n"
    "s_waitcnt vmcnt(1)\n"
    "v_mfma_f32_16x16x32_bf16 v[16:19], v[48:51], v[172:175], v[16:19]\n"
    "buffer_load_dwordx4 v[188:191], v83, s[28:31], s99 offen offset:0\n"
    "s_waitcnt vmcnt(1)\n"
    "v_mfma_f32_16x16x32_bf16 v[16:19], v[48:51], v[180:183], v[16:19]\n"
    "buffer_load_dwordx4 v[196:199], v83, s[28:31], s99 offen offset:128\n"
    "buffer_load_dwordx4 v[204:207], v83, s[28:31], s99 offen offset:256\n"
    "buffer_load_dwordx4 v[212:215], v83, s[28:31], s99 offen offset:384\n"
    "buffer_load_dwordx4 v[220:223], v83, s[28:31], s99 offen offset:512\n"
    "buffer_load_dwordx4 v[228:231], v83, s[28:31], s99 offen offset:640\n"
    "buffer_load_dwordx4 v[236:239], v83, s[28:31], s99 offen offset:768\n"
    "s_nop 1\n"
    "v_add_f32_dpp v16, v16, v16 quad_perm:[1,0,3,2] row_mask:0xf bank_mask:0xf\n"
    "s_nop 1\n"
    "v_add_f32_dpp v16, v16, v16 quad_perm:[2,3,0,1] row_mask:0xf bank_mask:0xf\n"
    "s_nop 1\n"
    "v_add_f32_dpp v16, v16, v16 row_half_mirror row_mask:0xf bank_mask:0xf\n"
    "v_mul_f32 v19, 0xbb124925, v16\n"
    "v_mul_f32 v18, 0xbb124925, v16\n"
    "v_mul_f32 v17, 0xbb124925, v16\n"
    "v_mul_f32 v16, 0xbb124925, v16\n"
    "s_nop 1\n"
    "v_mfma_f32_16x16x32_bf16 v[24:27], v[54:57], v[132:135], v[16:19]\n"
    "v_mfma_f32_16x16x32_bf16 v[28:31], v[52:55], v[132:135], v[16:19]\n"
    "v_mfma_f32_16x16x32_bf16 v[32:35], v[54:57], v[140:143], v[16:19]\n"
    "v_mfma_f32_16x16x32_bf16 v[36:39], v[52:55], v[140:143], v[16:19]\n"
    "v_mfma_f32_16x16x32_bf16 v[40:43], v[54:57], v[148:151], v[16:19]\n"
    "s_nop 3\n"
    "v_cvt_pk_bf16_f32 v132, v24, v25\n"
    "v_cvt_pk_bf16_f32 v133, v26, v27\n"
    "v_mfma_f32_16x16x32_bf16 v[44:47], v[52:55], v[148:151], v[16:19]\n"
    "v_cvt_pk_bf16_f32 v134, v28, v29\n"
    "v_cvt_pk_bf16_f32 v135, v30, v31\n"
    "ds_write_b128 v60, v[132:135] offset:0\n"
    "v_mfma_f32_16x16x32_bf16 v[24:27], v[54:57], v[156:159], v[16:19]\n"
    "v_cvt_pk_bf16_f32 v140, v32, v33\n"
    "v_cvt_pk_bf16_f32 v141, v34, v35\n"
    "v_mfma_f32_16x16x32_bf16 v[28:31], v[52:55], v[156:159], v[16:19]\n"
    "v_cvt_pk_bf16_f32 v142, v36, v37\n"
    "v_cvt_pk_bf16_f32 v143, v38, v39\n"
    "ds_write_b128 v60, v[140:143] offset:16384\n"
    "v_mfma_f32_16x16x32_bf16 v[32:35], v[54:57], v[164:167], v[16:19]\n"
    "v_cvt_pk_bf16_f32 v148, v40, v41\n"
    "v_cvt_pk_bf16_f32 v149, v42, v43\n"
    "v_mfma_f32_16x16x32_bf16 v[36:39], v[52:55], v[164:167], v[16:19]\n"
    "v_cvt_pk_bf16_f32 v150, v44, v45\n"
    "v_cvt_pk_bf16_f32 v151, v46, v47\n"
    "ds_write_b128 v60, v[148:151] offset:32768\n"
    "v_mfma_f32_16x16x32_bf16 v[40:43], v[54:57], v[172:175], v[16:19]\n"
    "v_cvt_pk_bf16_f32 v156, v24, v25\n"
    "v_cvt_pk_bf16_f32 v157, v26, v27\n"
    "v_mfma_f32_16x16x32_bf16 v[44:47], v[52:55], v[172:175], v[16:19]\n"
    "v_cvt_pk_bf16_f32 v158, v28, v29\n"
    "v_cvt_pk_bf16_f32 v159, v30, v31\n"
    "ds_write_b128 v60, v[156:159] offset:49152\n"
    "v_mfma_f32_16x16x32_bf16 v[24:27], v[54:57], v[180:183], v[16:19]\n"
    "v_cvt_pk_bf16_f32 v164, v32, v33\n"
    "v_cvt_pk_bf16_f32 v165, v34, v35\n"
    "v_mfma_f32_16x16x32_bf16 v[28:31], v[52:55], v[180:183], v[16:19]\n"
    "v_cvt_pk_bf16_f32 v166, v36, v37\n"
    "v_cvt_pk_bf16_f32 v167, v38, v39\n"
    "v_add_u32 v60, 0x10000, v60\n"
    "ds_write_b128 v60, v[164:167] offset:0\n"
    "v_cvt_pk_bf16_f32 v172, v40, v41\n"
    "v_cvt_pk_bf16_f32 v173, v42, v43\n"
    "v_cvt_pk_bf16_f32 v174, v44, v45\n"
    "v_cvt_pk_bf16_f32 v175, v46, v47\n"
    "ds_write_b128 v60, v[172:175] offset:16384\n"
    "v_cvt_pk_bf16_f32 v180, v24, v25\n"
    "v_cvt_pk_bf16_f32 v181, v26, v27\n"
    "v_cvt_pk_bf16_f32 v182, v28, v29\n"
    "v_cvt_pk_bf16_f32 v183, v30, v31\n"
    "ds_write_b128 v60, v[180:183] offset:32768\n"
    "v_dot2_f32_bf16 v20, v132, v132, 0\n"
    "s_nop 2\n"
    "v_dot2c_f32_bf16 v20, v133, v133\n"
    "v_dot2c_f32_bf16 v20, v134, v134\n"
    "v_dot2c_f32_bf16 v20, v135, v135\n"
    "v_dot2c_f32_bf16 v20, v140, v140\n"
    "v_dot2c_f32_bf16 v20, v141, v141\n"
    "v_dot2c_f32_bf16 v20, v142, v142\n"
    "v_dot2c_f32_bf16 v20, v143, v143\n"
    "v_dot2c_f32_bf16 v20, v148, v148\n"
    "v_dot2c_f32_bf16 v20, v149, v149\n"
    "v_dot2c_f32_bf16 v20, v150, v150\n"
    "v_dot2c_f32_bf16 v20, v151, v151\n"
    "v_dot2c_f32_bf16 v20, v156, v156\n"
    "v_dot2c_f32_bf16 v20, v157, v157\n"
    "v_dot2c_f32_bf16 v20, v158, v158\n"
    "v_dot2c_f32_bf16 v20, v159, v159\n"
    "v_dot2c_f32_bf16 v20, v164, v164\n"
    "v_dot2c_f32_bf16 v20, v165, v165\n"
    "v_dot2c_f32_bf16 v20, v166, v166\n"
    "v_dot2c_f32_bf16 v20, v167, v167\n"
    "v_dot2c_f32_bf16 v20, v172, v172\n"
    "v_dot2c_f32_bf16 v20, v173, v173\n"
    "v_dot2c_f32_bf16 v20, v174, v174\n"
    "v_dot2c_f32_bf16 v20, v175, v175\n"
    "v_dot2c_f32_bf16 v20, v180, v180\n"
    "v_dot2c_f32_bf16 v20, v181, v181\n"
    "v_dot2c_f32_bf16 v20, v182, v182\n"
    "v_dot2c_f32_bf16 v20, v183, v183\n"
    "v_mov_b32 v22, 0x3727c5ac\n"
    "s_nop 1\n"
    "v_add_f32_dpp v20, v20, v20 quad_perm:[1,0,3,2] row_mask:0xf bank_mask:0xf\n"
    "s_nop 1\n"
    "v_add_f32_dpp v20, v20, v20 quad_perm:[2,3,0,1] row_mask:0xf bank_mask:0xf\n"
    "s_nop 1\n"
    "v_add_f32_dpp v20, v20, v20 row_half_mirror row_mask:0xf bank_mask:0xf\n"
    "v_fmamk_f32 v13, v20, 0x3b124925, v22\n"
    "s_waitcnt vmcnt(6)\n"
    "v_mfma_f32_16x16x32_bf16 v[16:19], v[48:51], v[188:191], 0\n"
    "s_waitcnt vmcnt(5)\n"
    "v_mfma_f32_16x16x32_bf16 v[16:19], v[48:51], v[196:199], v[16:19]\n"
    "s_waitcnt vmcnt(4)\n"
    "v_mfma_f32_16x16x32_bf16 v[16:19], v[48:51], v[204:207], v[16:19]\n"
    "s_waitcnt vmcnt(3)\n"
    "v_mfma_f32_16x16x32_bf16 v[16:19], v[48:51], v[212:215], v[16:19]\n"
    "s_waitcnt vmcnt(2)\n"
    "v_mfma_f32_16x16x32_bf16 v[16:19], v[48:51], v[220:223], v[16:19]\n"
    "s_waitcnt vmcnt(1)\n"
    "v_mfma_f32_16x16x32_bf16 v[16:19], v[48:51], v[228:231], v[16:19]\n"
    "s_waitcnt vmcnt(0)\n"
    "v_mfma_f32_16x16x32_bf16 v[16:19], v[48:51], v[236:239], v[16:19]\n"
    "s_nop 7\n"
    "v_add_f32_dpp v16, v16, v16 quad_perm:[1,0,3,2] row_mask:0xf bank_mask:0xf\n"
    "s_nop 1\n"
    "v_add_f32_dpp v16, v16, v16 quad_perm:[2,3,0,1] row_mask:0xf bank_mask:0xf\n"
    "s_nop 1\n"
    "v_add_f32_dpp v16, v16, v16 row_half_mirror row_mask:0xf bank_mask:0xf\n"
    "v_mul_f32 v19, 0xbb124925, v16\n"
    "v_mul_f32 v18, 0xbb124925, v16\n"
    "v_mul_f32 v17, 0xbb124925, v16\n"
    "v_mul_f32 v16, 0xbb124925, v16\n"
    "s_nop 1\n"
    "v_mfma_f32_16x16x32_bf16 v[24:27], v[54:57], v[188:191], v[16:19]\n"
    "v_mfma_f32_16x16x32_bf16 v[28:31], v[52:55], v[188:191], v[16:19]\n"
    "v_mfma_f32_16x16x32_bf16 v[32:35], v[54:57], v[196:199], v[16:19]\n"
    "v_mfma_f32_16x16x32_bf16 v[36:39], v[52:55], v[196:199], v[16:19]\n"
    "v_mfma_f32_16x16x32_bf16 v[40:43], v[54:57], v[204:207], v[16:19]\n"
    "s_nop 3\n"
    "v_cvt_pk_bf16_f32 v188, v24, v25\n"
    "v_cvt_pk_bf16_f32 v189, v26, v27\n"
    "v_mfma_f32_16x16x32_bf16 v[44:47], v[52:55], v[204:207], v[16:19]\n"
    "v_cvt_pk_bf16_f32 v190, v28, v29\n"
    "v_cvt_pk_bf16_f32 v191, v30, v31\n"
    "v_add_u32 v60, 0xffff0000, v60\n"
    "ds_write_b128 v60, v[188:191] offset:128\n"
    "v_mfma_f32_16x16x32_bf16 v[24:27], v[54:57], v[212:215], v[16:19]\n"
    "v_cvt_pk_bf16_f32 v196, v32, v33\n"
    "v_cvt_pk_bf16_f32 v197, v34, v35\n"
    "v_mfma_f32_16x16x32_bf16 v[28:31], v[52:55], v[212:215], v[16:19]\n"
    "v_cvt_pk_bf16_f32 v198, v36, v37\n"
    "v_cvt_pk_bf16_f32 v199, v38, v39\n"
    "ds_write_b128 v60, v[196:199] offset:16512\n"
    "v_mfma_f32_16x16x32_bf16 v[32:35], v[54:57], v[220:223], v[16:19]\n"
    "v_cvt_pk_bf16_f32 v204, v40, v41\n"
    "v_cvt_pk_bf16_f32 v205, v42, v43\n"
    "v_mfma_f32_16x16x32_bf16 v[36:39], v[52:55], v[220:223], v[16:19]\n"
    "v_cvt_pk_bf16_f32 v206, v44, v45\n"
    "v_cvt_pk_bf16_f32 v207, v46, v47\n"
    "ds_write_b128 v60, v[204:207] offset:32896\n"
    "v_mfma_f32_16x16x32_bf16 v[40:43], v[54:57], v[228:231], v[16:19]\n"
    "v_cvt_pk_bf16_f32 v212, v24, v25\n"
    "v_cvt_pk_bf16_f32 v213, v26, v27\n"
    "v_mfma_f32_16x16x32_bf16 v[44:47], v[52:55], v[228:231], v[16:19]\n"
    "v_cvt_pk_bf16_f32 v214, v28, v29\n"
    "v_cvt_pk_bf16_f32 v215, v30, v31\n"
    "ds_write_b128 v60, v[212:215] offset:49280\n"
    "v_mfma_f32_16x16x32_bf16 v[24:27], v[54:57], v[236:239], v[16:19]\n"
    "v_cvt_pk_bf16_f32 v220, v32, v33\n"
    "v_cvt_pk_bf16_f32 v221, v34, v35\n"
    "v_mfma_f32_16x16x32_bf16 v[28:31], v[52:55], v[236:239], v[16:19]\n"
    "s_cmp_ge_u32 s70, 2\n"
    "v_cvt_pk_bf16_f32 v222, v36, v37\n"
    "v_cvt_pk_bf16_f32 v223, v38, v39\n"
    "v_add_u32 v60, 0x10000, v60\n"
    "ds_write_b128 v60, v[220:223] offset:128\n"
    "v_cvt_pk_bf16_f32 v228, v40, v41\n"
    "v_cvt_pk_bf16_f32 v229, v42, v43\n"
    "v_cvt_pk_bf16_f32 v230, v44, v45\n"
    "v_cvt_pk_bf16_f32 v231, v46, v47\n"
    "ds_write_b128 v60, v[228:231] offset:16512\n"
    "v_cvt_pk_bf16_f32 v236, v24, v25\n"
    "v_cvt_pk_bf16_f32 v237, v26, v27\n"
    "v_cvt_pk_bf16_f32 v238, v28, v29\n"
    "v_cvt_pk_bf16_f32 v239, v30, v31\n"
    "ds_write_b128 v60, v[236:239] offset:32896\n"
    "s_cbranch_scc1 LpathY_h1\n"
    "Lpx_h1:\n"
    "ds_read_b128 v[128:131], v94 offset:0\n"
    "ds_read_b128 v[136:139], v95 offset:8192\n"
    "ds_read_b128 v[144:147], v94 offset:16384\n"
    "ds_read_b128 v[152:155], v95 offset:24576\n"
    "ds_read_b128 v[160:163], v94 offset:32768\n"
    "ds_read_b128 v[168:171], v95 offset:40960\n"
    "ds_read_b128 v[176:179], v94 offset:49152\n"
    "ds_read_b128 v[184:187], v95 offset:57344\n"
    "v_add_u32 v94, 0x10000, v94\n"
    "ds_read_b128 v[192:195], v94 offset:0\n"
    "v_add_u32 v95, 0x10000, v95\n"
    "ds_read_b128 v[200:203], v95 offset:8192\n"
    "ds_read_b128 v[208:211], v94 offset:16384\n"
    "ds_read_b128 v[216:219], v95 offset:24576\n"
    "ds_read_b128 v[224:227], v94 offset:32768\n"
    "ds_read_b128 v[232:235], v95 offset:40960\n"
    "v_dot2_f32_bf16 v21, v188, v188, 0\n"
    "s_nop 2\n"
    "v_dot2c_f32_bf16 v21, v189, v189\n"
    "v_dot2c_f32_bf16 v21, v190, v190\n"
    "v_dot2c_f32_bf16 v21, v191, v191\n"
    "v_dot2c_f32_bf16 v21, v196, v196\n"
    "v_dot2c_f32_bf16 v21, v197, v197\n"
    "v_dot2c_f32_bf16 v21, v198, v198\n"
    "v_dot2c_f32_bf16 v21, v199, v199\n"
    "v_dot2c_f32_bf16 v21, v204, v204\n"
    "v_dot2c_f32_bf16 v21, v205, v205\n"
    "v_dot2c_f32_bf16 v21, v206, v206\n"
    "v_dot2c_f32_bf16 v21, v207, v207\n"
    "v_dot2c_f32_bf16 v21, v212, v212\n"
    "v_dot2c_f32_bf16 v21, v213, v213\n"
    "v_dot2c_f32_bf16 v21, v214, v214\n"
    "v_dot2c_f32_bf16 v21, v215, v215\n"
    "v_dot2c_f32_bf16 v21, v220, v220\n"
    "v_dot2c_f32_bf16 v21, v221, v221\n"
    "v_dot2c_f32_bf16 v21, v222, v222\n"
    "v_dot2c_f32_bf16 v21, v223, v223\n"
    "v_dot2c_f32_bf16 v21, v228, v228\n"
    "v_dot2c_f32_bf16 v21, v229, v229\n"
    "v_dot2c_f32_bf16 v21, v230, v230\n"
    "v_dot2c_f32_bf16 v21, v231, v231\n"
    "v_dot2c_f32_bf16 v21, v236, v236\n"
    "v_dot2c_f32_bf16 v21, v237, v237\n"
    "v_dot2c_f32_bf16 v21, v238, v238\n"
    "v_dot2c_f32_bf16 v21, v239, v239\n"
    "v_mov_b32 v22, 0x3727c5ac\n"
    "s_nop 1\n"
    "v_add_f32_dpp v21, v21, v21 quad_perm:[1,0,3,2] row_mask:0xf bank_mask:0xf\n"
    "s_nop 1\n"
    "v_add_f32_dpp v21, v21, v21 quad_perm:[2,3,0,1] row_mask:0xf bank_mask:0xf\n"
    "s_nop 1\n"
    "v_add_f32_dpp v21, v21, v21 row_half_mirror row_mask:0xf bank_mask:0xf\n"
    "v_fmamk_f32 v14, v21, 0x3b124925, v22\n"
    "s_add_u32 m0, s63, 0x4000\n"
    "s_add_u32 s6, s6, s5\n"
    "buffer_load_dwordx4 v2, s[32:35], s6 offen lds\n"
    "s_add_u32 m0, s63, 0x5000\n"
    "s_add_u32 s64, s6, 86016\n"
    "buffer_load_dwordx4 v2, s[32:35], s64 offen lds\n"
    "s_add_u32 m0, s63, 0x6000\n"
    "s_add_u32 s6, s6, s4\n"
    "buffer_load_dwordx4 v2, s[32:35], s6 offen lds\n"
    "s_add_u32 m0, s63, 0x7000\n"
    "s_add_u32 s64, s6, 86016\n"
    "buffer_load_dwordx4 v2, s[32:35], s64 offen lds\n"
    "s_add_u32 m0, s63, 0x8000\n"
    "s_add_u32 s6, s6, s5\n"
    "buffer_load_dwordx4 v2, s[32:35], s6 offen lds\n"
    "s_add_u32 m0, s63, 0x9000\n"
    "s_add_u32 s64, s6, 86016\n"
    "buffer_load_dwordx4 v2, s[32:35], s64 offen lds\n"
    "s_add_u32 m0, s63, 0xa000\n"
    "s_add_u32 s6, s6, s4\n"
    "buffer_load_dwordx4 v2, s[32:35], s6 offen lds\n"
    "s_add_u32 m0, s63, 0xb000\n"
    "s_add_u32 s64, s6, 86016\n"
    "buffer_load_dwordx4 v2, s[32:35], s64 offen lds\n"
    "s_waitcnt lgkmcnt(14)\n"
    "s_barrier\n"
    "s_setprio 2\n"
    "ds_read_b64_tr_b16 v[48:49], v6 offset:0\n"
    "ds_read_b64_tr_b16 v[50:51], v6 offset:512\n"
    "ds_read_b128 v[132:135], v4 offset:0\n"
    "ds_read_b64_tr_b16 v[52:53], v7 offset:0\n"
    "ds_read_b64_tr_b16 v[54:55], v7 offset:512\n"
    "ds_read_b64_tr_b16 v[56:57], v6 offset:4096\n"
    "ds_read_b64_tr_b16 v[58:59], v6 offset:4608\n"
    "ds_read_b128 v[140:143], v12 offset:8192\n"
    "ds_read_b64_tr_b16 v[60:61], v7 offset:4096\n"
    "ds_read_b64_tr_b16 v[62:63], v7 offset:4608\n"
    "s_waitcnt lgkmcnt(8)\n"
    "v_mfma_f32_16x16x32_bf16 v[16:19], v[128:131], v[48:51], 0\n"
    "s_waitcnt lgkmcnt(5)\n"
    "ds_read_b128 v[148:151], v4 offset:16384\n"
    "ds_read_b64_tr_b16 v[64:65], v6 offset:8192\n"
    "v_mfma_f32_16x16x32_bf16 v[24:27], v[128:131], v[52:55], 0\n"
    "ds_read_b64_tr_b16 v[66:67], v6 offset:8704\n"
    "v_mfma_f32_16x16x32_bf16 v[28:31], v[132:135], v[52:55], 0\n"
    "ds_read_b128 v[156:159], v12 offset:24576\n"
    "ds_read_b64_tr_b16 v[68:69], v7 offset:8192\n"
    "v_mfma_f32_16x16x32_bf16 v[20:23], v[132:135], v[48:51], 0\n"
    "s_waitcnt lgkmcnt(8)\n"
    "ds_read_b64_tr_b16 v[70:71], v7 offset:8704\n"
    "v_mfma_f32_16x16x32_bf16 v[16:19], v[136:139], v[56:59], v[16:19]\n"
    "s_setprio 1\n"
    "ds_read_b128 v[164:167], v4 offset:32768\n"
    "ds_read_b64_tr_b16 v[48:49], v6 offset:12288\n"
    "buffer_load_dwordx2 v[252:253], off, s[56:59], 0 sc1\n"
    "s_waitcnt lgkmcnt(8)\n"
    "v_mfma_f32_16x16x32_bf16 v[24:27], v[136:139], v[60:63], v[24:27]\n"
    "ds_read_b64_tr_b16 v[50:51], v6 offset:12800\n"
    "v_mfma_f32_16x16x32_bf16 v[28:31], v[140:143], v[60:63], v[28:31]\n"
    "ds_read_b128 v[172:175], v12 offset:40960\n"
    "ds_read_b64_tr_b16 v[52:53], v7 offset:12288\n"
    "v_mfma_f32_16x16x32_bf16 v[20:23], v[140:143], v[56:59], v[20:23]\n"
    "s_setprio 0\n"
    "ds_read_b64_tr_b16 v[54:55], v7 offset:12800\n"
    "s_waitcnt vmcnt(7)\n"
    "s_barrier\n"
    "s_setprio 2\n"
    "s_waitcnt lgkmcnt(9)\n"
    "v_mfma_f32_16x16x32_bf16 v[16:19], v[144:147], v[64:67], v[16:19]\n"
    "s_waitcnt lgkmcnt(6)\n"
    "ds_read_b128 v[180:183], v4 offset:49152\n"
    "ds_read_b64_tr_b16 v[56:57], v6 offset:16384\n"
    "v_mfma_f32_16x16x32_bf16 v[24:27], v[144:147], v[68:71], v[24:27]\n"
    "ds_read_b64_tr_b16 v[58:59], v6 offset:16896\n"
    "v_mfma_f32_16x16x32_bf16 v[28:31], v[148:151], v[68:71], v[28:31]\n"
    "ds_read_b64_tr_b16 v[60:61], v7 offset:16384\n"
    "v_mfma_f32_16x16x32_bf16 v[20:23], v[148:151], v[64:67], v[20:23]\n"
    "s_waitcnt lgkmcnt(7)\n"
    "ds_read_b64_tr_b16 v[62:63], v7 offset:16896\n"
    "ds_write_b32 v73, v13 offset:57344\n"
    "v_mfma_f32_16x16x32_bf16 v[16:19], v[152:155], v[48:51], v[16:19]\n"
    "s_setprio 1\n"
    "ds_read_b128 v[188:191], v12 offset:57344\n"
    "ds_read_b64_tr_b16 v[64:65], v6 offset:20480\n"
    "ds_write_b32 v73, v14 offset:57376\n"
    "s_waitcnt lgkmcnt(9)\n"
    "v_mfma_f32_16x16x32_bf16 v[24:27], v[152:155], v[52:55], v[24:27]\n"
    "ds_read_b64_tr_b16 v[66:67], v6 offset:20992\n"
    "v_mfma_f32_16x16x32_bf16 v[28:31], v[156:159], v[52:55], v[28:31]\n"
    "ds_read_b64_tr_b16 v[68:69], v7 offset:20480\n"
    "v_mfma_f32_16x16x32_bf16 v[20:23], v[156:159], v[48:51], v[20:23]\n"
    "s_setprio 0\n"
    "ds_read_b64_tr_b16 v[70:71], v7 offset:20992\n"
    "s_waitcnt vmcnt(1)\n"
    "s_barrier\n"
    "s_setprio 2\n"
    "s_waitcnt lgkmcnt(9)\n"
    "v_mfma_f32_16x16x32_bf16 v[16:19], v[160:163], v[56:59], v[16:19]\n"
    "s_waitcnt lgkmcnt(7)\n"
    "v_add_u32 v4, 0x10000, v4\n"
    "ds_read_b128 v[196:199], v4 offset:0\n"
    "ds_read_b64_tr_b16 v[48:49], v6 offset:24576\n"
    "v_mfma_f32_16x16x32_bf16 v[24:27], v[160:163], v[60:63], v[24:27]\n"
    "ds_read_b64_tr_b16 v[50:51], v6 offset:25088\n"
    "v_mfma_f32_16x16x32_bf16 v[28:31], v[164:167], v[60:63], v[28:31]\n"
    "v_add_u32 v12, 0x10000, v12\n"
    "ds_read_b128 v[204:207], v12 offset:8192\n"
    "ds_read_b64_tr_b16 v[52:53], v7 offset:24576\n"
    "v_mfma_f32_16x16x32_bf16 v[20:23], v[164:167], v[56:59], v[20:23]\n"
    "s_waitcnt lgkmcnt(7)\n"
    "ds_read_b64_tr_b16 v[54:55], v7 offset:25088\n"
    "v_mfma_f32_16x16x32_bf16 v[16:19], v[168:171], v[64:67], v[16:19]\n"
    "s_waitcnt lgkmcnt(6)\n"
    "ds_read_b128 v[212:215], v4 offset:16384\n"
    "ds_read_b64_tr_b16 v[56:57], v6 offset:28672\n"
    "v_mfma_f32_16x16x32_bf16 v[24:27], v[168:171], v[68:71], v[24:27]\n"
    "ds_read_b64_tr_b16 v[58:59], v6 offset:29184\n"
    "v_mfma_f32_16x16x32_bf16 v[28:31], v[172:175], v[68:71], v[28:31]\n"
    "ds_read_b128 v[220:223], v12 offset:24576\n"
    "ds_read_b64_tr_b16 v[60:61], v7 offset:28672\n"
    "v_mfma_f32_16x16x32_bf16 v[20:23], v[172:175], v[64:67], v[20:23]\n"
    "s_waitcnt lgkmcnt(8)\n"
    "ds_read_b64_tr_b16 v[62:63], v7 offset:29184\n"
    "v_mfma_f32_16x16x32_bf16 v[16:19], v[176:179], v[48:51], v[16:19]\n"
    "s_waitcnt lgkmcnt(6)\n"
    "ds_read_b128 v[228:231], v4 offset:32768\n"
    "ds_read_b64_tr_b16 v[64:65], v6 offset:32768\n"
    "v_mfma_f32_16x16x32_bf16 v[24:27], v[176:179], v[52:55], v[24:27]\n"
    "ds_read_b64_tr_b16 v[66:67], v6 offset:33280\n"
    "v_mfma_f32_16x16x32_bf16 v[28:31], v[180:183], v[52:55], v[28:31]\n"
    "ds_read_b64_tr_b16 v[68:69], v7 offset:32768\n"
    "v_mfma_f32_16x16x32_bf16 v[20:23], v[180:183], v[48:51], v[20:23]\n"
    "s_waitcnt lgkmcnt(7)\n"
    "ds_read_b64_tr_b16 v[70:71], v7 offset:33280\n"
    "v_mfma_f32_16x16x32_bf16 v[16:19], v[184:187], v[56:59], v[16:19]\n"
    "s_waitcnt lgkmcnt(5)\n"
    "ds_read_b128 v[236:239], v12 offset:40960\n"
    "ds_read_b64_tr_b16 v[48:49], v6 offset:36864\n"
    "v_mfma_f32_16x16x32_bf16 v[24:27], v[184:187], v[60:63], v[24:27]\n"
    "s_setprio 1\n"
    "ds_read_b64_tr_b16 v[50:51], v6 offset:37376\n"
    "v_mfma_f32_16x16x32_bf16 v[28:31], v[188:191], v[60:63], v[28:31]\n"
    "ds_read_b64_tr_b16 v[52:53], v7 offset:36864\n"
    "v_mfma_f32_16x16x32_bf16 v[20:23], v[188:191], v[56:59], v[20:23]\n"
    "s_waitcnt lgkmcnt(4)\n"
    "ds_read_b64_tr_b16 v[54:55], v7 offset:37376\n"
    "v_mfma_f32_16x16x32_bf16 v[16:19], v[192:195], v[64:67], v[16:19]\n"
    "ds_read_b64_tr_b16 v[56:57], v6 offset:40960\n"
    "v_mfma_f32_16x16x32_bf16 v[24:27], v[192:195], v[68:71], v[24:27]\n"
    "ds_read_b64_tr_b16 v[58:59], v6 offset:41472\n"
    "v_mfma_f32_16x16x32_bf16 v[28:31], v[196:199], v[68:71], v[28:31]\n"
    "ds_read_b64_tr_b16 v[60:61], v7 offset:40960\n"
    "v_mfma_f32_16x16x32_bf16 v[20:23], v[196:199], v[64:67], v[20:23]\n"
    "Lst1_0:\n"
    "s_cbranch_execz Lr1_0\n"
    "s_waitcnt lgkmcnt(3)\n"
    "ds_read_b64_tr_b16 v[62:63], v7 offset:41472\n"
    "v_mfma_f32_16x16x32_bf16 v[16:19], v[200:203], v[48:51], v[16:19]\n"
    "ds_read_b64_tr_b16 v[64:65], v6 offset:45056\n"
    "v_mfma_f32_16x16x32_bf16 v[24:27], v[200:203], v[52:55], v[24:27]\n"
    "ds_read_b64_tr_b16 v[66:67], v6 offset:45568\n"
    "v_mfma_f32_16x16x32_bf16 v[28:31], v[204:207], v[52:55], v[28:31]\n"
    "s_setprio 0\n"
    "ds_read_b64_tr_b16 v[68:69], v7 offset:45056\n"
    "v_mfma_f32_16x16x32_bf16 v[20:23], v[204:207], v[48:51], v[20:23]\n"
    "ds_read_b64_tr_b16 v[70:71], v7 offset:45568\n"
    "s_barrier\n"
    "s_setprio 2\n"
    "s_waitcnt lgkmcnt(4)\n"
    "v_mfma_f32_16x16x32_bf16 v[16:19], v[208:211], v[56:59], v[16:19]\n"
    "ds_read_b64_tr_b16 v[48:49], v6 offset:49152\n"
    "ds_read_b32 v114, v74 offset:57344\n"
    "v_mfma_f32_16x16x32_bf16 v[24:27], v[208:211], v[60:63], v[24:27]\n"
    "ds_read_b64_tr_b16 v[50:51], v6 offset:49664\n"
    "ds_read_b32 v115, v75 offset:57344\n"
    "v_mfma_f32_16x16x32_bf16 v[28:31], v[212:215], v[60:63], v[28:31]\n"
    "ds_read_b64_tr_b16 v[52:53], v7 offset:49152\n"
    "ds_read_b128 v[120:123], v76 offset:57344\n"
    "v_mfma_f32_16x16x32_bf16 v[20:23], v[212:215], v[56:59], v[20:23]\n"
    "s_waitcnt lgkmcnt(6)\n"
    "ds_read_b64_tr_b16 v[54:55], v7 offset:49664\n"
    "ds_read_b128 v[124:127], v77 offset:57344\n"
    "v_mfma_f32_16x16x32_bf16 v[16:19], v[216:219], v[64:67], v[16:19]\n"
    "s_setprio 1\n"
    "ds_read_b64_tr_b16 v[56:57], v6 offset:53248\n"
    "v_mfma_f32_16x16x32_bf16 v[24:27], v[216:219], v[68:71], v[24:27]\n"
    "ds_read_b64_tr_b16 v[58:59], v6 offset:53760\n"
    "v_mfma_f32_16x16x32_bf16 v[28:31], v[220:223], v[68:71], v[28:31]\n"
    "ds_read_b64_tr_b16 v[60:61], v7 offset:53248\n"
    "v_mfma_f32_16x16x32_bf16 v[20:23], v[220:223], v[64:67], v[20:23]\n"
    "s_setprio 0\n"
    "ds_read_b64_tr_b16 v[62:63], v7 offset:53760\n"
    "s_barrier\n"
    "s_setprio 2\n"
    "s_waitcnt lgkmcnt(5)\n"
    "v_mfma_f32_16x16x32_bf16 v[16:19], v[224:227], v[48:51], v[16:19]\n"
    "v_add_u32 v8, 0x10000, v8\n"
    "ds_read_b64_tr_b16 v[64:65], v8 offset:0\n"
    "v_rsq_f32 v116, v114\n"
    "v_rsq_f32 v117, v115\n"
    "v_mfma_f32_16x16x32_bf16 v[24:27], v[224:227], v[52:55], v[24:27]\n"
    "ds_read_b64_tr_b16 v[66:67], v8 offset:512\n"
    "v_rsq_f32 v120, v120\n"
    "v_mfma_f32_16x16x32_bf16 v[28:31], v[228:231], v[52:55], v[28:31]\n"
    "ds_read_b64_tr_b16 v[68:69], v8 offset:8192\n"
    "v_rsq_f32 v121, v121\n"
    "v_mfma_f32_16x16x32_bf16 v[20:23], v[228:231], v[48:51], v[20:23]\n"
    "s_waitcnt lgkmcnt(3)\n"
    "ds_read_b64_tr_b16 v[70:71], v8 offset:8704\n"
    "v_rsq_f32 v122, v122\n"
    "v_mfma_f32_16x16x32_bf16 v[16:19], v[232:235], v[56:59], v[16:19]\n"
    "ds_read_b64_tr_b16 v[48:49], v8 offset:4096\n"
    "v_rsq_f32 v123, v123\n"
    "v_mfma_f32_16x16x32_bf16 v[24:27], v[232:235], v[60:63], v[24:27]\n"
    "ds_read_b64_tr_b16 v[50:51], v8 offset:4608\n"
    "v_rsq_f32 v124, v124\n"
    "v_mfma_f32_16x16x32_bf16 v[28:31], v[236:239], v[60:63], v[28:31]\n"
    "ds_read_b64_tr_b16 v[52:53], v8 offset:12288\n"
    "v_rsq_f32 v125, v125\n"
    "v_mfma_f32_16x16x32_bf16 v[20:23], v[236:239], v[56:59], v[20:23]\n"
    "s_waitcnt lgkmcnt(3)\n"
    "ds_read_b64_tr_b16 v[54:55], v8 offset:12800\n"
    "v_rsq_f32 v126, v126\n"
    "v_mfma_f32_16x16x32_bf16 v[32:35], v[64:67], v[128:131], 0\n"
    "v_add_u32 v9, 0x10000, v9\n"
    "ds_read_b64_tr_b16 v[56:57], v9 offset:0\n"
    "v_rsq_f32 v127, v127\n"
    "v_mfma_f32_16x16x32_bf16 v[40:43], v[68:71], v[128:131], 0\n"
    "ds_read_b64_tr_b16 v[58:59], v9 offset:512\n"
    "v_mul_f32 v112, 0xbfb8aa3b, v116\n"
    "v_mul_f32 v113, 0xbfb8aa3b, v117\n"
    "v_mfma_f32_16x16x32_bf16 v[44:47], v[68:71], v[132:135], 0\n"
    "ds_read_b64_tr_b16 v[60:61], v9 offset:8192\n"
    "v_mfma_f32_16x16x32_bf16 v[36:39], v[64:67], v[132:135], 0\n"
    "s_waitcnt lgkmcnt(3)\n"
    "ds_read_b64_tr_b16 v[62:63], v9 offset:8704\n"
    "v_mfma_f32_16x16x32_bf16 v[32:35], v[48:51], v[136:139], v[32:35]\n"
    "ds_read_b64_tr_b16 v[64:65], v9 offset:4096\n"
    "v_mfma_f32_16x16x32_bf16 v[40:43], v[52:55], v[136:139], v[40:43]\n"
    "ds_read_b64_tr_b16 v[66:67], v9 offset:4608\n"
    "v_mfma_f32_16x16x32_bf16 v[44:47], v[52:55], v[140:143], v[44:47]\n"
    "ds_read_b64_tr_b16 v[68:69], v9 offset:12288\n"
    "v_mfma_f32_16x16x32_bf16 v[36:39], v[48:51], v[140:143], v[36:39]\n"
    "s_waitcnt lgkmcnt(3)\n"
    "ds_read_b64_tr_b16 v[70:71], v9 offset:12800\n"
    "v_mfma_f32_16x16x32_bf16 v[96:99], v[56:59], v[128:131], 0\n"
    "ds_read_b64_tr_b16 v[48:49], v8 offset:16384\n"
    "v_mfma_f32_16x16x32_bf16 v[104:107], v[60:63], v[128:131], 0\n"
    "ds_read_b64_tr_b16 v[50:51], v8 offset:16896\n"
    "v_mfma_f32_16x16x32_bf16 v[108:111], v[60:63], v[132:135], 0\n"
    "ds_read_b64_tr_b16 v[52:53], v8 offset:24576\n"
    "v_mfma_f32_16x16x32_bf16 v[100:103], v[56:59], v[132:135], 0\n"
    "s_waitcnt lgkmcnt(3)\n"
    "ds_read_b64_tr_b16 v[54:55], v8 offset:25088\n"
    "v_mul_f32 v16, v16, v120\n"
    "v_mul_f32 v17, v17, v121\n"
    "v_mul_f32 v18, v18, v122\n"
    "v_mfma_f32_16x16x32_bf16 v[96:99], v[64:67], v[136:139], v[96:99]\n"
    "ds_read_b64_tr_b16 v[56:57], v8 offset:20480\n"
    "v_mul_f32 v19, v19, v123\n"
    "v_mul_f32 v20, v20, v124\n"
    "v_mul_f32 v21, v21, v125\n"
    "v_mfma_f32_16x16x32_bf16 v[104:107], v[68:71], v[136:139], v[104:107]\n"
    "s_setprio 1\n"
    "ds_read_b64_tr_b16 v[58:59], v8 offset:20992\n"
    "v_mul_f32 v22, v22, v126\n"
    "v_mul_f32 v23, v23, v127\n"
    "v_mfma_f32_16x16x32_bf16 v[108:111], v[68:71], v[140:143], v[108:111]\n"
    "ds_read_b64_tr_b16 v[60:61], v8 offset:28672\n"
    "v_cvt_pk_bf16_f32 v80, v16, v17\n"
    "v_cvt_pk_bf16_f32 v81, v18, v19\n"
    "v_cvt_pk_bf16_f32 v82, v20, v21\n"
    "v_mfma_f32_16x16x32_bf16 v[100:103], v[64:67], v[140:143], v[100:103]\n"
    "s_waitcnt lgkmcnt(3)\n"
    "ds_read_b64_tr_b16 v[62:63], v8 offset:29184\n"
    "v_cvt_pk_bf16_f32 v83, v22, v23\n"
    "v_permlane16_swap_b32 v80, v82\n"
    "s_nop 0\n"
    "v_permlane16_swap_b32 v81, v83\n"
    "v_mfma_f32_16x16x32_bf16 v[32:35], v[48:51], v[144:147], v[32:35]\n"
    "ds_read_b64_tr_b16 v[64:65], v9 offset:16384\n"
    "v_mul_f32 v24, v24, v120\n"
    "v_mul_f32 v25, v25, v121\n"
    "v_mfma_f32_16x16x32_bf16 v[40:43], v[52:55], v[144:147], v[40:43]\n"
    "ds_read_b64_tr_b16 v[66:67], v9 offset:16896\n"
    "s_waitcnt vmcnt(0)\n"
    "v_readfirstlane_b32 s60, v252\n"
    "v_mul_f32 v26, v26, v122\n"
    "v_mul_f32 v27, v27, v123\n"
    "v_mul_f32 v28, v28, v124\n"
    "v_mfma_f32_16x16x32_bf16 v[44:47], v[52:55], v[148:151], v[44:47]\n"
    "ds_read_b64_tr_b16 v[68:69], v9 offset:24576\n"
    "v_readfirstlane_b32 s61, v253\n"
    "v_mul_f32 v29, v29, v125\n"
    "v_mul_f32 v30, v30, v126\n"
    "v_mfma_f32_16x16x32_bf16 v[36:39], v[48:51], v[148:151], v[36:39]\n"
    "ds_read_b64_tr_b16 v[70:71], v9 offset:25088\n"
    "s_add_u32 s60, s60, 0x7f4a7c15\n"
    "s_addc_u32 s61, s61, 0x9e3779b9\n"
    "v_mul_f32 v31, v31, v127\n"
    "v_cvt_pk_bf16_f32 v84, v24, v25\n"
    "v_cvt_pk_bf16_f32 v85, v26, v27\n"
    "s_waitcnt lgkmcnt(4)\n"
    "v_mfma_f32_16x16x32_bf16 v[32:35], v[56:59], v[152:155], v[32:35]\n"
    "ds_read_b64_tr_b16 v[48:49], v9 offset:20480\n"
    "v_cvt_pk_bf16_f32 v86, v28, v29\n"
    "v_cvt_pk_bf16_f32 v87, v30, v31\n"
    "s_nop 0\n"
    "v_permlane16_swap_b32 v84, v86\n"
    "v_mfma_f32_16x16x32_bf16 v[40:43], v[60:63], v[152:155], v[40:43]\n"
    "ds_read_b64_tr_b16 v[50:51], v9 offset:20992\n"
    "v_mov_b32 v252, s60\n"
    "v_permlane16_swap_b32 v85, v87\n"
    "v_cndmask_b32 v84, v84, v78, s[88:89]\n"
    "v_mfma_f32_16x16x32_bf16 v[44:47], v[60:63], v[156:159], v[44:47]\n"
    "ds_read_b64_tr_b16 v[52:53], v9 offset:28672\n"
    "v_mov_b32 v253, s61\n"
    "v_cndmask_b32 v85, v85, v78, s[88:89]\n"
    "v_cndmask_b32 v86, v86, v78, s[88:89]\n"
    "v_cndmask_b32 v87, v87, v78, s[88:89]\n"
    "v_mfma_f32_16x16x32_bf16 v[36:39], v[56:59], v[156:159], v[36:39]\n"
    "s_waitcnt lgkmcnt(3)\n"
    "ds_read_b64_tr_b16 v[54:55], v9 offset:29184\n"
    "buffer_store_dwordx4 v[80:83], v11, s[56:59], s74 offen offset:2048 sc1\n"
    "buffer_store_dwordx4 v[84:87], v11, s[56:59], s74 offen offset:2304 sc1\n"
    "v_mfma_f32_16x16x32_bf16 v[96:99], v[64:67], v[144:147], v[96:99]\n"
    "ds_read_b64_tr_b16 v[56:57], v8 offset:32768\n"
    "v_mfma_f32_16x16x32_bf16 v[104:107], v[68:71], v[144:147], v[104:107]\n"
    "s_and_b32 s65, s27, 1\n"
    "ds_read_b64_tr_b16 v[58:59], v8 offset:33280\n"
    "s_lshl_b32 s66, s26, 1\n"
    "v_mfma_f32_16x16x32_bf16 v[108:111], v[68:71], v[148:151], v[108:111]\n"
    "s_add_u32 s65, s65, s66\n"
    "ds_read_b64_tr_b16 v[60:61], v8 offset:40960\n"
    "s_lshr_b32 s91, s27, 1\n"
    "v_mfma_f32_16x16x32_bf16 v[100:103], v[64:67], v[148:151], v[100:103]\n"
    "s_lshl_b32 s90, s65, 18\n"
    "ds_read_b64_tr_b16 v[62:63], v8 offset:41472\n"
    "s_add_u32 s90, s90, s17\n"
    "s_waitcnt lgkmcnt(4)\n"
    "v_mfma_f32_16x16x32_bf16 v[96:99], v[48:51], v[152:155], v[96:99]\n"
    "s_add_u32 s90, s90, 0x210000\n"
    "ds_read_b64_tr_b16 v[64:65], v8 offset:36864\n"
    "s_and_b32 s66, s25, 3\n"
    "v_mfma_f32_16x16x32_bf16 v[104:107], v[52:55], v[152:155], v[104:107]\n"
    "s_setprio 0\n"
    "ds_read_b64_tr_b16 v[66:67], v8 offset:37376\n"
    "s_lshl_b32 s67, s91, 2\n"
    "s_add_u32 s67, s67, s66\n"
    "v_mfma_f32_16x16x32_bf16 v[108:111], v[52:55], v[156:159], v[108:111]\n"
    "s_lshl_b32 s92, s65, 6\n"
    "ds_read_b64_tr_b16 v[68:69], v8 offset:45056\n"
    "s_add_u32 s92, s92, s67\n"
    "v_mfma_f32_16x16x32_bf16 v[100:103], v[48:51], v[156:159], v[100:103]\n"
    "s_lshl_b32 s92, s92, 11\n"
    "ds_read_b64_tr_b16 v[70:71], v8 offset:45568\n"
    "s_add_u32 s92, s92, 0x10000\n"
    "s_barrier\n"
    "s_setprio 2\n"
    "s_waitcnt lgkmcnt(4)\n"
    "v_mfma_f32_16x16x32_bf16 v[32:35], v[56:59], v[160:163], v[32:35]\n"
    "s_lshl_b32 s93, s67, 15\n"
    "ds_read_b64_tr_b16 v[48:49], v8 offset:49152\n"
    "s_lshl_b32 s7, s65, 8\n"
    "v_mfma_f32_16x16x32_bf16 v[40:43], v[60:63], v[160:163], v[40:43]\n"
    "s_add_u32 s7, s7, s93\n"
    "ds_read_b64_tr_b16 v[50:51], v8 offset:49664\n"
    "s_add_u32 s7, s7, 0xd13000\n"
    "v_mfma_f32_16x16x32_bf16 v[44:47], v[60:63], v[164:167], v[44:47]\n"
    "s_mul_i32 s66, s65, 768\n"
    "ds_read_b64_tr_b16 v[52:53], v8 offset:57344\n"
    "s_add_u32 s93, s93, s66\n"
    "v_mfma_f32_16x16x32_bf16 v[36:39], v[56:59], v[164:167], v[36:39]\n"
    "s_add_u32 s93, s93, 0xd10000\n"
    "ds_read_b64_tr_b16 v[54:55], v8 offset:57856\n"
    "s_lshl_b32 s94, s67, 4\n"
    "s_waitcnt lgkmcnt(4)\n"
    "v_mfma_f32_16x16x32_bf16 v[32:35], v[64:67], v[168:171], v[32:35]\n"
    "s_add_u32 s94, s94, s65\n"
    "ds_read_b64_tr_b16 v[56:57], v8 offset:53248\n"
    "s_lshl_b32 s94, s94, 3\n"
    "v_mfma_f32_16x16x32_bf16 v[40:43], v[68:71], v[168:171], v[40:43]\n"
    "s_add_u32 s94, s94, 8192\n"
    "ds_read_b64_tr_b16 v[58:59], v8 offset:53760\n"
    "s_lshl_b32 s65, s26, 7\n"
    "v_mfma_f32_16x16x32_bf16 v[44:47], v[68:71], v[172:175], v[44:47]\n"
    "s_lshl_b32 s66, s91, 3\n"
    "ds_read_b64_tr_b16 v[60:61], v8 offset:61440\n"
    "s_add_u32 s66, s66, s65\n"
    "v_mfma_f32_16x16x32_bf16 v[36:39], v[64:67], v[172:175], v[36:39]\n"
    "s_add_u32 s66, s66, 256\n"
    "ds_read_b64_tr_b16 v[62:63], v8 offset:61952\n"
    "s_waitcnt lgkmcnt(4)\n"
    "v_mfma_f32_16x16x32_bf16 v[32:35], v[48:51], v[176:179], v[32:35]\n"
    "s_add_u32 s65, s65, 1280\n"
    "v_add_u32 v8, 0xffff0000, v8\n"
    "ds_read_b64_tr_b16 v[64:65], v8 offset:0\n"
    "v_and_b32 v250, 15, v0\n"
    "v_mfma_f32_16x16x32_bf16 v[40:43], v[52:55], v[176:179], v[40:43]\n"
    "ds_read_b64_tr_b16 v[66:67], v8 offset:512\n"
    "v_and_b32 v251, 16, v0\n"
    "v_lshlrev_b32 v251, 6, v251\n"
    "v_mfma_f32_16x16x32_bf16 v[44:47], v[52:55], v[180:183], v[44:47]\n"
    "ds_read_b64_tr_b16 v[68:69], v8 offset:8192\n"
    "v_lshl_add_u32 v250, v250, 3, v251\n"
    "v_add_u32 v250, s65, v250\n"
    "v_mfma_f32_16x16x32_bf16 v[36:39], v[48:51], v[180:183], v[36:39]\n"
    "s_waitcnt lgkmcnt(3)\n"
    "ds_read_b64_tr_b16 v[70:71], v8 offset:8704\n"
    "v_mov_b32 v251, s66\n"
    "v_cmp_gt_u32 vcc, 32, v0\n"
    "v_mfma_f32_16x16x32_bf16 v[32:35], v[56:59], v[184:187], v[32:35]\n"
    "s_and_b32 s65, s27, 3\n"
    "ds_read_b64_tr_b16 v[48:49], v8 offset:4096\n"
    "v_cndmask_b32 v248, v251, v250, vcc\n"
    "v_mfma_f32_16x16x32_bf16 v[40:43], v[60:63], v[184:187], v[40:43]\n"
    "s_lshl_b32 s66, s26, 2\n"
    "ds_read_b64_tr_b16 v[50:51], v8 offset:4608\n"
    "s_add_u32 s65, s65, s66\n"
    "v_mfma_f32_16x16x32_bf16 v[44:47], v[60:63], v[188:191], v[44:47]\n"
    "s_lshr_b32 s97, s27, 2\n"
    "ds_read_b64_tr_b16 v[52:53], v8 offset:12288\n"
    "s_lshl_b32 s97, s97, 7\n"
    "v_mfma_f32_16x16x32_bf16 v[36:39], v[56:59], v[188:191], v[36:39]\n"
    "s_mul_i32 s99, s65, 57344\n"
    "ds_read_b64_tr_b16 v[54:55], v8 offset:12800\n"
    "s_add_u32 s99, s99, s97\n"
    "s_waitcnt lgkmcnt(4)\n"
    "v_mfma_f32_16x16x32_bf16 v[32:35], v[64:67], v[192:195], v[32:35]\n"
    "s_lshr_b32 s66, s65, 1\n"
    "ds_read_b64_tr_b16 v[56:57], v9 offset:32768\n"
    "s_lshl_b32 s66, s66, 7\n"
    "v_mfma_f32_16x16x32_bf16 v[40:43], v[68:71], v[192:195], v[40:43]\n"
    "s_setprio 1\n"
    "ds_read_b64_tr_b16 v[58:59], v9 offset:33280\n"
    "s_add_u32 s66, s66, 4096\n"
    "v_and_b32 v12, 15, v0\n"
    "v_mfma_f32_16x16x32_bf16 v[44:47], v[68:71], v[196:199], v[44:47]\n"
    "s_lshr_b32 s67, s25, 1\n"
    "ds_read_b64_tr_b16 v[60:61], v9 offset:40960\n"
    "v_lshl_add_u32 v75, v12, 3, s66\n"
    "v_mfma_f32_16x16x32_bf16 v[36:39], v[64:67], v[196:199], v[36:39]\n"
    "s_lshl_b32 s65, s65, 1\n"
    "ds_read_b64_tr_b16 v[62:63], v9 offset:41472\n"
    "s_and_b32 s66, s25, 1\n"
    "s_waitcnt lgkmcnt(4)\n"
    "v_mfma_f32_16x16x32_bf16 v[32:35], v[48:51], v[200:203], v[32:35]\n"
    "s_add_u32 s65, s65, s66\n"
    "ds_read_b64_tr_b16 v[64:65], v9 offset:36864\n"
    "s_lshl_b32 s96, s65, 7\n"
    "v_mfma_f32_16x16x32_bf16 v[40:43], v[52:55], v[200:203], v[40:43]\n"
    "s_add_u32 s96, s96, 8192\n"
    "ds_read_b64_tr_b16 v[66:67], v9 offset:37376\n"
    "v_mfma_f32_16x16x32_bf16 v[44:47], v[52:55], v[204:207], v[44:47]\n"
    "ds_read_b64_tr_b16 v[68:69], v9 offset:45056\n"
    "v_lshl_add_u32 v7, v12, 3, s96\n"
    "v_and_b32 v12, 3, v0\n"
    "v_mfma_f32_16x16x32_bf16 v[36:39], v[48:51], v[204:207], v[36:39]\n"
    "s_mul_i32 s96, s65, 114688\n"
    "ds_read_b64_tr_b16 v[70:71], v9 offset:45568\n"
    "s_mul_i32 s98, s67, 14336\n"
    "s_waitcnt lgkmcnt(4)\n"
    "v_mfma_f32_16x16x32_bf16 v[96:99], v[56:59], v[160:163], v[96:99]\n"
    "s_add_u32 s96, s96, s98\n"
    "ds_read_b64_tr_b16 v[48:49], v8 offset:16384\n"
    "s_add_u32 s96, s96, 0x610000\n"
    "v_mfma_f32_16x16x32_bf16 v[104:107], v[60:63], v[160:163], v[104:107]\n"
    "s_add_u32 s21, s96, 57344\n"
    "ds_read_b64_tr_b16 v[50:51], v8 offset:16896\n"
    "s_mul_i32 s98, s67, 3072\n"
    "v_mfma_f32_16x16x32_bf16 v[108:111], v[60:63], v[164:167], v[108:111]\n"
    "s_lshl_b32 s65, s65, 15\n"
    "ds_read_b64_tr_b16 v[52:53], v8 offset:24576\n"
    "s_add_u32 s98, s98, s65\n"
    "v_mfma_f32_16x16x32_bf16 v[100:103], v[56:59], v[164:167], v[100:103]\n"
    "s_add_u32 s98, s98, 0xd10000\n"
    "ds_read_b64_tr_b16 v[54:55], v8 offset:25088\n"
    "v_lshrrev_b32 v13, 3, v0\n"
    "s_waitcnt lgkmcnt(4)\n"
    "v_mfma_f32_16x16x32_bf16 v[96:99], v[64:67], v[168:171], v[96:99]\n"
    "ds_read_b64_tr_b16 v[56:57], v8 offset:20480\n"
    "v_and_b32 v14, 3, v13\n"
    "v_lshlrev_b32 v14, 5, v14\n"
    "v_mfma_f32_16x16x32_bf16 v[104:107], v[68:71], v[168:171], v[104:107]\n"
    "s_setprio 0\n"
    "ds_read_b64_tr_b16 v[58:59], v8 offset:20992\n"
    "v_lshl_add_u32 v14, v12, 3, v14\n"
    "v_lshrrev_b32 v12, 2, v0\n"
    "v_mfma_f32_16x16x32_bf16 v[108:111], v[68:71], v[172:175], v[108:111]\n"
    "s_lshl_b32 s65, s67, 12\n"
    "ds_read_b64_tr_b16 v[60:61], v8 offset:28672\n"
    "v_lshlrev_b32 v12, 7, v12\n"
    "v_mfma_f32_16x16x32_bf16 v[100:103], v[64:67], v[172:175], v[100:103]\n"
    "ds_read_b64_tr_b16 v[62:63], v8 offset:29184\n"
    "v_add3_u32 v124, v12, v14, s65\n"
    "v_xor_b32 v125, 32, v124\n"
    "s_barrier\n"
    "s_setprio 2\n"
    "s_waitcnt lgkmcnt(4)\n"
    "v_mfma_f32_16x16x32_bf16 v[32:35], v[48:51], v[208:211], v[32:35]\n"
    "ds_read_b64_tr_b16 v[64:65], v8 offset:32768\n"
    "v_xor_b32 v126, 64, v124\n"
    "v_xor_b32 v127, 0x60, v124\n"
    "v_mfma_f32_16x16x32_bf16 v[40:43], v[52:55], v[208:211], v[40:43]\n"
    "ds_read_b64_tr_b16 v[66:67], v8 offset:33280\n"
    "v_and_b32 v12, 7, v0\n"
    "v_and_b32 v14, 6, v13\n"
    "v_mfma_f32_16x16x32_bf16 v[44:47], v[52:55], v[212:215], v[44:47]\n"
    "ds_read_b64_tr_b16 v[68:69], v8 offset:40960\n"
    "v_xor_b32 v14, v14, v12\n"
    "v_mul_u32_u24 v116, 896, v13\n"
    "v_mfma_f32_16x16x32_bf16 v[36:39], v[48:51], v[212:215], v[36:39]\n"
    "s_and_b32 s67, s67, 1\n"
    "ds_read_b64_tr_b16 v[70:71], v8 offset:41472\n"
    "v_lshl_add_u32 v14, v14, 4, v116\n"
    "s_waitcnt lgkmcnt(4)\n"
    "v_mfma_f32_16x16x32_bf16 v[32:35], v[56:59], v[216:219], v[32:35]\n"
    "s_mul_i32 s65, s67, 448\n"
    "ds_read_b64_tr_b16 v[48:49], v8 offset:36864\n"
    "v_mfma_f32_16x16x32_bf16 v[40:43], v[60:63], v[216:219], v[40:43]\n"
    "s_lshl_b32 s66, s66, 3\n"
    "ds_read_b64_tr_b16 v[50:51], v8 offset:37376\n"
    "s_add_u32 s65, s65, s66\n"
    "v_mfma_f32_16x16x32_bf16 v[44:47], v[60:63], v[220:223], v[44:47]\n"
    "s_add_u32 s65, s65, s66\n"
    "ds_read_b64_tr_b16 v[52:53], v8 offset:45056\n"
    "s_mul_i32 s65, s65, 896\n"
    "v_mfma_f32_16x16x32_bf16 v[36:39], v[56:59], v[220:223], v[36:39]\n"
    "s_waitcnt lgkmcnt(3)\n"
    "ds_read_b64_tr_b16 v[54:55], v8 offset:45568\n"
    "v_add_u32 v2, s65, v14\n"
    "s_mul_i32 s65, s67, 112\n"
    "v_mfma_f32_16x16x32_bf16 v[32:35], v[64:67], v[224:227], v[32:35]\n"
    "s_add_u32 s65, s65, s66\n"
    "ds_read_b64_tr_b16 v[56:57], v9 offset:49152\n"
    "v_mfma_f32_16x16x32_bf16 v[40:43], v[68:71], v[224:227], v[40:43]\n"
    "s_mul_i32 s65, s65, 896\n"
    "ds_read_b64_tr_b16 v[58:59], v9 offset:49664\n"
    "v_add_u32 v4, s65, v14\n"
    "v_mfma_f32_16x16x32_bf16 v[44:47], v[68:71], v[228:231], v[44:47]\n"
    "s_mul_i32 s65, s25, 7168\n"
    "ds_read_b64_tr_b16 v[60:61], v9 offset:57344\n"
    "v_lshl_add_u32 v14, v12, 4, v116\n"
    "v_add_u32 v6, s65, v14\n"
    "v_mfma_f32_16x16x32_bf16 v[36:39], v[64:67], v[228:231], v[36:39]\n"
    "s_lshl_b32 s65, s79, 13\n"
    "ds_read_b64_tr_b16 v[62:63], v9 offset:57856\n"
    "v_add_u32 v78, s65, v1\n"
    "s_waitcnt lgkmcnt(4)\n"
    "v_mfma_f32_16x16x32_bf16 v[32:35], v[48:51], v[232:235], v[32:35]\n"
    "ds_read_b64_tr_b16 v[64:65], v9 offset:53248\n"
    "v_add_u32 v73, 0xc000, v78\n"
    "v_add_u32 v74, 0xc000, v73\n"
    "v_mfma_f32_16x16x32_bf16 v[40:43], v[52:55], v[232:235], v[40:43]\n"
    "s_and_b32 s65, s25, 3\n"
    "ds_read_b64_tr_b16 v[66:67], v9 offset:53760\n"
    "s_lshl_b32 s65, s65, 12\n"
    "v_mfma_f32_16x16x32_bf16 v[44:47], v[52:55], v[236:239], v[44:47]\n"
    "s_add_u32 s65, s65, 0x18000\n"
    "ds_read_b64_tr_b16 v[68:69], v9 offset:61440\n"
    "v_add_u32 v76, s65, v1\n"
    "v_mfma_f32_16x16x32_bf16 v[36:39], v[48:51], v[236:239], v[36:39]\n"
    "s_and_b32 s65, s27, 1\n"
    "ds_read_b64_tr_b16 v[70:71], v9 offset:61952\n"
    "v_and_b32 v13, 1, v13\n"
    "s_waitcnt lgkmcnt(4)\n"
    "v_mfma_f32_16x16x32_bf16 v[96:99], v[56:59], v[176:179], v[96:99]\n"
    "v_add_u32 v9, 0xffff0000, v9\n"
    "ds_read_b64_tr_b16 v[48:49], v9 offset:0\n"
    "v_xor_b32 v13, s65, v13\n"
    "v_lshrrev_b32 v14, 4, v0\n"
    "v_mfma_f32_16x16x32_bf16 v[104:107], v[60:63], v[176:179], v[104:107]\n"
    "ds_read_b64_tr_b16 v[50:51], v9 offset:512\n"
    "v_lshlrev_b32 v12, 4, v12\n"
    "v_lshl_add_u32 v12, v14, 7, v12\n"
    "v_mfma_f32_16x16x32_bf16 v[108:111], v[60:63], v[180:183], v[108:111]\n"
    "ds_read_b64_tr_b16 v[52:53], v9 offset:8192\n"
    "v_lshl_add_u32 v77, v13, 14, v12\n"
    "v_mfma_f32_16x16x32_bf16 v[100:103], v[56:59], v[180:183], v[100:103]\n"
    "s_waitcnt lgkmcnt(3)\n"
    "ds_read_b64_tr_b16 v[54:55], v9 offset:8704\n"
    "v_mul_f32 v88, v32, v112\n"
    "v_mul_f32 v89, v33, v112\n"
    "v_mfma_f32_16x16x32_bf16 v[96:99], v[64:67], v[184:187], v[96:99]\n"
    "ds_read_b64_tr_b16 v[56:57], v9 offset:4096\n"
    "v_mul_f32 v90, v34, v112\n"
    "v_mul_f32 v91, v35, v112\n"
    "v_mfma_f32_16x16x32_bf16 v[104:107], v[68:71], v[184:187], v[104:107]\n"
    "s_setprio 1\n"
    "ds_read_b64_tr_b16 v[58:59], v9 offset:4608\n"
    "v_exp_f32 v88, v88\n"
    "v_mfma_f32_16x16x32_bf16 v[108:111], v[68:71], v[188:191], v[108:111]\n"
    "ds_read_b64_tr_b16 v[60:61], v9 offset:12288\n"
    "v_mul_f32 v40, v32, v40\n"
    "v_mfma_f32_16x16x32_bf16 v[100:103], v[64:67], v[188:191], v[100:103]\n"
    "s_waitcnt lgkmcnt(3)\n"
    "ds_read_b64_tr_b16 v[62:63], v9 offset:12800\n"
    "v_exp_f32 v89, v89\n"
    "v_mfma_f32_16x16x32_bf16 v[96:99], v[48:51], v[192:195], v[96:99]\n"
    "ds_read_b64_tr_b16 v[64:65], v9 offset:16384\n"
    "v_mul_f32 v41, v33, v41\n"
    "v_mfma_f32_16x16x32_bf16 v[104:107], v[52:55], v[192:195], v[104:107]\n"
    "ds_read_b64_tr_b16 v[66:67], v9 offset:16896\n"
    "v_exp_f32 v90, v90\n"
    "v_mfma_f32_16x16x32_bf16 v[108:111], v[52:55], v[196:199], v[108:111]\n"
    "ds_read_b64_tr_b16 v[68:69], v9 offset:24576\n"
    "v_mul_f32 v42, v34, v42\n"
    "v_mfma_f32_16x16x32_bf16 v[100:103], v[48:51], v[196:199], v[100:103]\n"
    "s_waitcnt lgkmcnt(3)\n"
    "ds_read_b64_tr_b16 v[70:71], v9 offset:25088\n"
    "v_exp_f32 v91, v91\n"
    "v_mfma_f32_16x16x32_bf16 v[96:99], v[56:59], v[200:203], v[96:99]\n"
    "ds_read_b64_tr_b16 v[48:49], v9 offset:20480\n"
    "v_mul_f32 v43, v35, v43\n"
    "v_fma_f32 v88, v88, v114, v114\n"
    "v_mfma_f32_16x16x32_bf16 v[104:107], v[60:63], v[200:203], v[104:107]\n"
    "ds_read_b64_tr_b16 v[50:51], v9 offset:20992\n"
    "v_fma_f32 v89, v89, v114, v114\n"
    "v_mfma_f32_16x16x32_bf16 v[108:111], v[60:63], v[204:207], v[108:111]\n"
    "ds_read_b64_tr_b16 v[52:53], v9 offset:28672\n"
    "v_fma_f32 v90, v90, v114, v114\n"
    "v_fma_f32 v91, v91, v114, v114\n"
    "v_mfma_f32_16x16x32_bf16 v[100:103], v[56:59], v[204:207], v[100:103]\n"
    "s_waitcnt lgkmcnt(3)\n"
    "ds_read_b64_tr_b16 v[54:55], v9 offset:29184\n"
    "v_rcp_f32 v88, v88\n"
    "v_mfma_f32_16x16x32_bf16 v[96:99], v[64:67], v[208:211], v[96:99]\n"
    "ds_read_b64_tr_b16 v[56:57], v9 offset:32768\n"
    "v_rcp_f32 v89, v89\n"
    "v_mfma_f32_16x16x32_bf16 v[104:107], v[68:71], v[208:211], v[104:107]\n"
    "ds_read_b64_tr_b16 v[58:59], v9 offset:33280\n"
    "v_rcp_f32 v90, v90\n"
    "v_mfma_f32_16x16x32_bf16 v[108:111], v[68:71], v[212:215], v[108:111]\n"
    "ds_read_b64_tr_b16 v[60:61], v9 offset:40960\n"
    "v_mfma_f32_16x16x32_bf16 v[100:103], v[64:67], v[212:215], v[100:103]\n"
    "s_waitcnt lgkmcnt(3)\n"
    "ds_read_b64_tr_b16 v[62:63], v9 offset:41472\n"
    "v_rcp_f32 v91, v91\n"
    "v_mfma_f32_16x16x32_bf16 v[96:99], v[48:51], v[216:219], v[96:99]\n"
    "ds_read_b64_tr_b16 v[64:65], v9 offset:36864\n"
    "v_mul_f32 v40, v40, v88\n"
    "v_mul_f32 v41, v41, v89\n"
    "v_mfma_f32_16x16x32_bf16 v[104:107], v[52:55], v[216:219], v[104:107]\n"
    "s_setprio 0\n"
    "ds_read_b64_tr_b16 v[66:67], v9 offset:37376\n"
    "v_mul_f32 v42, v42, v90\n"
    "v_mfma_f32_16x16x32_bf16 v[108:111], v[52:55], v[220:223], v[108:111]\n"
    "ds_read_b64_tr_b16 v[68:69], v9 offset:45056\n"
    "v_mul_f32 v43, v43, v91\n"
    "v_cvt_pk_bf16_f32 v240, v40, v41\n"
    "v_mfma_f32_16x16x32_bf16 v[100:103], v[48:51], v[220:223], v[100:103]\n"
    "ds_read_b64_tr_b16 v[70:71], v9 offset:45568\n"
    "v_cvt_pk_bf16_f32 v241, v42, v43\n"
    "s_waitcnt vmcnt(0)\n"
    "s_barrier\n"
    "s_setprio 2\n"
    "s_waitcnt lgkmcnt(4)\n"
    "v_mfma_f32_16x16x32_bf16 v[96:99], v[56:59], v[224:227], v[96:99]\n"
    "s_cmp_lg_u32 s25, 4\n"
    "ds_read_b64_tr_b16 v[48:49], v8 offset:49152\n"
    "s_cbranch_scc1 Lpub_17\n"
    "s_mov_b64 exec, 1\n"
    "buffer_store_dwordx2 v[252:253], off, s[56:59], s77 sc1\n"
    "s_mov_b64 exec, -1\n"
    "Lpub_17:\n"
    "v_mul_f32 v92, v36, v113\n"
    "v_mul_f32 v93, v37, v113\n"
    "v_mfma_f32_16x16x32_bf16 v[104:107], v[60:63], v[224:227], v[104:107]\n"
    "ds_read_b64_tr_b16 v[50:51], v8 offset:49664\n"
    "v_mul_f32 v94, v38, v113\n"
    "v_mul_f32 v95, v39, v113\n"
    "v_mfma_f32_16x16x32_bf16 v[108:111], v[60:63], v[228:231], v[108:111]\n"
    "ds_read_b64_tr_b16 v[52:53], v8 offset:57344\n"
    "v_exp_f32 v92, v92\n"
    "v_mfma_f32_16x16x32_bf16 v[100:103], v[56:59], v[228:231], v[100:103]\n"
    "s_waitcnt lgkmcnt(3)\n"
    "ds_read_b64_tr_b16 v[54:55], v8 offset:57856\n"
    "v_mul_f32 v44, v36, v44\n"
    "v_mfma_f32_16x16x32_bf16 v[96:99], v[64:67], v[232:235], v[96:99]\n"
    "ds_read_b64_tr_b16 v[56:57], v8 offset:53248\n"
    "v_exp_f32 v93, v93\n"
    "v_mfma_f32_16x16x32_bf16 v[104:107], v[68:71], v[232:235], v[104:107]\n"
    "ds_read_b64_tr_b16 v[58:59], v8 offset:53760\n"
    "v_mul_f32 v45, v37, v45\n"
    "v_mfma_f32_16x16x32_bf16 v[108:111], v[68:71], v[236:239], v[108:111]\n"
    "ds_read_b64_tr_b16 v[60:61], v8 offset:61440\n"
    "v_exp_f32 v94, v94\n"
    "v_mfma_f32_16x16x32_bf16 v[100:103], v[64:67], v[236:239], v[100:103]\n"
    "s_waitcnt lgkmcnt(3)\n"
    "ds_read_b64_tr_b16 v[62:63], v8 offset:61952\n"
    "v_mul_f32 v46, v38, v46\n"
    "v_mfma_f32_16x16x32_bf16 v[16:19], v[48:51], v[128:131], 0\n"
    "v_add_u32 v8, 0x10000, v8\n"
    "ds_read_b64_tr_b16 v[64:65], v8 offset:0\n"
    "v_exp_f32 v95, v95\n"
    "v_mfma_f32_16x16x32_bf16 v[24:27], v[52:55], v[128:131], 0\n"
    "ds_read_b64_tr_b16 v[66:67], v8 offset:512\n"
    "v_mul_f32 v47, v39, v47\n"
    "v_fma_f32 v92, v92, v115, v115\n"
    "v_mfma_f32_16x16x32_bf16 v[28:31], v[52:55], v[132:135], 0\n"
    "ds_read_b64_tr_b16 v[68:69], v8 offset:8192\n"
    "v_fma_f32 v93, v93, v115, v115\n"
    "v_mfma_f32_16x16x32_bf16 v[20:23], v[48:51], v[132:135], 0\n"
    "s_waitcnt lgkmcnt(3)\n"
    "ds_read_b64_tr_b16 v[70:71], v8 offset:8704\n"
    "v_fma_f32 v94, v94, v115, v115\n"
    "v_fma_f32 v95, v95, v115, v115\n"
    "v_mfma_f32_16x16x32_bf16 v[16:19], v[56:59], v[136:139], v[16:19]\n"
    "ds_read_b64_tr_b16 v[48:49], v8 offset:4096\n"
    "v_rcp_f32 v92, v92\n"
    "v_mfma_f32_16x16x32_bf16 v[24:27], v[60:63], v[136:139], v[24:27]\n"
    "ds_read_b64_tr_b16 v[50:51], v8 offset:4608\n"
    "v_rcp_f32 v93, v93\n"
    "v_mfma_f32_16x16x32_bf16 v[28:31], v[60:63], v[140:143], v[28:31]\n"
    "ds_read_b64_tr_b16 v[52:53], v8 offset:12288\n"
    "v_rcp_f32 v94, v94\n"
    "v_mfma_f32_16x16x32_bf16 v[20:23], v[56:59], v[140:143], v[20:23]\n"
    "s_waitcnt lgkmcnt(3)\n"
    "ds_read_b64_tr_b16 v[54:55], v8 offset:12800\n"
    "v_mfma_f32_16x16x32_bf16 v[16:19], v[64:67], v[144:147], v[16:19]\n"
    "ds_read_b64_tr_b16 v[56:57], v8 offset:16384\n"
    "v_rcp_f32 v95, v95\n"
    "v_mfma_f32_16x16x32_bf16 v[24:27], v[68:71], v[144:147], v[24:27]\n"
    "s_setprio 1\n"
    "ds_read_b64_tr_b16 v[58:59], v8 offset:16896\n"
    "v_mul_f32 v44, v44, v92\n"
    "v_mul_f32 v45, v45, v93\n"
    "v_mfma_f32_16x16x32_bf16 v[28:31], v[68:71], v[148:151], v[28:31]\n"
    "ds_read_b64_tr_b16 v[60:61], v8 offset:24576\n"
    "v_mul_f32 v46, v46, v94\n"
    "v_mfma_f32_16x16x32_bf16 v[20:23], v[64:67], v[148:151], v[20:23]\n"
    "s_waitcnt lgkmcnt(3)\n"
    "ds_read_b64_tr_b16 v[62:63], v8 offset:25088\n"
    "v_mul_f32 v47, v47, v95\n"
    "v_cvt_pk_bf16_f32 v244, v44, v45\n"
    "v_mfma_f32_16x16x32_bf16 v[16:19], v[48:51], v[152:155], v[16:19]\n"
    "ds_read_b64_tr_b16 v[64:65], v8 offset:20480\n"
    "v_cvt_pk_bf16_f32 v245, v46, v47\n"
    "v_mfma_f32_16x16x32_bf16 v[24:27], v[52:55], v[152:155], v[24:27]\n"
    "ds_read_b64_tr_b16 v[66:67], v8 offset:20992\n"
    "v_mul_f32 v88, v96, v112\n"
    "v_mul_f32 v89, v97, v112\n"
    "v_mul_f32 v90, v98, v112\n"
    "v_mfma_f32_16x16x32_bf16 v[28:31], v[52:55], v[156:159], v[28:31]\n"
    "ds_read_b64_tr_b16 v[68:69], v8 offset:28672\n"
    "v_mul_f32 v91, v99, v112\n"
    "v_exp_f32 v88, v88\n"
    "v_mfma_f32_16x16x32_bf16 v[20:23], v[48:51], v[156:159], v[20:23]\n"
    "s_waitcnt lgkmcnt(3)\n"
    "ds_read_b64_tr_b16 v[70:71], v8 offset:29184\n"
    "v_mul_f32 v104, v96, v104\n"
    "v_mfma_f32_16x16x32_bf16 v[16:19], v[56:59], v[160:163], v[16:19]\n"
    "ds_read_b64_tr_b16 v[48:49], v8 offset:32768\n"
    "v_exp_f32 v89, v89\n"
    "v_mfma_f32_16x16x32_bf16 v[24:27], v[60:63], v[160:163], v[24:27]\n"
    "ds_read_b64_tr_b16 v[50:51], v8 offset:33280\n"
    "v_mul_f32 v105, v97, v105\n"
    "v_exp_f32 v90, v90\n"
    "v_mfma_f32_16x16x32_bf16 v[28:31], v[60:63], v[164:167], v[28:31]\n"
    "ds_read_b64_tr_b16 v[52:53], v8 offset:40960\n"
    "v_mul_f32 v106, v98, v106\n"
    "v_mfma_f32_16x16x32_bf16 v[20:23], v[56:59], v[164:167], v[20:23]\n"
    "s_waitcnt lgkmcnt(3)\n"
    "ds_read_b64_tr_b16 v[54:55], v8 offset:41472\n"
    "v_exp_f32 v91, v91\n"
    "v_mfma_f32_16x16x32_bf16 v[16:19], v[64:67], v[168:171], v[16:19]\n"
    "ds_read_b64_tr_b16 v[56:57], v8 offset:36864\n"
    "v_mul_f32 v107, v99, v107\n"
    "v_fma_f32 v88, v88, v114, v114\n"
    "v_mfma_f32_16x16x32_bf16 v[24:27], v[68:71], v[168:171], v[24:27]\n"
    "s_setprio 0\n"
    "ds_read_b64_tr_b16 v[58:59], v8 offset:37376\n"
    "s_lshl_b32 s64, s24, 2\n"
    "s_add_u32 s64, s64, s25\n"
    "s_sub_u32 s64, s64, 4\n"
    "s_mul_i32 s10, s64, 896\n"
    "s_mul_i32 s64, s64, 3200\n"
    "v_min_u32 v119, 24, v0\n"
    "v_lshlrev_b32 v119, 7, v119\n"
    "buffer_load_dword v249, v119, s[48:51], s64 offen\n"
    "v_min_u32 v119, 768, v119\n"
    "buffer_load_dword v15, v119, s[36:39], s10 offen\n"
    "v_fma_f32 v89, v89, v114, v114\n"
    "v_fma_f32 v90, v90, v114, v114\n"
    "v_mfma_f32_16x16x32_bf16 v[28:31], v[68:71], v[172:175], v[28:31]\n"
    "Lst1_1:\n"
    "s_cbranch_execz Lr1_1\n"
    "ds_read_b64_tr_b16 v[60:61], v8 offset:45056\n"
    "v_fma_f32 v91, v91, v114, v114\n"
    "v_rcp_f32 v88, v88\n"
    "v_mfma_f32_16x16x32_bf16 v[20:23], v[64:67], v[172:175], v[20:23]\n"
    "ds_read_b64_tr_b16 v[62:63], v8 offset:45568\n"
    "v_rcp_f32 v89, v89\n"
    "s_barrier\n"
    "s_setprio 2\n"
    "s_waitcnt lgkmcnt(4)\n"
    "v_mfma_f32_16x16x32_bf16 v[16:19], v[48:51], v[176:179], v[16:19]\n"
    "v_add_u32 v8, 0x8000, v8\n"
    "ds_read_b64_tr_b16 v[64:65], v8 offset:32768\n"
    "v_rcp_f32 v90, v90\n"
    "v_mfma_f32_16x16x32_bf16 v[24:27], v[52:55], v[176:179], v[24:27]\n"
    "ds_read_b64_tr_b16 v[66:67], v8 offset:33280\n"
    "v_rcp_f32 v91, v91\n"
    "v_mfma_f32_16x16x32_bf16 v[28:31], v[52:55], v[180:183], v[28:31]\n"
    "ds_read_b64_tr_b16 v[68:69], v8 offset:40960\n"
    "v_mul_f32 v104, v104, v88\n"
    "v_mfma_f32_16x16x32_bf16 v[20:23], v[48:51], v[180:183], v[20:23]\n"
    "s_waitcnt lgkmcnt(3)\n"
    "ds_read_b64_tr_b16 v[70:71], v8 offset:41472\n"
    "v_mul_f32 v105, v105, v89\n"
    "v_mul_f32 v106, v106, v90\n"
    "v_mfma_f32_16x16x32_bf16 v[16:19], v[56:59], v[184:187], v[16:19]\n"
    "ds_read_b64_tr_b16 v[48:49], v8 offset:36864\n"
    "v_mul_f32 v107, v107, v91\n"
    "v_cvt_pk_bf16_f32 v242, v104, v105\n"
    "v_mfma_f32_16x16x32_bf16 v[24:27], v[60:63], v[184:187], v[24:27]\n"
    "ds_read_b64_tr_b16 v[50:51], v8 offset:37376\n"
    "v_cvt_pk_bf16_f32 v243, v106, v107\n"
    "v_mul_f32 v92, v100, v113\n"
    "v_mfma_f32_16x16x32_bf16 v[28:31], v[60:63], v[188:191], v[28:31]\n"
    "ds_read_b64_tr_b16 v[52:53], v8 offset:45056\n"
    "v_mul_f32 v93, v101, v113\n"
    "v_mul_f32 v94, v102, v113\n"
    "v_mfma_f32_16x16x32_bf16 v[20:23], v[56:59], v[188:191], v[20:23]\n"
    "s_waitcnt lgkmcnt(3)\n"
    "ds_read_b64_tr_b16 v[54:55], v8 offset:45568\n"
    "v_mul_f32 v95, v103, v113\n"
    "v_exp_f32 v92, v92\n"
    "v_mfma_f32_16x16x32_bf16 v[16:19], v[64:67], v[192:195], v[16:19]\n"
    "ds_read_b64_tr_b16 v[56:57], v8 offset:49152\n"
    "v_mul_f32 v108, v100, v108\n"
    "v_mfma_f32_16x16x32_bf16 v[24:27], v[68:71], v[192:195], v[24:27]\n"
    "ds_read_b64_tr_b16 v[58:59], v8 offset:49664\n"
    "v_exp_f32 v93, v93\n"
    "v_mfma_f32_16x16x32_bf16 v[28:31], v[68:71], v[196:199], v[28:31]\n"
    "ds_read_b64_tr_b16 v[60:61], v8 offset:57344\n"
    "v_mul_f32 v109, v101, v109\n"
    "v_exp_f32 v94, v94\n"
    "v_mfma_f32_16x16x32_bf16 v[20:23], v[64:67], v[196:199], v[20:23]\n"
    "s_waitcnt lgkmcnt(3)\n"
    "ds_read_b64_tr_b16 v[62:63], v8 offset:57856\n"
    "v_mul_f32 v110, v102, v110\n"
    "v_mfma_f32_16x16x32_bf16 v[16:19], v[48:51], v[200:203], v[16:19]\n"
    "ds_read_b64_tr_b16 v[64:65], v8 offset:53248\n"
    "v_exp_f32 v95, v95\n"
    "v_mfma_f32_16x16x32_bf16 v[24:27], v[52:55], v[200:203], v[24:27]\n"
    "ds_read_b64_tr_b16 v[66:67], v8 offset:53760\n"
    "v_mul_f32 v111, v103, v111\n"
    "v_fma_f32 v92, v92, v115, v115\n"
    "v_mfma_f32_16x16x32_bf16 v[28:31], v[52:55], v[204:207], v[28:31]\n"
    "ds_read_b64_tr_b16 v[68:69], v8 offset:61440\n"
    "v_fma_f32 v93, v93, v115, v115\n"
    "v_fma_f32 v94, v94, v115, v115\n"
    "v_mfma_f32_16x16x32_bf16 v[20:23], v[48:51], v[204:207], v[20:23]\n"
    "s_waitcnt lgkmcnt(3)\n"
    "ds_read_b64_tr_b16 v[70:71], v8 offset:61952\n"
    "v_fma_f32 v95, v95, v115, v115\n"
    "v_rcp_f32 v92, v92\n"
    "v_mfma_f32_16x16x32_bf16 v[16:19], v[56:59], v[208:211], v[16:19]\n"
    "ds_read_b64_tr_b16 v[48:49], v8 offset:16384\n"
    "v_rcp_f32 v93, v93\n"
    "v_mfma_f32_16x16x32_bf16 v[24:27], v[60:63], v[208:211], v[24:27]\n"
    "ds_read_b64_tr_b16 v[50:51], v8 offset:16896\n"
    "v_rcp_f32 v94, v94\n"
    "v_mfma_f32_16x16x32_bf16 v[28:31], v[60:63], v[212:215], v[28:31]\n"
    "ds_read_b64_tr_b16 v[52:53], v8 offset:24576\n"
    "v_rcp_f32 v95, v95\n"
    "v_mfma_f32_16x16x32_bf16 v[20:23], v[56:59], v[212:215], v[20:23]\n"
    "s_waitcnt lgkmcnt(3)\n"
    "ds_read_b64_tr_b16 v[54:55], v8 offset:25088\n"
    "v_mul_f32 v108, v108, v92\n"
    "v_mfma_f32_16x16x32_bf16 v[16:19], v[64:67], v[216:219], v[16:19]\n"
    "ds_read_b64_tr_b16 v[56:57], v8 offset:20480\n"
    "buffer_load_dwordx2 v[250:251], v248, s[56:59], 0 offen sc1\n"
    "v_mul_f32 v109, v109, v93\n"
    "v_mul_f32 v110, v110, v94\n"
    "v_mfma_f32_16x16x32_bf16 v[24:27], v[68:71], v[216:219], v[24:27]\n"
    "ds_read_b64_tr_b16 v[58:59], v8 offset:20992\n"
    "v_mul_f32 v111, v111, v95\n"
    "v_cvt_pk_bf16_f32 v246, v108, v109\n"
    "v_mfma_f32_16x16x32_bf16 v[28:31], v[68:71], v[220:223], v[28:31]\n"
    "ds_read_b64_tr_b16 v[60:61], v8 offset:28672\n"
    "buffer_store_dwordx4 v[240:243], v1, s[56:59], s82 offen sc1\n"
    "v_cvt_pk_bf16_f32 v247, v110, v111\n"
    "v_mfma_f32_16x16x32_bf16 v[20:23], v[64:67], v[220:223], v[20:23]\n"
    "s_add_u32 s82, s82, s76\n"
    "ds_read_b64_tr_b16 v[62:63], v8 offset:29184\n"
    "buffer_store_dwordx4 v[244:247], v1, s[56:59], s82 offen sc1\n"
    "s_waitcnt lgkmcnt(4)\n"
    "v_mfma_f32_16x16x32_bf16 v[16:19], v[48:51], v[224:227], v[16:19]\n"
    "ds_read_b64_tr_b16 v[64:65], v9 offset:49152\n"
    "v_mfma_f32_16x16x32_bf16 v[24:27], v[52:55], v[224:227], v[24:27]\n"
    "ds_read_b64_tr_b16 v[66:67], v9 offset:49664\n"
    "v_mfma_f32_16x16x32_bf16 v[28:31], v[52:55], v[228:231], v[28:31]\n"
    "ds_read_b64_tr_b16 v[68:69], v9 offset:57344\n"
    "v_mfma_f32_16x16x32_bf16 v[20:23], v[48:51], v[228:231], v[20:23]\n"
    "s_waitcnt lgkmcnt(3)\n"
    "ds_read_b64_tr_b16 v[70:71], v9 offset:57856\n"
    "v_mfma_f32_16x16x32_bf16 v[16:19], v[56:59], v[232:235], v[16:19]\n"
    "ds_read_b64_tr_b16 v[48:49], v9 offset:53248\n"
    "v_mfma_f32_16x16x32_bf16 v[24:27], v[60:63], v[232:235], v[24:27]\n"
    "ds_read_b64_tr_b16 v[50:51], v9 offset:53760\n"
    "v_mfma_f32_16x16x32_bf16 v[28:31], v[60:63], v[236:239], v[28:31]\n"
    "s_setprio 1\n"
    "ds_read_b64_tr_b16 v[52:53], v9 offset:61440\n"
    "v_mfma_f32_16x16x32_bf16 v[20:23], v[56:59], v[236:239], v[20:23]\n"
    "s_waitcnt lgkmcnt(3)\n"
    "ds_read_b64_tr_b16 v[54:55], v9 offset:61952\n"
    "v_mfma_f32_16x16x32_bf16 v[32:35], v[64:67], v[128:131], 0\n"
    "v_add_u32 v9, 0x10000, v9\n"
    "ds_read_b64_tr_b16 v[56:57], v9 offset:0\n"
    "v_mfma_f32_16x16x32_bf16 v[40:43], v[68:71], v[128:131], 0\n"
    "ds_read_b64_tr_b16 v[58:59], v9 offset:512\n"
    "v_mfma_f32_16x16x32_bf16 v[44:47], v[68:71], v[132:135], 0\n"
    "ds_read_b64_tr_b16 v[60:61], v9 offset:8192\n"
    "v_mfma_f32_16x16x32_bf16 v[36:39], v[64:67], v[132:135], 0\n"
    "s_waitcnt lgkmcnt(3)\n"
    "ds_read_b64_tr_b16 v[62:63], v9 offset:8704\n"
    "v_mfma_f32_16x16x32_bf16 v[32:35], v[48:51], v[136:139], v[32:35]\n"
    "ds_read_b64_tr_b16 v[64:65], v9 offset:4096\n"
    "v_mfma_f32_16x16x32_bf16 v[40:43], v[52:55], v[136:139], v[40:43]\n"
    "ds_read_b64_tr_b16 v[66:67], v9 offset:4608\n"
    "v_mul_f32 v88, v16, v112\n"
    "v_mul_f32 v89, v17, v112\n"
    "v_mfma_f32_16x16x32_bf16 v[44:47], v[52:55], v[140:143], v[44:47]\n"
    "ds_read_b64_tr_b16 v[68:69], v9 offset:12288\n"
    "v_mul_f32 v90, v18, v112\n"
    "v_mfma_f32_16x16x32_bf16 v[36:39], v[48:51], v[140:143], v[36:39]\n"
    "s_waitcnt lgkmcnt(3)\n"
    "ds_read_b64_tr_b16 v[70:71], v9 offset:12800\n"
    "v_mul_f32 v91, v19, v112\n"
    "v_exp_f32 v88, v88\n"
    "v_mfma_f32_16x16x32_bf16 v[32:35], v[56:59], v[144:147], v[32:35]\n"
    "ds_read_b64_tr_b16 v[48:49], v9 offset:16384\n"
    "v_mfma_f32_16x16x32_bf16 v[40:43], v[60:63], v[144:147], v[40:43]\n"
    "ds_read_b64_tr_b16 v[50:51], v9 offset:16896\n"
    "v_mul_f32 v24, v16, v24\n"
    "v_mfma_f32_16x16x32_bf16 v[44:47], v[60:63], v[148:151], v[44:47]\n"
    "ds_read_b64_tr_b16 v[52:53], v9 offset:24576\n"
    "v_exp_f32 v89, v89\n"
    "v_mfma_f32_16x16x32_bf16 v[36:39], v[56:59], v[148:151], v[36:39]\n"
    "s_waitcnt lgkmcnt(3)\n"
    "ds_read_b64_tr_b16 v[54:55], v9 offset:25088\n"
    "v_mul_f32 v25, v17, v25\n"
    "v_mfma_f32_16x16x32_bf16 v[32:35], v[64:67], v[152:155], v[32:35]\n"
    "ds_read_b64_tr_b16 v[56:57], v9 offset:20480\n"
    "v_exp_f32 v90, v90\n"
    "v_mfma_f32_16x16x32_bf16 v[40:43], v[68:71], v[152:155], v[40:43]\n"
    "ds_read_b64_tr_b16 v[58:59], v9 offset:20992\n"
    "v_mul_f32 v26, v18, v26\n"
    "v_mfma_f32_16x16x32_bf16 v[44:47], v[68:71], v[156:159], v[44:47]\n"
    "ds_read_b64_tr_b16 v[60:61], v9 offset:28672\n"
    "v_exp_f32 v91, v91\n"
    "v_mfma_f32_16x16x32_bf16 v[36:39], v[64:67], v[156:159], v[36:39]\n"
    "s_waitcnt lgkmcnt(3)\n"
    "ds_read_b64_tr_b16 v[62:63], v9 offset:29184\n"
    "v_mul_f32 v27, v19, v27\n"
    "v_mfma_f32_16x16x32_bf16 v[32:35], v[48:51], v[160:163], v[32:35]\n"
    "ds_read_b64_tr_b16 v[64:65], v9 offset:32768\n"
    "v_fma_f32 v88, v88, v114, v114\n"
    "v_mfma_f32_16x16x32_bf16 v[40:43], v[52:55], v[160:163], v[40:43]\n"
    "ds_read_b64_tr_b16 v[66:67], v9 offset:33280\n"
    "v_fma_f32 v89, v89, v114, v114\n"
    "v_fma_f32 v90, v90, v114, v114\n"
    "v_mfma_f32_16x16x32_bf16 v[44:47], v[52:55], v[164:167], v[44:47]\n"
    "ds_read_b64_tr_b16 v[68:69], v9 offset:40960\n"
    "v_fma_f32 v91, v91, v114, v114\n"
    "v_mfma_f32_16x16x32_bf16 v[36:39], v[48:51], v[164:167], v[36:39]\n"
    "s_waitcnt lgkmcnt(3)\n"
    "ds_read_b64_tr_b16 v[70:71], v9 offset:41472\n"
    "v_rcp_f32 v88, v88\n"
    "v_mfma_f32_16x16x32_bf16 v[32:35], v[56:59], v[168:171], v[32:35]\n"
    "s_setprio 0\n"
    "ds_read_b64_tr_b16 v[48:49], v9 offset:36864\n"
    "v_rcp_f32 v89, v89\n"
    "v_mfma_f32_16x16x32_bf16 v[40:43], v[60:63], v[168:171], v[40:43]\n"
    "ds_read_b64_tr_b16 v[50:51], v9 offset:37376\n"
    "v_mfma_f32_16x16x32_bf16 v[44:47], v[60:63], v[172:175], v[44:47]\n"
    "ds_read_b64_tr_b16 v[52:53], v9 offset:45056\n"
    "v_rcp_f32 v90, v90\n"
    "v_mfma_f32_16x16x32_bf16 v[36:39], v[56:59], v[172:175], v[36:39]\n"
    "ds_read_b64_tr_b16 v[54:55], v9 offset:45568\n"
    "v_rcp_f32 v91, v91\n"
    "s_waitcnt vmcnt(2)\n"
    "v_cmp_ne_u64 vcc, s[60:61], v[250:251]\n"
    "s_nop 0\n"
    "s_cbranch_vccz Lok_18\n"
    "s_mov_b32 s10, 0x8000\n"
    "Lspin_19:\n"
    "buffer_load_dwordx2 v[250:251], v248, s[56:59], 0 offen sc1\n"
    "s_sub_u32 s10, s10, 1\n"
    "s_waitcnt vmcnt(0)\n"
    "s_cbranch_scc1 Lok_18\n"
    "v_cmp_ne_u64 vcc, s[60:61], v[250:251]\n"
    "s_nop 0\n"
    "s_cbranch_vccnz Lspin_19\n"
    "Lok_18:\n"
    "s_barrier\n"
    "s_setprio 2\n"
    "s_waitcnt lgkmcnt(4)\n"
    "v_mfma_f32_16x16x32_bf16 v[32:35], v[64:67], v[176:179], v[32:35]\n"
    "v_add_u32 v9, 0x8000, v9\n"
    "ds_read_b64_tr_b16 v[56:57], v9 offset:32768\n"
    "buffer_load_dwordx4 v[104:107], v1, s[56:59], s92 offen offset:0 sc1\n"
    "v_mfma_f32_16x16x32_bf16 v[40:43], v[68:71], v[176:179], v[40:43]\n"
    "ds_read_b64_tr_b16 v[58:59], v9 offset:33280\n"
    "v_mul_f32 v24, v24, v88\n"
    "v_mul_f32 v25, v25, v89\n"
    "v_mfma_f32_16x16x32_bf16 v[44:47], v[68:71], v[180:183], v[44:47]\n"
    "ds_read_b64_tr_b16 v[60:61], v9 offset:40960\n"
    "v_mul_f32 v26, v26, v90\n"
    "v_mfma_f32_16x16x32_bf16 v[36:39], v[64:67], v[180:183], v[36:39]\n"
    "s_waitcnt lgkmcnt(3)\n"
    "ds_read_b64_tr_b16 v[62:63], v9 offset:41472\n"
    "v_mul_f32 v27, v27, v91\n"
    "v_mfma_f32_16x16x32_bf16 v[32:35], v[48:51], v[184:187], v[32:35]\n"
    "ds_read_b64_tr_b16 v[64:65], v9 offset:36864\n"
    "v_cvt_pk_bf16_f32 v240, v24, v25\n"
    "v_cvt_pk_bf16_f32 v241, v26, v27\n"
    "v_mfma_f32_16x16x32_bf16 v[40:43], v[52:55], v[184:187], v[40:43]\n"
    "ds_read_b64_tr_b16 v[66:67], v9 offset:37376\n"
    "v_mul_f32 v92, v20, v113\n"
    "v_mfma_f32_16x16x32_bf16 v[44:47], v[52:55], v[188:191], v[44:47]\n"
    "ds_read_b64_tr_b16 v[68:69], v9 offset:45056\n"
    "v_mul_f32 v93, v21, v113\n"
    "v_mul_f32 v94, v22, v113\n"
    "v_mfma_f32_16x16x32_bf16 v[36:39], v[48:51], v[188:191], v[36:39]\n"
    "s_waitcnt lgkmcnt(3)\n"
    "ds_read_b64_tr_b16 v[70:71], v9 offset:45568\n"
    "v_mul_f32 v95, v23, v113\n"
    "v_mfma_f32_16x16x32_bf16 v[32:35], v[56:59], v[192:195], v[32:35]\n"
    "ds_read_b64_tr_b16 v[48:49], v9 offset:49152\n"
    "v_exp_f32 v92, v92\n"
    "v_mfma_f32_16x16x32_bf16 v[40:43], v[60:63], v[192:195], v[40:43]\n"
    "ds_read_b64_tr_b16 v[50:51], v9 offset:49664\n"
    "v_mul_f32 v28, v20, v28\n"
    "v_mfma_f32_16x16x32_bf16 v[44:47], v[60:63], v[196:199], v[44:47]\n"
    "ds_read_b64_tr_b16 v[52:53], v9 offset:57344\n"
    "v_exp_f32 v93, v93\n"
    "v_mfma_f32_16x16x32_bf16 v[36:39], v[56:59], v[196:199], v[36:39]\n"
    "s_waitcnt lgkmcnt(3)\n"
    "ds_read_b64_tr_b16 v[54:55], v9 offset:57856\n"
    "v_mul_f32 v29, v21, v29\n"
    "v_mfma_f32_16x16x32_bf16 v[32:35], v[64:67], v[200:203], v[32:35]\n"
    "ds_read_b64_tr_b16 v[56:57], v9 offset:53248\n"
    "buffer_load_dwordx4 v[108:111], v1, s[56:59], s92 offen offset:1024 sc1\n"
    "v_exp_f32 v94, v94\n"
    "v_mfma_f32_16x16x32_bf16 v[40:43], v[68:71], v[200:203], v[40:43]\n"
    "ds_read_b64_tr_b16 v[58:59], v9 offset:53760\n"
    "v_mfma_f32_16x16x32_bf16 v[44:47], v[68:71], v[204:207], v[44:47]\n"
    "ds_read_b64_tr_b16 v[60:61], v9 offset:61440\n"
    "v_mul_f32 v30, v22, v30\n"
    "v_exp_f32 v95, v95\n"
    "v_mfma_f32_16x16x32_bf16 v[36:39], v[64:67], v[204:207], v[36:39]\n"
    "s_waitcnt lgkmcnt(3)\n"
    "ds_read_b64_tr_b16 v[62:63], v9 offset:61952\n"
    "v_mfma_f32_16x16x32_bf16 v[32:35], v[48:51], v[208:211], v[32:35]\n"
    "ds_read_b64_tr_b16 v[64:65], v9 offset:16384\n"
    "v_mul_f32 v31, v23, v31\n"
    "v_mfma_f32_16x16x32_bf16 v[40:43], v[52:55], v[208:211], v[40:43]\n"
    "ds_read_b64_tr_b16 v[66:67], v9 offset:16896\n"
    "v_fma_f32 v92, v92, v115, v115\n"
    "v_fma_f32 v93, v93, v115, v115\n"
    "v_mfma_f32_16x16x32_bf16 v[44:47], v[52:55], v[212:215], v[44:47]\n"
    "ds_read_b64_tr_b16 v[68:69], v9 offset:24576\n"
    "v_fma_f32 v94, v94, v115, v115\n"
    "v_mfma_f32_16x16x32_bf16 v[36:39], v[48:51], v[212:215], v[36:39]\n"
    "s_waitcnt lgkmcnt(3)\n"
    "ds_read_b64_tr_b16 v[70:71], v9 offset:25088\n"
    "v_fma_f32 v95, v95, v115, v115\n"
    "v_rcp_f32 v92, v92\n"
    "v_mfma_f32_16x16x32_bf16 v[32:35], v[56:59], v[216:219], v[32:35]\n"
    "ds_read_b64_tr_b16 v[48:49], v9 offset:20480\n"
    "v_mfma_f32_16x16x32_bf16 v[40:43], v[60:63], v[216:219], v[40:43]\n"
    "ds_read_b64_tr_b16 v[50:51], v9 offset:20992\n"
    "v_rcp_f32 v93, v93\n"
    "v_mfma_f32_16x16x32_bf16 v[44:47], v[60:63], v[220:223], v[44:47]\n"
    "ds_read_b64_tr_b16 v[52:53], v9 offset:28672\n"
    "v_rcp_f32 v94, v94\n"
    "v_mfma_f32_16x16x32_bf16 v[36:39], v[56:59], v[220:223], v[36:39]\n"
    "s_waitcnt lgkmcnt(3)\n"
    "ds_read_b64_tr_b16 v[54:55], v9 offset:29184\n"
    "v_mfma_f32_16x16x32_bf16 v[32:35], v[64:67], v[224:227], v[32:35]\n"
    "v_rcp_f32 v95, v95\n"
    "v_mfma_f32_16x16x32_bf16 v[40:43], v[68:71], v[224:227], v[40:43]\n"
    "s_setprio 1\n"
    "v_mul_f32 v28, v28, v92\n"
    "v_mfma_f32_16x16x32_bf16 v[44:47], v[68:71], v[228:231], v[44:47]\n"
    "v_mul_f32 v29, v29, v93\n"
    "v_mfma_f32_16x16x32_bf16 v[36:39], v[64:67], v[228:231], v[36:39]\n"
    "s_waitcnt lgkmcnt(0)\n"
    "v_mul_f32 v30, v30, v94\n"
    "v_mul_f32 v31, v31, v95\n"
    "v_mfma_f32_16x16x32_bf16 v[32:35], v[48:51], v[232:235], v[32:35]\n"
    "v_cvt_pk_bf16_f32 v244, v28, v29\n"
    "v_mfma_f32_16x16x32_bf16 v[40:43], v[52:55], v[232:235], v[40:43]\n"
    "v_cvt_pk_bf16_f32 v245, v30, v31\n"
    "v_mfma_f32_16x16x32_bf16 v[44:47], v[52:55], v[236:239], v[44:47]\n"
    "v_mfma_f32_16x16x32_bf16 v[36:39], v[48:51], v[236:239], v[36:39]\n"
    "s_branch Lattn_h1\n"
    "Lidle_h1:\n"
    "s_waitcnt vmcnt(0)\n"
    "s_mov_b64 exec, 1\n"
    "buffer_store_dwordx2 v[252:253], off, s[56:59], s94 sc1\n"
    "s_mov_b64 exec, -1\n"
    "s_branch Lend\n"
    "Lfallback:\n"
    "v_cmp_lt_u32 vcc, 31, v0\n"
    "s_nop 1\n"
    "v_cndmask_b32 v110, v110, 0, vcc\n"
    "v_mov_b32 v118, 0xff800000\n"
    "s_sub_u32 s65, s90, s17\n"
    "s_add_u32 s66, s65, 0x40000\n"
    "s_mov_b32 s64, s65\n"
    "Lfb1:\n"
    "buffer_load_dwordx4 v[80:83], v1, s[56:59], s64 offen offset:0 sc1\n"
    "buffer_load_dwordx4 v[84:87], v1, s[56:59], s64 offen offset:1024 sc1\n"
    "s_waitcnt vmcnt(0)\n"
    "v_mfma_f32_32x32x16_bf16 v[48:63], v[80:83], v[104:107], 0\n"
    "v_mfma_f32_32x32x16_bf16 v[48:63], v[84:87], v[108:111], v[48:63]\n"
    "s_add_u32 s64, s64, 4096\n"
    "s_cmp_lt_u32 s64, s66\n"
    "s_nop 9\n"
    "v_max3_f32 v118, v118, v48, v49\n"
    "v_max3_f32 v118, v118, v50, v51\n"
    "v_max3_f32 v118, v118, v52, v53\n"
    "v_max3_f32 v118, v118, v54, v55\n"
    "v_max3_f32 v118, v118, v56, v57\n"
    "v_max3_f32 v118, v118, v58, v59\n"
    "v_max3_f32 v118, v118, v60, v61\n"
    "v_max3_f32 v118, v118, v62, v63\n"
    "s_cbranch_scc1 Lfb1\n"
    "v_mov_b32 v119, v118\n"
    "s_nop 1\n"
    "v_permlane32_swap_b32 v118, v119\n"
    "v_max_f32 v118, v118, v119\n"
    "s_mov_b32 s64, s65\n"
    "v_mov_b32 v16, 0\n"
    "v_mov_b32 v17, 0\n"
    "v_mov_b32 v18, 0\n"
    "v_mov_b32 v19, 0\n"
    "v_mov_b32 v20, 0\n"
    "v_mov_b32 v21, 0\n"
    "v_mov_b32 v22, 0\n"
    "v_mov_b32 v23, 0\n"
    "v_mov_b32 v24, 0\n"
    "v_mov_b32 v25, 0\n"
    "v_mov_b32 v26, 0\n"
    "v_mov_b32 v27, 0\n"
    "v_mov_b32 v28, 0\n"
    "v_mov_b32 v29, 0\n"
    "v_mov_b32 v30, 0\n"
    "v_mov_b32 v31, 0\n"
    "Lfb2:\n"
    "buffer_load_dwordx4 v[80:83], v1, s[56:59], s64 offen offset:0 sc1\n"
    "buffer_load_dwordx4 v[84:87], v1, s[56:59], s64 offen offset:1024 sc1\n"
    "buffer_load_dwordx4 v[88:91], v1, s[56:59], s64 offen offset:2048 sc1\n"
    "buffer_load_dwordx4 v[92:95], v1, s[56:59], s64 offen offset:3072 sc1\n"
    "s_waitcnt vmcnt(0)\n"
    "v_mfma_f32_32x32x16_bf16 v[48:63], v[80:83], v[104:107], 0\n"
    "v_mfma_f32_32x32x16_bf16 v[48:63], v[84:87], v[108:111], v[48:63]\n"
    "s_nop 11\n"
    "v_sub_f32 v48, v48, v118\n"
    "v_sub_f32 v49, v49, v118\n"
    "v_sub_f32 v50, v50, v118\n"
    "v_sub_f32 v51, v51, v118\n"
    "v_sub_f32 v52, v52, v118\n"
    "v_sub_f32 v53, v53, v118\n"
    "v_sub_f32 v54, v54, v118\n"
    "v_sub_f32 v55, v55, v118\n"
    "v_sub_f32 v56, v56, v118\n"
    "v_sub_f32 v57, v57, v118\n"
    "v_sub_f32 v58, v58, v118\n"
    "v_sub_f32 v59, v59, v118\n"
    "v_sub_f32 v60, v60, v118\n"
    "v_sub_f32 v61, v61, v118\n"
    "v_sub_f32 v62, v62, v118\n"
    "v_sub_f32 v63, v63, v118\n"
    "v_exp_f32 v48, v48\n"
    "v_exp_f32 v49, v49\n"
    "v_exp_f32 v50, v50\n"
    "v_exp_f32 v51, v51\n"
    "v_exp_f32 v52, v52\n"
    "v_exp_f32 v53, v53\n"
    "v_exp_f32 v54, v54\n"
    "v_exp_f32 v55, v55\n"
    "v_exp_f32 v56, v56\n"
    "v_exp_f32 v57, v57\n"
    "v_exp_f32 v58, v58\n"
    "v_exp_f32 v59, v59\n"
    "v_exp_f32 v60, v60\n"
    "v_exp_f32 v61, v61\n"
    "v_exp_f32 v62, v62\n"
    "v_exp_f32 v63, v63\n"
    "v_smfmac_f32_32x32x32_bf16 v[16:31], v[88:91], v[48:55], v72\n"
    "s_nop 0\n"
    "v_smfmac_f32_32x32x32_bf16 v[16:31], v[92:95], v[56:63], v72\n"
    "s_add_u32 s64, s64, 4096\n"
    "s_cmp_lt_u32 s64, s66\n"
    "s_cbranch_scc1 Lfb2\n"
    "s_nop 8\n"
    "v_mov_b32 v116, v28\n"
    "v_mov_b32 v117, v28\n"
    "s_nop 1\n"
    "v_permlane32_swap_b32 v116, v117\n"
    "s_branch Lnorm\n"
    "LpathY_h1:\n"
    "Lst1_2:\n"
    "s_cbranch_execz Lr1_2\n"
    "ds_read_b128 v[128:131], v94 offset:0\n"
    "ds_read_b128 v[136:139], v95 offset:8192\n"
    "ds_read_b128 v[144:147], v94 offset:16384\n"
    "ds_read_b128 v[152:155], v95 offset:24576\n"
    "ds_read_b128 v[160:163], v94 offset:32768\n"
    "ds_read_b128 v[168:171], v95 offset:40960\n"
    "ds_read_b128 v[176:179], v94 offset:49152\n"
    "ds_read_b128 v[184:187], v95 offset:57344\n"
    "v_add_u32 v94, 0x10000, v94\n"
    "ds_read_b128 v[192:195], v94 offset:0\n"
    "v_add_u32 v95, 0x10000, v95\n"
    "ds_read_b128 v[200:203], v95 offset:8192\n"
    "ds_read_b128 v[208:211], v94 offset:16384\n"
    "ds_read_b128 v[216:219], v95 offset:24576\n"
    "ds_read_b128 v[224:227], v94 offset:32768\n"
    "ds_read_b128 v[232:235], v95 offset:40960\n"
    "v_dot2_f32_bf16 v21, v188, v188, 0\n"
    "s_nop 2\n"
    "v_dot2c_f32_bf16 v21, v189, v189\n"
    "v_dot2c_f32_bf16 v21, v190, v190\n"
    "v_dot2c_f32_bf16 v21, v191, v191\n"
    "v_dot2c_f32_bf16 v21, v196, v196\n"
    "v_dot2c_f32_bf16 v21, v197, v197\n"
    "v_dot2c_f32_bf16 v21, v198, v198\n"
    "v_dot2c_f32_bf16 v21, v199, v199\n"
    "v_dot2c_f32_bf16 v21, v204, v204\n"
    "v_dot2c_f32_bf16 v21, v205, v205\n"
    "v_dot2c_f32_bf16 v21, v206, v206\n"
    "v_dot2c_f32_bf16 v21, v207, v207\n"
    "v_dot2c_f32_bf16 v21, v212, v212\n"
    "v_dot2c_f32_bf16 v21, v213, v213\n"
    "v_dot2c_f32_bf16 v21, v214, v214\n"
    "v_dot2c_f32_bf16 v21, v215, v215\n"
    "v_dot2c_f32_bf16 v21, v220, v220\n"
    "v_dot2c_f32_bf16 v21, v221, v221\n"
    "v_dot2c_f32_bf16 v21, v222, v222\n"
    "v_dot2c_f32_bf16 v21, v223, v223\n"
    "v_dot2c_f32_bf16 v21, v228, v228\n"
    "v_dot2c_f32_bf16 v21, v229, v229\n"
    "v_dot2c_f32_bf16 v21, v230, v230\n"
    "v_dot2c_f32_bf16 v21, v231, v231\n"
    "v_dot2c_f32_bf16 v21, v236, v236\n"
    "v_dot2c_f32_bf16 v21, v237, v237\n"
    "v_dot2c_f32_bf16 v21, v238, v238\n"
    "v_dot2c_f32_bf16 v21, v239, v239\n"
    "v_mov_b32 v22, 0x3727c5ac\n"
    "s_nop 1\n"
    "v_add_f32_dpp v21, v21, v21 quad_perm:[1,0,3,2] row_mask:0xf bank_mask:0xf\n"
    "s_nop 1\n"
    "v_add_f32_dpp v21, v21, v21 quad_perm:[2,3,0,1] row_mask:0xf bank_mask:0xf\n"
    "s_nop 1\n"
    "v_add_f32_dpp v21, v21, v21 row_half_mirror row_mask:0xf bank_mask:0xf\n"
    "v_fmamk_f32 v14, v21, 0x3b124925, v22\n"
    "s_add_u32 m0, s63, 0x4000\n"
    "s_add_u32 s6, s6, s5\n"
    "buffer_load_dwordx4 v2, s[32:35], s6 offen lds\n"
    "s_add_u32 m0, s63, 0x5000\n"
    "s_add_u32 s64, s6, 86016\n"
    "buffer_load_dwordx4 v2, s[32:35], s64 offen lds\n"
    "s_add_u32 m0, s63, 0x6000\n"
    "s_add_u32 s6, s6, s4\n"
    "buffer_load_dwordx4 v2, s[32:35], s6 offen lds\n"
    "s_add_u32 m0, s63, 0x7000\n"
    "s_add_u32 s64, s6, 86016\n"
    "buffer_load_dwordx4 v2, s[32:35], s64 offen lds\n"
    "s_add_u32 m0, s63, 0x8000\n"
    "s_add_u32 s6, s6, s5\n"
    "buffer_load_dwordx4 v2, s[32:35], s6 offen lds\n"
    "s_add_u32 m0, s63, 0x9000\n"
    "s_add_u32 s64, s6, 86016\n"
    "buffer_load_dwordx4 v2, s[32:35], s64 offen lds\n"
    "s_add_u32 m0, s63, 0xa000\n"
    "s_add_u32 s6, s6, s4\n"
    "buffer_load_dwordx4 v2, s[32:35], s6 offen lds\n"
    "s_add_u32 m0, s63, 0xb000\n"
    "s_add_u32 s64, s6, 86016\n"
    "buffer_load_dwordx4 v2, s[32:35], s64 offen lds\n"
    "s_waitcnt lgkmcnt(14)\n"
    "s_barrier\n"
    "s_setprio 2\n"
    "ds_read_b64_tr_b16 v[48:49], v6 offset:0\n"
    "ds_read_b64_tr_b16 v[50:51], v6 offset:512\n"
    "ds_read_b128 v[132:135], v4 offset:0\n"
    "ds_read_b64_tr_b16 v[52:53], v7 offset:0\n"
    "ds_read_b64_tr_b16 v[54:55], v7 offset:512\n"
    "ds_read_b64_tr_b16 v[56:57], v6 offset:4096\n"
    "ds_read_b64_tr_b16 v[58:59], v6 offset:4608\n"
    "ds_read_b128 v[140:143], v12 offset:8192\n"
    "ds_read_b64_tr_b16 v[60:61], v7 offset:4096\n"
    "ds_read_b64_tr_b16 v[62:63], v7 offset:4608\n"
    "s_waitcnt lgkmcnt(8)\n"
    "v_mfma_f32_16x16x32_bf16 v[16:19], v[48:51], v[128:131], 0\n"
    "s_waitcnt lgkmcnt(5)\n"
    "ds_read_b128 v[148:151], v4 offset:16384\n"
    "ds_read_b64_tr_b16 v[64:65], v6 offset:8192\n"
    "v_mfma_f32_16x16x32_bf16 v[24:27], v[52:55], v[128:131], 0\n"
    "ds_read_b64_tr_b16 v[66:67], v6 offset:8704\n"
    "v_mfma_f32_16x16x32_bf16 v[28:31], v[52:55], v[132:135], 0\n"
    "ds_read_b128 v[156:159], v12 offset:24576\n"
    "ds_read_b64_tr_b16 v[68:69], v7 offset:8192\n"
    "v_mfma_f32_16x16x32_bf16 v[20:23], v[48:51], v[132:135], 0\n"
    "s_waitcnt lgkmcnt(8)\n"
    "ds_read_b64_tr_b16 v[70:71], v7 offset:8704\n"
    "v_mfma_f32_16x16x32_bf16 v[16:19], v[56:59], v[136:139], v[16:19]\n"
    "s_setprio 1\n"
    "ds_read_b128 v[164:167], v4 offset:32768\n"
    "ds_read_b64_tr_b16 v[48:49], v6 offset:12288\n"
    "buffer_load_dwordx2 v[252:253], off, s[56:59], 0 sc1\n"
    "s_waitcnt lgkmcnt(8)\n"
    "v_mfma_f32_16x16x32_bf16 v[24:27], v[60:63], v[136:139], v[24:27]\n"
    "ds_read_b64_tr_b16 v[50:51], v6 offset:12800\n"
    "v_mfma_f32_16x16x32_bf16 v[28:31], v[60:63], v[140:143], v[28:31]\n"
    "ds_read_b128 v[172:175], v12 offset:40960\n"
    "ds_read_b64_tr_b16 v[52:53], v7 offset:12288\n"
    "v_mfma_f32_16x16x32_bf16 v[20:23], v[56:59], v[140:143], v[20:23]\n"
    "s_setprio 0\n"
    "ds_read_b64_tr_b16 v[54:55], v7 offset:12800\n"
    "s_waitcnt vmcnt(7)\n"
    "s_barrier\n"
    "s_setprio 2\n"
    "s_waitcnt lgkmcnt(9)\n"
    "v_mfma_f32_16x16x32_bf16 v[32:35], v[64:67], v[128:131], 0\n"
    "s_waitcnt lgkmcnt(6)\n"
    "ds_read_b128 v[180:183], v4 offset:49152\n"
    "ds_read_b64_tr_b16 v[56:57], v6 offset:16384\n"
    "v_mfma_f32_16x16x32_bf16 v[40:43], v[68:71], v[128:131], 0\n"
    "ds_read_b64_tr_b16 v[58:59], v6 offset:16896\n"
    "v_mfma_f32_16x16x32_bf16 v[44:47], v[68:71], v[132:135], 0\n"
    "ds_read_b64_tr_b16 v[60:61], v7 offset:16384\n"
    "v_mfma_f32_16x16x32_bf16 v[36:39], v[64:67], v[132:135], 0\n"
    "s_waitcnt lgkmcnt(7)\n"
    "ds_read_b64_tr_b16 v[62:63], v7 offset:16896\n"
    "ds_write_b32 v73, v13 offset:57344\n"
    "v_mfma_f32_16x16x32_bf16 v[32:35], v[48:51], v[136:139], v[32:35]\n"
    "s_setprio 1\n"
    "ds_read_b128 v[188:191], v12 offset:57344\n"
    "ds_read_b64_tr_b16 v[64:65], v6 offset:20480\n"
    "ds_write_b32 v73, v14 offset:57376\n"
    "s_waitcnt lgkmcnt(9)\n"
    "v_mfma_f32_16x16x32_bf16 v[40:43], v[52:55], v[136:139], v[40:43]\n"
    "ds_read_b64_tr_b16 v[66:67], v6 offset:20992\n"
    "v_mfma_f32_16x16x32_bf16 v[44:47], v[52:55], v[140:143], v[44:47]\n"
    "ds_read_b64_tr_b16 v[68:69], v7 offset:20480\n"
    "v_mfma_f32_16x16x32_bf16 v[36:39], v[48:51], v[140:143], v[36:39]\n"
    "s_setprio 0\n"
    "ds_read_b64_tr_b16 v[70:71], v7 offset:20992\n"
    "s_waitcnt vmcnt(1)\n"
    "s_barrier\n"
    "s_setprio 2\n"
    "s_waitcnt lgkmcnt(9)\n"
    "v_mfma_f32_16x16x32_bf16 v[16:19], v[56:59], v[144:147], v[16:19]\n"
    "s_waitcnt lgkmcnt(7)\n"
    "v_add_u32 v4, 0x10000, v4\n"
    "ds_read_b128 v[196:199], v4 offset:0\n"
    "ds_read_b64_tr_b16 v[48:49], v6 offset:24576\n"
    "v_mfma_f32_16x16x32_bf16 v[24:27], v[60:63], v[144:147], v[24:27]\n"
    "ds_read_b64_tr_b16 v[50:51], v6 offset:25088\n"
    "v_mfma_f32_16x16x32_bf16 v[28:31], v[60:63], v[148:151], v[28:31]\n"
    "v_add_u32 v12, 0x10000, v12\n"
    "ds_read_b128 v[204:207], v12 offset:8192\n"
    "ds_read_b64_tr_b16 v[52:53], v7 offset:24576\n"
    "v_mfma_f32_16x16x32_bf16 v[20:23], v[56:59], v[148:151], v[20:23]\n"
    "s_waitcnt lgkmcnt(7)\n"
    "ds_read_b64_tr_b16 v[54:55], v7 offset:25088\n"
    "v_mfma_f32_16x16x32_bf16 v[16:19], v[64:67], v[152:155], v[16:19]\n"
    "s_waitcnt lgkmcnt(6)\n"
    "ds_read_b128 v[212:215], v4 offset:16384\n"
    "ds_read_b64_tr_b16 v[56:57], v6 offset:28672\n"
    "v_mfma_f32_16x16x32_bf16 v[24:27], v[68:71], v[152:155], v[24:27]\n"
    "ds_read_b64_tr_b16 v[58:59], v6 offset:29184\n"
    "v_mfma_f32_16x16x32_bf16 v[28:31], v[68:71], v[156:159], v[28:31]\n"
    "ds_read_b128 v[220:223], v12 offset:24576\n"
    "ds_read_b64_tr_b16 v[60:61], v7 offset:28672\n"
    "v_mfma_f32_16x16x32_bf16 v[20:23], v[64:67], v[156:159], v[20:23]\n"
    "s_waitcnt lgkmcnt(8)\n"
    "ds_read_b64_tr_b16 v[62:63], v7 offset:29184\n"
    "v_mfma_f32_16x16x32_bf16 v[32:35], v[48:51], v[144:147], v[32:35]\n"
    "s_waitcnt lgkmcnt(6)\n"
    "ds_read_b128 v[228:231], v4 offset:32768\n"
    "ds_read_b64_tr_b16 v[64:65], v6 offset:32768\n"
    "v_mfma_f32_16x16x32_bf16 v[40:43], v[52:55], v[144:147], v[40:43]\n"
    "ds_read_b64_tr_b16 v[66:67], v6 offset:33280\n"
    "v_mfma_f32_16x16x32_bf16 v[44:47], v[52:55], v[148:151], v[44:47]\n"
    "ds_read_b64_tr_b16 v[68:69], v7 offset:32768\n"
    "v_mfma_f32_16x16x32_bf16 v[36:39], v[48:51], v[148:151], v[36:39]\n"
    "s_waitcnt lgkmcnt(7)\n"
    "ds_read_b64_tr_b16 v[70:71], v7 offset:33280\n"
    "v_mfma_f32_16x16x32_bf16 v[32:35], v[56:59], v[152:155], v[32:35]\n"
    "s_waitcnt lgkmcnt(5)\n"
    "ds_read_b128 v[236:239], v12 offset:40960\n"
    "ds_read_b64_tr_b16 v[48:49], v6 offset:36864\n"
    "v_mfma_f32_16x16x32_bf16 v[40:43], v[60:63], v[152:155], v[40:43]\n"
    "s_setprio 1\n"
    "ds_read_b64_tr_b16 v[50:51], v6 offset:37376\n"
    "v_mfma_f32_16x16x32_bf16 v[44:47], v[60:63], v[156:159], v[44:47]\n"
    "ds_read_b64_tr_b16 v[52:53], v7 offset:36864\n"
    "v_mfma_f32_16x16x32_bf16 v[36:39], v[56:59], v[156:159], v[36:39]\n"
    "s_waitcnt lgkmcnt(4)\n"
    "ds_read_b64_tr_b16 v[54:55], v7 offset:37376\n"
    "v_mfma_f32_16x16x32_bf16 v[16:19], v[64:67], v[160:163], v[16:19]\n"
    "ds_read_b64_tr_b16 v[56:57], v6 offset:40960\n"
    "v_mfma_f32_16x16x32_bf16 v[24:27], v[68:71], v[160:163], v[24:27]\n"
    "ds_read_b64_tr_b16 v[58:59], v6 offset:41472\n"
    "v_mfma_f32_16x16x32_bf16 v[28:31], v[68:71], v[164:167], v[28:31]\n"
    "ds_read_b64_tr_b16 v[60:61], v7 offset:40960\n"
    "v_mfma_f32_16x16x32_bf16 v[20:23], v[64:67], v[164:167], v[20:23]\n"
    "s_waitcnt lgkmcnt(3)\n"
    "ds_read_b64_tr_b16 v[62:63], v7 offset:41472\n"
    "v_mfma_f32_16x16x32_bf16 v[16:19], v[48:51], v[168:171], v[16:19]\n"
    "ds_read_b64_tr_b16 v[64:65], v6 offset:45056\n"
    "v_mfma_f32_16x16x32_bf16 v[24:27], v[52:55], v[168:171], v[24:27]\n"
    "ds_read_b64_tr_b16 v[66:67], v6 offset:45568\n"
    "v_mfma_f32_16x16x32_bf16 v[28:31], v[52:55], v[172:175], v[28:31]\n"
    "s_setprio 0\n"
    "ds_read_b64_tr_b16 v[68:69], v7 offset:45056\n"
    "v_mfma_f32_16x16x32_bf16 v[20:23], v[48:51], v[172:175], v[20:23]\n"
    "ds_read_b64_tr_b16 v[70:71], v7 offset:45568\n"
    "s_barrier\n"
    "s_setprio 2\n"
    "s_waitcnt lgkmcnt(4)\n"
    "v_mfma_f32_16x16x32_bf16 v[32:35], v[56:59], v[160:163], v[32:35]\n"
    "ds_read_b64_tr_b16 v[48:49], v6 offset:49152\n"
    "ds_read_b32 v114, v74 offset:57344\n"
    "v_mfma_f32_16x16x32_bf16 v[40:43], v[60:63], v[160:163], v[40:43]\n"
    "ds_read_b64_tr_b16 v[50:51], v6 offset:49664\n"
    "v_mfma_f32_16x16x32_bf16 v[44:47], v[60:63], v[164:167], v[44:47]\n"
    "Lst1_3:\n"
    "s_cbranch_execz Lr1_3\n"
    "ds_read_b64_tr_b16 v[52:53], v7 offset:49152\n"
    "ds_read_b32 v115, v75 offset:57344\n"
    "v_mfma_f32_16x16x32_bf16 v[36:39], v[56:59], v[164:167], v[36:39]\n"
    "s_waitcnt lgkmcnt(5)\n"
    "ds_read_b64_tr_b16 v[54:55], v7 offset:49664\n"
    "v_mfma_f32_16x16x32_bf16 v[32:35], v[64:67], v[168:171], v[32:35]\n"
    "ds_read_b64_tr_b16 v[56:57], v6 offset:53248\n"
    "v_mfma_f32_16x16x32_bf16 v[40:43], v[68:71], v[168:171], v[40:43]\n"
    "ds_read_b64_tr_b16 v[58:59], v6 offset:53760\n"
    "v_mfma_f32_16x16x32_bf16 v[44:47], v[68:71], v[172:175], v[44:47]\n"
    "ds_read_b64_tr_b16 v[60:61], v7 offset:53248\n"
    "v_mfma_f32_16x16x32_bf16 v[36:39], v[64:67], v[172:175], v[36:39]\n"
    "s_waitcnt lgkmcnt(3)\n"
    "ds_read_b64_tr_b16 v[62:63], v7 offset:53760\n"
    "v_mfma_f32_16x16x32_bf16 v[16:19], v[48:51], v[176:179], v[16:19]\n"
    "s_setprio 1\n"
    "ds_read_b64_tr_b16 v[64:65], v6 offset:0\n"
    "v_rsq_f32 v116, v114\n"
    "v_mfma_f32_16x16x32_bf16 v[24:27], v[52:55], v[176:179], v[24:27]\n"
    "ds_read_b64_tr_b16 v[66:67], v6 offset:512\n"
    "v_mfma_f32_16x16x32_bf16 v[28:31], v[52:55], v[180:183], v[28:31]\n"
    "ds_read_b64_tr_b16 v[68:69], v7 offset:0\n"
    "v_rsq_f32 v117, v115\n"
    "v_mfma_f32_16x16x32_bf16 v[20:23], v[48:51], v[180:183], v[20:23]\n"
    "s_waitcnt lgkmcnt(3)\n"
    "ds_read_b64_tr_b16 v[70:71], v7 offset:512\n"
    "v_mfma_f32_16x16x32_bf16 v[16:19], v[56:59], v[184:187], v[16:19]\n"
    "ds_read_b64_tr_b16 v[48:49], v6 offset:4096\n"
    "v_mfma_f32_16x16x32_bf16 v[24:27], v[60:63], v[184:187], v[24:27]\n"
    "ds_read_b64_tr_b16 v[50:51], v6 offset:4608\n"
    "v_mul_f32 v112, 0xbfb8aa3b, v116\n"
    "v_mfma_f32_16x16x32_bf16 v[28:31], v[60:63], v[188:191], v[28:31]\n"
    "s_setprio 0\n"
    "ds_read_b64_tr_b16 v[52:53], v7 offset:4096\n"
    "v_mul_f32 v113, 0xbfb8aa3b, v117\n"
    "v_mfma_f32_16x16x32_bf16 v[20:23], v[56:59], v[188:191], v[20:23]\n"
    "ds_read_b64_tr_b16 v[54:55], v7 offset:4608\n"
    "v_mul_f32 v116, s84, v116\n"
    "s_barrier\n"
    "s_setprio 2\n"
    "s_waitcnt lgkmcnt(4)\n"
    "v_mfma_f32_16x16x32_bf16 v[32:35], v[64:67], v[176:179], v[32:35]\n"
    "ds_read_b64_tr_b16 v[56:57], v6 offset:8192\n"
    "v_mul_f32 v117, s84, v117\n"
    "v_mfma_f32_16x16x32_bf16 v[40:43], v[68:71], v[176:179], v[40:43]\n"
    "ds_read_b64_tr_b16 v[58:59], v6 offset:8704\n"
    "v_mfma_f32_16x16x32_bf16 v[44:47], v[68:71], v[180:183], v[44:47]\n"
    "ds_read_b64_tr_b16 v[60:61], v7 offset:8192\n"
    "v_mfma_f32_16x16x32_bf16 v[36:39], v[64:67], v[180:183], v[36:39]\n"
    "s_waitcnt lgkmcnt(3)\n"
    "ds_read_b64_tr_b16 v[62:63], v7 offset:8704\n"
    "v_mfma_f32_16x16x32_bf16 v[32:35], v[48:51], v[184:187], v[32:35]\n"
    "s_setprio 1\n"
    "ds_read_b64_tr_b16 v[64:65], v6 offset:12288\n"
    "s_and_b32 s65, s27, 1\n"
    "v_mfma_f32_16x16x32_bf16 v[40:43], v[52:55], v[184:187], v[40:43]\n"
    "s_lshl_b32 s66, s26, 1\n"
    "ds_read_b64_tr_b16 v[66:67], v6 offset:12800\n"
    "v_mfma_f32_16x16x32_bf16 v[44:47], v[52:55], v[188:191], v[44:47]\n"
    "s_add_u32 s65, s65, s66\n"
    "ds_read_b64_tr_b16 v[68:69], v7 offset:12288\n"
    "v_mfma_f32_16x16x32_bf16 v[36:39], v[48:51], v[188:191], v[36:39]\n"
    "s_setprio 0\n"
    "ds_read_b64_tr_b16 v[70:71], v7 offset:12800\n"
    "s_lshr_b32 s91, s27, 1\n"
    "s_barrier\n"
    "s_setprio 2\n"
    "s_waitcnt lgkmcnt(4)\n"
    "v_mfma_f32_16x16x32_bf16 v[16:19], v[56:59], v[192:195], v[16:19]\n"
    "s_lshl_b32 s90, s65, 18\n"
    "v_add_u32 v6, 0x10000, v6\n"
    "ds_read_b64_tr_b16 v[48:49], v6 offset:0\n"
    "v_mfma_f32_16x16x32_bf16 v[24:27], v[60:63], v[192:195], v[24:27]\n"
    "s_add_u32 s90, s90, s17\n"
    "ds_read_b64_tr_b16 v[50:51], v6 offset:512\n"
    "v_mfma_f32_16x16x32_bf16 v[28:31], v[60:63], v[196:199], v[28:31]\n"
    "s_add_u32 s90, s90, 0x210000\n"
    "v_add_u32 v7, 0x10000, v7\n"
    "ds_read_b64_tr_b16 v[52:53], v7 offset:0\n"
    "v_mfma_f32_16x16x32_bf16 v[20:23], v[56:59], v[196:199], v[20:23]\n"
    "s_waitcnt lgkmcnt(3)\n"
    "ds_read_b64_tr_b16 v[54:55], v7 offset:512\n"
    "v_mfma_f32_16x16x32_bf16 v[16:19], v[64:67], v[200:203], v[16:19]\n"
    "s_and_b32 s66, s25, 3\n"
    "ds_read_b64_tr_b16 v[56:57], v6 offset:4096\n"
    "v_mfma_f32_16x16x32_bf16 v[24:27], v[68:71], v[200:203], v[24:27]\n"
    "s_lshl_b32 s67, s91, 2\n"
    "ds_read_b64_tr_b16 v[58:59], v6 offset:4608\n"
    "v_mfma_f32_16x16x32_bf16 v[28:31], v[68:71], v[204:207], v[28:31]\n"
    "s_add_u32 s67, s67, s66\n"
    "ds_read_b64_tr_b16 v[60:61], v7 offset:4096\n"
    "v_mfma_f32_16x16x32_bf16 v[20:23], v[64:67], v[204:207], v[20:23]\n"
    "s_lshl_b32 s92, s65, 6\n"
    "ds_read_b64_tr_b16 v[62:63], v7 offset:4608\n"
    "s_waitcnt lgkmcnt(4)\n"
    "v_mfma_f32_16x16x32_bf16 v[32:35], v[48:51], v[192:195], v[32:35]\n"
    "s_add_u32 s92, s92, s67\n"
    "ds_read_b64_tr_b16 v[64:65], v6 offset:8192\n"
    "v_mfma_f32_16x16x32_bf16 v[40:43], v[52:55], v[192:195], v[40:43]\n"
    "s_lshl_b32 s92, s92, 11\n"
    "ds_read_b64_tr_b16 v[66:67], v6 offset:8704\n"
    "s_waitcnt vmcnt(0)\n"
    "v_readfirstlane_b32 s60, v252\n"
    "v_mfma_f32_16x16x32_bf16 v[44:47], v[52:55], v[196:199], v[44:47]\n"
    "ds_read_b64_tr_b16 v[68:69], v7 offset:8192\n"
    "v_readfirstlane_b32 s61, v253\n"
    "v_mfma_f32_16x16x32_bf16 v[36:39], v[48:51], v[196:199], v[36:39]\n"
    "ds_read_b64_tr_b16 v[70:71], v7 offset:8704\n"
    "s_add_u32 s60, s60, 0x7f4a7c15\n"
    "s_addc_u32 s61, s61, 0x9e3779b9\n"
    "s_add_u32 s92, s92, 0x10000\n"
    "s_waitcnt lgkmcnt(4)\n"
    "v_mfma_f32_16x16x32_bf16 v[32:35], v[56:59], v[200:203], v[32:35]\n"
    "s_lshl_b32 s93, s67, 15\n"
    "ds_read_b64_tr_b16 v[48:49], v6 offset:12288\n"
    "v_mfma_f32_16x16x32_bf16 v[40:43], v[60:63], v[200:203], v[40:43]\n"
    "s_setprio 1\n"
    "ds_read_b64_tr_b16 v[50:51], v6 offset:12800\n"
    "s_lshl_b32 s7, s65, 8\n"
    "v_mov_b32 v252, s60\n"
    "v_mfma_f32_16x16x32_bf16 v[44:47], v[60:63], v[204:207], v[44:47]\n"
    "s_add_u32 s7, s7, s93\n"
    "ds_read_b64_tr_b16 v[52:53], v7 offset:12288\n"
    "v_mov_b32 v253, s61\n"
    "v_mfma_f32_16x16x32_bf16 v[36:39], v[56:59], v[204:207], v[36:39]\n"
    "s_add_u32 s7, s7, 0xd13000\n"
    "ds_read_b64_tr_b16 v[54:55], v7 offset:12800\n"
    "s_waitcnt lgkmcnt(4)\n"
    "v_mfma_f32_16x16x32_bf16 v[16:19], v[64:67], v[208:211], v[16:19]\n"
    "s_mul_i32 s66, s65, 768\n"
    "ds_read_b64_tr_b16 v[56:57], v6 offset:16384\n"
    "v_mfma_f32_16x16x32_bf16 v[24:27], v[68:71], v[208:211], v[24:27]\n"
    "ds_read_b64_tr_b16 v[58:59], v6 offset:16896\n"
    "v_mfma_f32_16x16x32_bf16 v[28:31], v[68:71], v[212:215], v[28:31]\n"
    "s_add_u32 s93, s93, s66\n"
    "ds_read_b64_tr_b16 v[60:61], v7 offset:16384\n"
    "v_mfma_f32_16x16x32_bf16 v[20:23], v[64:67], v[212:215], v[20:23]\n"
    "s_add_u32 s93, s93, 0xd10000\n"
    "ds_read_b64_tr_b16 v[62:63], v7 offset:16896\n"
    "s_waitcnt lgkmcnt(4)\n"
    "v_mfma_f32_16x16x32_bf16 v[16:19], v[48:51], v[216:219], v[16:19]\n"
    "s_lshl_b32 s94, s67, 4\n"
    "ds_read_b64_tr_b16 v[64:65], v6 offset:20480\n"
    "v_mfma_f32_16x16x32_bf16 v[24:27], v[52:55], v[216:219], v[24:27]\n"
    "s_add_u32 s94, s94, s65\n"
    "ds_read_b64_tr_b16 v[66:67], v6 offset:20992\n"
    "v_mfma_f32_16x16x32_bf16 v[28:31], v[52:55], v[220:223], v[28:31]\n"
    "s_setprio 0\n"
    "ds_read_b64_tr_b16 v[68:69], v7 offset:20480\n"
    "s_lshl_b32 s94, s94, 3\n"
    "v_mfma_f32_16x16x32_bf16 v[20:23], v[48:51], v[220:223], v[20:23]\n"
    "s_add_u32 s94, s94, 8192\n"
    "ds_read_b64_tr_b16 v[70:71], v7 offset:20992\n"
    "s_barrier\n"
    "s_setprio 2\n"
    "s_waitcnt lgkmcnt(4)\n"
    "v_mfma_f32_16x16x32_bf16 v[32:35], v[56:59], v[208:211], v[32:35]\n"
    "s_lshl_b32 s65, s26, 7\n"
    "ds_read_b64_tr_b16 v[48:49], v6 offset:24576\n"
    "v_mfma_f32_16x16x32_bf16 v[40:43], v[60:63], v[208:211], v[40:43]\n"
    "ds_read_b64_tr_b16 v[50:51], v6 offset:25088\n"
    "v_mfma_f32_16x16x32_bf16 v[44:47], v[60:63], v[212:215], v[44:47]\n"
    "s_lshl_b32 s66, s91, 3\n"
    "ds_read_b64_tr_b16 v[52:53], v7 offset:24576\n"
    "v_mfma_f32_16x16x32_bf16 v[36:39], v[56:59], v[212:215], v[36:39]\n"
    "s_add_u32 s66, s66, s65\n"
    "ds_read_b64_tr_b16 v[54:55], v7 offset:25088\n"
    "s_waitcnt lgkmcnt(4)\n"
    "v_mfma_f32_16x16x32_bf16 v[32:35], v[64:67], v[216:219], v[32:35]\n"
    "s_add_u32 s66, s66, 256\n"
    "ds_read_b64_tr_b16 v[56:57], v6 offset:28672\n"
    "v_mfma_f32_16x16x32_bf16 v[40:43], v[68:71], v[216:219], v[40:43]\n"
    "s_add_u32 s65, s65, 1280\n"
    "ds_read_b64_tr_b16 v[58:59], v6 offset:29184\n"
    "v_mfma_f32_16x16x32_bf16 v[44:47], v[68:71], v[220:223], v[44:47]\n"
    "ds_read_b64_tr_b16 v[60:61], v7 offset:28672\n"
    "v_and_b32 v250, 15, v0\n"
    "v_mfma_f32_16x16x32_bf16 v[36:39], v[64:67], v[220:223], v[36:39]\n"
    "s_waitcnt lgkmcnt(3)\n"
    "ds_read_b64_tr_b16 v[62:63], v7 offset:29184\n"
    "v_and_b32 v251, 16, v0\n"
    "v_mfma_f32_16x16x32_bf16 v[16:19], v[48:51], v[224:227], v[16:19]\n"
    "s_setprio 1\n"
    "v_add_u32 v6, 0xffff8000, v6\n"
    "ds_read_b64_tr_b16 v[64:65], v6 offset:0\n"
    "v_mfma_f32_16x16x32_bf16 v[24:27], v[52:55], v[224:227], v[24:27]\n"
    "ds_read_b64_tr_b16 v[66:67], v6 offset:512\n"
    "v_lshlrev_b32 v251, 6, v251\n"
    "v_mfma_f32_16x16x32_bf16 v[28:31], v[52:55], v[228:231], v[28:31]\n"
    "v_add_u32 v7, 0xffff8000, v7\n"
    "ds_read_b64_tr_b16 v[68:69], v7 offset:0\n"
    "v_lshl_add_u32 v250, v250, 3, v251\n"
    "v_mfma_f32_16x16x32_bf16 v[20:23], v[48:51], v[228:231], v[20:23]\n"
    "s_waitcnt lgkmcnt(3)\n"
    "ds_read_b64_tr_b16 v[70:71], v7 offset:512\n"
    "v_add_u32 v250, s65, v250\n"
    "v_mfma_f32_16x16x32_bf16 v[16:19], v[56:59], v[232:235], v[16:19]\n"
    "ds_read_b64_tr_b16 v[48:49], v6 offset:4096\n"
    "v_mov_b32 v251, s66\n"
    "v_mfma_f32_16x16x32_bf16 v[24:27], v[60:63], v[232:235], v[24:27]\n"
    "ds_read_b64_tr_b16 v[50:51], v6 offset:4608\n"
    "v_cmp_gt_u32 vcc, 32, v0\n"
    "v_mfma_f32_16x16x32_bf16 v[28:31], v[60:63], v[236:239], v[28:31]\n"
    "s_setprio 0\n"
    "ds_read_b64_tr_b16 v[52:53], v7 offset:4096\n"
    "v_cndmask_b32 v248, v251, v250, vcc\n"
    "v_mfma_f32_16x16x32_bf16 v[20:23], v[56:59], v[236:239], v[20:23]\n"
    "ds_read_b64_tr_b16 v[54:55], v7 offset:4608\n"
    "s_barrier\n"
    "s_setprio 2\n"
    "s_waitcnt lgkmcnt(4)\n"
    "v_mfma_f32_16x16x32_bf16 v[32:35], v[64:67], v[224:227], v[32:35]\n"
    "ds_read_b64_tr_b16 v[56:57], v5 offset:16384\n"
    "v_mfma_f32_16x16x32_bf16 v[40:43], v[68:71], v[224:227], v[40:43]\n"
    "ds_read_b64_tr_b16 v[58:59], v5 offset:16640\n"
    "v_mfma_f32_16x16x32_bf16 v[44:47], v[68:71], v[228:231], v[44:47]\n"
    "ds_read_b64_tr_b16 v[60:61], v5 offset:20480\n"
    "v_mfma_f32_16x16x32_bf16 v[36:39], v[64:67], v[228:231], v[36:39]\n"
    "s_waitcnt lgkmcnt(3)\n"
    "ds_read_b64_tr_b16 v[62:63], v5 offset:20736\n"
    "v_mul_f32 v16, v16, v116\n"
    "v_mul_f32 v17, v17, v116\n"
    "v_mul_f32 v18, v18, v116\n"
    "v_mfma_f32_16x16x32_bf16 v[32:35], v[48:51], v[232:235], v[32:35]\n"
    "ds_read_b64_tr_b16 v[64:65], v5 offset:18432\n"
    "v_mul_f32 v19, v19, v116\n"
    "v_mul_f32 v24, v24, v116\n"
    "v_mfma_f32_16x16x32_bf16 v[40:43], v[52:55], v[232:235], v[40:43]\n"
    "ds_read_b64_tr_b16 v[66:67], v5 offset:18688\n"
    "v_mul_f32 v25, v25, v116\n"
    "v_mul_f32 v26, v26, v116\n"
    "v_mul_f32 v27, v27, v116\n"
    "v_mfma_f32_16x16x32_bf16 v[44:47], v[52:55], v[236:239], v[44:47]\n"
    "ds_read_b64_tr_b16 v[68:69], v5 offset:22528\n"
    "v_cvt_pk_bf16_f32 v80, v16, v17\n"
    "v_cvt_pk_bf16_f32 v81, v18, v19\n"
    "v_mfma_f32_16x16x32_bf16 v[36:39], v[48:51], v[236:239], v[36:39]\n"
    "s_waitcnt lgkmcnt(3)\n"
    "ds_read_b64_tr_b16 v[70:71], v5 offset:22784\n"
    "v_cvt_pk_bf16_f32 v82, v24, v25\n"
    "v_cvt_pk_bf16_f32 v83, v26, v27\n"
    "v_cndmask_b32 v82, v82, v118, s[86:87]\n"
    "v_mfma_f32_16x16x32_bf16 v[96:99], v[56:59], v[128:131], 0\n"
    "ds_read_b64_tr_b16 v[48:49], v5 offset:24576\n"
    "v_cndmask_b32 v83, v83, 0, s[86:87]\n"
    "v_mul_f32 v20, v20, v117\n"
    "v_mfma_f32_16x16x32_bf16 v[104:107], v[60:63], v[128:131], 0\n"
    "ds_read_b64_tr_b16 v[50:51], v5 offset:24832\n"
    "v_mul_f32 v21, v21, v117\n"
    "v_mul_f32 v22, v22, v117\n"
    "v_mul_f32 v23, v23, v117\n"
    "v_mfma_f32_16x16x32_bf16 v[108:111], v[60:63], v[132:135], 0\n"
    "ds_read_b64_tr_b16 v[52:53], v5 offset:28672\n"
    "v_mul_f32 v28, v28, v117\n"
    "v_mul_f32 v29, v29, v117\n"
    "v_mfma_f32_16x16x32_bf16 v[100:103], v[56:59], v[132:135], 0\n"
    "s_waitcnt lgkmcnt(3)\n"
    "ds_read_b64_tr_b16 v[54:55], v5 offset:28928\n"
    "v_mul_f32 v30, v30, v117\n"
    "v_mul_f32 v31, v31, v117\n"
    "v_cvt_pk_bf16_f32 v84, v20, v21\n"
    "v_mfma_f32_16x16x32_bf16 v[96:99], v[64:67], v[136:139], v[96:99]\n"
    "ds_read_b64_tr_b16 v[56:57], v5 offset:26624\n"
    "v_cvt_pk_bf16_f32 v85, v22, v23\n"
    "v_cvt_pk_bf16_f32 v86, v28, v29\n"
    "v_mfma_f32_16x16x32_bf16 v[104:107], v[68:71], v[136:139], v[104:107]\n"
    "s_setprio 1\n"
    "ds_read_b64_tr_b16 v[58:59], v5 offset:26880\n"
    "v_cvt_pk_bf16_f32 v87, v30, v31\n"
    "v_cndmask_b32 v86, v86, v118, s[86:87]\n"
    "v_cndmask_b32 v87, v87, 0, s[86:87]\n"
    "v_mfma_f32_16x16x32_bf16 v[108:111], v[68:71], v[140:143], v[108:111]\n"
    "ds_read_b64_tr_b16 v[60:61], v5 offset:30720\n"
    "buffer_store_dwordx4 v[80:83], v10, s[56:59], s74 offen offset:0 sc1\n"
    "buffer_store_dwordx4 v[84:87], v10, s[56:59], s74 offen offset:128 sc1\n"
    "v_mfma_f32_16x16x32_bf16 v[100:103], v[64:67], v[140:143], v[100:103]\n"
    "s_waitcnt lgkmcnt(3)\n"
    "ds_read_b64_tr_b16 v[62:63], v5 offset:30976\n"
    "v_mul_f32 v32, v32, v116\n"
    "v_mul_f32 v33, v33, v116\n"
    "v_mfma_f32_16x16x32_bf16 v[96:99], v[48:51], v[144:147], v[96:99]\n"
    "v_add_u32 v5, 0x18000, v5\n"
    "ds_read_b64_tr_b16 v[64:65], v5 offset:0\n"
    "v_mul_f32 v34, v34, v116\n"
    "v_mul_f32 v35, v35, v116\n"
    "v_mfma_f32_16x16x32_bf16 v[104:107], v[52:55], v[144:147], v[104:107]\n"
    "ds_read_b64_tr_b16 v[66:67], v5 offset:256\n"
    "v_mul_f32 v40, v40, v116\n"
    "v_mfma_f32_16x16x32_bf16 v[108:111], v[52:55], v[148:151], v[108:111]\n"
    "ds_read_b64_tr_b16 v[68:69], v5 offset:4096\n"
    "v_mul_f32 v41, v41, v116\n"
    "v_mul_f32 v42, v42, v116\n"
    "v_mfma_f32_16x16x32_bf16 v[100:103], v[48:51], v[148:151], v[100:103]\n"
    "s_waitcnt lgkmcnt(3)\n"
    "ds_read_b64_tr_b16 v[70:71], v5 offset:4352\n"
    "v_mul_f32 v43, v43, v116\n"
    "v_cvt_pk_bf16_f32 v80, v32, v33\n"
    "v_mfma_f32_16x16x32_bf16 v[96:99], v[56:59], v[152:155], v[96:99]\n"
    "ds_read_b64_tr_b16 v[48:49], v5 offset:2048\n"
    "v_cvt_pk_bf16_f32 v81, v34, v35\n"
    "v_mfma_f32_16x16x32_bf16 v[104:107], v[60:63], v[152:155], v[104:107]\n"
    "ds_read_b64_tr_b16 v[50:51], v5 offset:2304\n"
    "v_cvt_pk_bf16_f32 v82, v40, v41\n"
    "v_cvt_pk_bf16_f32 v83, v42, v43\n"
    "v_mfma_f32_16x16x32_bf16 v[108:111], v[60:63], v[156:159], v[108:111]\n"
    "s_setprio 0\n"
    "ds_read_b64_tr_b16 v[52:53], v5 offset:6144\n"
    "v_cndmask_b32 v82, v82, v118, s[86:87]\n"
    "v_cndmask_b32 v83, v83, 0, s[86:87]\n"
    "v_mfma_f32_16x16x32_bf16 v[100:103], v[56:59], v[156:159], v[100:103]\n"
    "ds_read_b64_tr_b16 v[54:55], v5 offset:6400\n"
    "v_mul_f32 v36, v36, v117\n"
    "s_barrier\n"
    "s_setprio 2\n"
    "s_waitcnt lgkmcnt(4)\n"
    "v_mfma_f32_16x16x32_bf16 v[96:99], v[64:67], v[160:163], v[96:99]\n"
    "ds_read_b64_tr_b16 v[56:57], v5 offset:8192\n"
    "v_mul_f32 v37, v37, v117\n"
    "v_mul_f32 v38, v38, v117\n"
    "v_mfma_f32_16x16x32_bf16 v[104:107], v[68:71], v[160:163], v[104:107]\n"
    "ds_read_b64_tr_b16 v[58:59], v5 offset:8448\n"
    "v_mul_f32 v39, v39, v117\n"
    "v_mul_f32 v44, v44, v117\n"
    "v_mfma_f32_16x16x32_bf16 v[108:111], v[68:71], v[164:167], v[108:111]\n"
    "ds_read_b64_tr_b16 v[60:61], v5 offset:12288\n"
    "v_mul_f32 v45, v45, v117\n"
    "v_mfma_f32_16x16x32_bf16 v[100:103], v[64:67], v[164:167], v[100:103]\n"
    "s_waitcnt lgkmcnt(3)\n"
    "ds_read_b64_tr_b16 v[62:63], v5 offset:12544\n"
    "v_mul_f32 v46, v46, v117\n"
    "v_mul_f32 v47, v47, v117\n"
    "v_mfma_f32_16x16x32_bf16 v[96:99], v[48:51], v[168:171], v[96:99]\n"
    "ds_read_b64_tr_b16 v[64:65], v5 offset:10240\n"
    "v_cvt_pk_bf16_f32 v84, v36, v37\n"
    "v_cvt_pk_bf16_f32 v85, v38, v39\n"
    "v_mfma_f32_16x16x32_bf16 v[104:107], v[52:55], v[168:171], v[104:107]\n"
    "ds_read_b64_tr_b16 v[66:67], v5 offset:10496\n"
    "v_cvt_pk_bf16_f32 v86, v44, v45\n"
    "v_mfma_f32_16x16x32_bf16 v[108:111], v[52:55], v[172:175], v[108:111]\n"
    "ds_read_b64_tr_b16 v[68:69], v5 offset:14336\n"
    "v_cvt_pk_bf16_f32 v87, v46, v47\n"
    "v_cndmask_b32 v86, v86, v118, s[86:87]\n"
    "v_mfma_f32_16x16x32_bf16 v[100:103], v[48:51], v[172:175], v[100:103]\n"
    "s_waitcnt lgkmcnt(3)\n"
    "ds_read_b64_tr_b16 v[70:71], v5 offset:14592\n"
    "buffer_store_dwordx4 v[80:83], v10, s[56:59], s75 offen offset:0 sc1\n"
    "v_cndmask_b32 v87, v87, 0, s[86:87]\n"
    "v_mfma_f32_16x16x32_bf16 v[96:99], v[56:59], v[176:179], v[96:99]\n"
    "v_add_u32 v5, 0xffff0000, v5\n"
    "ds_read_b64_tr_b16 v[48:49], v5 offset:8192\n"
    "buffer_store_dwordx4 v[84:87], v10, s[56:59], s75 offen offset:128 sc1\n"
    "v_mfma_f32_16x16x32_bf16 v[104:107], v[60:63], v[176:179], v[104:107]\n"
    "s_and_b32 s65, s27, 3\n"
    "ds_read_b64_tr_b16 v[50:51], v5 offset:8448\n"
    "s_lshl_b32 s66, s26, 2\n"
    "s_add_u32 s65, s65, s66\n"
    "v_mfma_f32_16x16x32_bf16 v[108:111], v[60:63], v[180:183], v[108:111]\n"
    "s_lshr_b32 s97, s27, 2\n"
    "ds_read_b64_tr_b16 v[52:53], v5 offset:12288\n"
    "s_lshl_b32 s97, s97, 7\n"
    "v_mfma_f32_16x16x32_bf16 v[100:103], v[56:59], v[180:183], v[100:103]\n"
    "s_mul_i32 s99, s65, 57344\n"
    "ds_read_b64_tr_b16 v[54:55], v5 offset:12544\n"
    "s_add_u32 s99, s99, s97\n"
    "s_waitcnt lgkmcnt(4)\n"
    "v_mfma_f32_16x16x32_bf16 v[96:99], v[64:67], v[184:187], v[96:99]\n"
    "s_lshr_b32 s66, s65, 1\n"
    "ds_read_b64_tr_b16 v[56:57], v5 offset:10240\n"
    "s_lshl_b32 s66, s66, 7\n"
    "v_mfma_f32_16x16x32_bf16 v[104:107], v[68:71], v[184:187], v[104:107]\n"
    "s_add_u32 s66, s66, 4096\n"
    "ds_read_b64_tr_b16 v[58:59], v5 offset:10496\n"
    "v_and_b32 v12, 15, v0\n"
    "v_mfma_f32_16x16x32_bf16 v[108:111], v[68:71], v[188:191], v[108:111]\n"
    "s_lshr_b32 s67, s25, 1\n"
    "ds_read_b64_tr_b16 v[60:61], v5 offset:14336\n"
    "s_lshl_b32 s65, s65, 1\n"
    "v_lshl_add_u32 v75, v12, 3, s66\n"
    "v_mfma_f32_16x16x32_bf16 v[100:103], v[64:67], v[188:191], v[100:103]\n"
    "s_and_b32 s66, s25, 1\n"
    "ds_read_b64_tr_b16 v[62:63], v5 offset:14592\n"
    "s_add_u32 s65, s65, s66\n"
    "s_waitcnt lgkmcnt(4)\n"
    "v_mfma_f32_16x16x32_bf16 v[96:99], v[48:51], v[192:195], v[96:99]\n"
    "s_lshl_b32 s96, s65, 7\n"
    "v_add_u32 v5, 0xffff8000, v5\n"
    "ds_read_b64_tr_b16 v[64:65], v5 offset:0\n"
    "v_mfma_f32_16x16x32_bf16 v[104:107], v[52:55], v[192:195], v[104:107]\n"
    "s_add_u32 s96, s96, 8192\n"
    "ds_read_b64_tr_b16 v[66:67], v5 offset:256\n"
    "v_lshl_add_u32 v7, v12, 3, s96\n"
    "v_and_b32 v12, 3, v0\n"
    "v_mfma_f32_16x16x32_bf16 v[108:111], v[52:55], v[196:199], v[108:111]\n"
    "s_mul_i32 s96, s65, 114688\n"
    "ds_read_b64_tr_b16 v[68:69], v5 offset:4096\n"
    "s_mul_i32 s98, s67, 14336\n"
    "v_mfma_f32_16x16x32_bf16 v[100:103], v[48:51], v[196:199], v[100:103]\n"
    "s_add_u32 s96, s96, s98\n"
    "ds_read_b64_tr_b16 v[70:71], v5 offset:4352\n"
    "s_add_u32 s96, s96, 0x610000\n"
    "s_waitcnt lgkmcnt(4)\n"
    "v_mfma_f32_16x16x32_bf16 v[96:99], v[56:59], v[200:203], v[96:99]\n"
    "s_add_u32 s21, s96, 57344\n"
    "ds_read_b64_tr_b16 v[48:49], v5 offset:2048\n"
    "s_mul_i32 s98, s67, 3072\n"
    "s_lshl_b32 s65, s65, 15\n"
    "v_mfma_f32_16x16x32_bf16 v[104:107], v[60:63], v[200:203], v[104:107]\n"
    "s_add_u32 s98, s98, s65\n"
    "ds_read_b64_tr_b16 v[50:51], v5 offset:2304\n"
    "v_mfma_f32_16x16x32_bf16 v[108:111], v[60:63], v[204:207], v[108:111]\n"
    "s_add_u32 s98, s98, 0xd10000\n"
    "ds_read_b64_tr_b16 v[52:53], v5 offset:6144\n"
    "v_lshrrev_b32 v13, 3, v0\n"
    "v_and_b32 v14, 3, v13\n"
    "v_mfma_f32_16x16x32_bf16 v[100:103], v[56:59], v[204:207], v[100:103]\n"
    "s_waitcnt lgkmcnt(3)\n"
    "ds_read_b64_tr_b16 v[54:55], v5 offset:6400\n"
    "v_lshlrev_b32 v14, 5, v14\n"
    "v_lshl_add_u32 v14, v12, 3, v14\n"
    "v_mfma_f32_16x16x32_bf16 v[96:99], v[64:67], v[208:211], v[96:99]\n"
    "v_add_u32 v5, 0x1c000, v5\n"
    "ds_read_b64_tr_b16 v[56:57], v5 offset:0\n"
    "v_lshrrev_b32 v12, 2, v0\n"
    "v_lshlrev_b32 v12, 7, v12\n"
    "v_mfma_f32_16x16x32_bf16 v[104:107], v[68:71], v[208:211], v[104:107]\n"
    "s_lshl_b32 s65, s67, 12\n"
    "ds_read_b64_tr_b16 v[58:59], v5 offset:256\n"
    "v_add3_u32 v124, v12, v14, s65\n"
    "v_mfma_f32_16x16x32_bf16 v[108:111], v[68:71], v[212:215], v[108:111]\n"
    "s_setprio 1\n"
    "ds_read_b64_tr_b16 v[60:61], v5 offset:4096\n"
    "v_xor_b32 v125, 32, v124\n"
    "v_xor_b32 v126, 64, v124\n"
    "v_xor_b32 v127, 0x60, v124\n"
    "v_mfma_f32_16x16x32_bf16 v[100:103], v[64:67], v[212:215], v[100:103]\n"
    "s_waitcnt lgkmcnt(3)\n"
    "ds_read_b64_tr_b16 v[62:63], v5 offset:4352\n"
    "v_and_b32 v12, 7, v0\n"
    "v_and_b32 v14, 6, v13\n"
    "v_mfma_f32_16x16x32_bf16 v[96:99], v[48:51], v[216:219], v[96:99]\n"
    "ds_read_b64_tr_b16 v[64:65], v5 offset:2048\n"
    "v_xor_b32 v14, v14, v12\n"
    "v_mul_u32_u24 v116, 896, v13\n"
    "v_mfma_f32_16x16x32_bf16 v[104:107], v[52:55], v[216:219], v[104:107]\n"
    "s_and_b32 s67, s67, 1\n"
    "ds_read_b64_tr_b16 v[66:67], v5 offset:2304\n"
    "v_lshl_add_u32 v14, v14, 4, v116\n"
    "v_mfma_f32_16x16x32_bf16 v[108:111], v[52:55], v[220:223], v[108:111]\n"
    "s_mul_i32 s65, s67, 448\n"
    "ds_read_b64_tr_b16 v[68:69], v5 offset:6144\n"
    "s_lshl_b32 s66, s66, 3\n"
    "v_mfma_f32_16x16x32_bf16 v[100:103], v[48:51], v[220:223], v[100:103]\n"
    "s_add_u32 s65, s65, s66\n"
    "ds_read_b64_tr_b16 v[70:71], v5 offset:6400\n"
    "s_add_u32 s65, s65, s66\n"
    "s_waitcnt lgkmcnt(4)\n"
    "v_mfma_f32_16x16x32_bf16 v[96:99], v[56:59], v[224:227], v[96:99]\n"
    "s_mul_i32 s65, s65, 896\n"
    "ds_read_b64_tr_b16 v[48:49], v8 offset:49152\n"
    "v_add_u32 v2, s65, v14\n"
    "s_mul_i32 s65, s67, 112\n"
    "v_mfma_f32_16x16x32_bf16 v[104:107], v[60:63], v[224:227], v[104:107]\n"
    "s_add_u32 s65, s65, s66\n"
    "ds_read_b64_tr_b16 v[50:51], v8 offset:49664\n"
    "v_mfma_f32_16x16x32_bf16 v[108:111], v[60:63], v[228:231], v[108:111]\n"
    "s_mul_i32 s65, s65, 896\n"
    "ds_read_b64_tr_b16 v[52:53], v8 offset:57344\n"
    "v_add_u32 v4, s65, v14\n"
    "s_mul_i32 s65, s25, 7168\n"
    "v_mfma_f32_16x16x32_bf16 v[100:103], v[56:59], v[228:231], v[100:103]\n"
    "s_waitcnt lgkmcnt(3)\n"
    "ds_read_b64_tr_b16 v[54:55], v8 offset:57856\n"
    "v_lshl_add_u32 v14, v12, 4, v116\n"
    "v_add_u32 v6, s65, v14\n"
    "v_mfma_f32_16x16x32_bf16 v[96:99], v[64:67], v[232:235], v[96:99]\n"
    "s_lshl_b32 s65, s79, 13\n"
    "ds_read_b64_tr_b16 v[56:57], v8 offset:53248\n"
    "v_add_u32 v78, s65, v1\n"
    "v_mfma_f32_16x16x32_bf16 v[104:107], v[68:71], v[232:235], v[104:107]\n"
    "s_and_b32 s65, s25, 3\n"
    "ds_read_b64_tr_b16 v[58:59], v8 offset:53760\n"
    "v_add_u32 v73, 0xc000, v78\n"
    "v_add_u32 v74, 0xc000, v73\n"
    "v_mfma_f32_16x16x32_bf16 v[108:111], v[68:71], v[236:239], v[108:111]\n"
    "s_lshl_b32 s65, s65, 12\n"
    "ds_read_b64_tr_b16 v[60:61], v8 offset:61440\n"
    "s_add_u32 s65, s65, 0x18000\n"
    "v_mfma_f32_16x16x32_bf16 v[100:103], v[64:67], v[236:239], v[100:103]\n"
    "s_waitcnt lgkmcnt(3)\n"
    "ds_read_b64_tr_b16 v[62:63], v8 offset:61952\n"
    "v_add_u32 v76, s65, v1\n"
    "v_and_b32 v13, 1, v13\n"
    "v_mfma_f32_16x16x32_bf16 v[16:19], v[48:51], v[128:131], 0\n"
    "s_and_b32 s65, s27, 1\n"
    "v_add_u32 v8, 0x10000, v8\n"
    "ds_read_b64_tr_b16 v[64:65], v8 offset:0\n"
    "v_xor_b32 v13, s65, v13\n"
    "v_mfma_f32_16x16x32_bf16 v[24:27], v[52:55], v[128:131], 0\n"
    "ds_read_b64_tr_b16 v[66:67], v8 offset:512\n"
    "v_lshrrev_b32 v14, 4, v0\n"
    "v_lshlrev_b32 v12, 4, v12\n"
    "v_mfma_f32_16x16x32_bf16 v[28:31], v[52:55], v[132:135], 0\n"
    "ds_read_b64_tr_b16 v[68:69], v8 offset:8192\n"
    "v_lshl_add_u32 v12, v14, 7, v12\n"
    "v_lshl_add_u32 v77, v13, 14, v12\n"
    "v_mfma_f32_16x16x32_bf16 v[20:23], v[48:51], v[132:135], 0\n"
    "s_waitcnt lgkmcnt(3)\n"
    "ds_read_b64_tr_b16 v[70:71], v8 offset:8704\n"
    "v_mul_f32 v88, v96, v112\n"
    "v_mul_f32 v89, v97, v112\n"
    "v_mfma_f32_16x16x32_bf16 v[16:19], v[56:59], v[136:139], v[16:19]\n"
    "s_setprio 0\n"
    "ds_read_b64_tr_b16 v[48:49], v8 offset:4096\n"
    "v_mul_f32 v90, v98, v112\n"
    "v_mfma_f32_16x16x32_bf16 v[24:27], v[60:63], v[136:139], v[24:27]\n"
    "ds_read_b64_tr_b16 v[50:51], v8 offset:4608\n"
    "v_mul_f32 v91, v99, v112\n"
    "v_exp_f32 v88, v88\n"
    "v_mfma_f32_16x16x32_bf16 v[28:31], v[60:63], v[140:143], v[28:31]\n"
    "ds_read_b64_tr_b16 v[52:53], v8 offset:12288\n"
    "v_mfma_f32_16x16x32_bf16 v[20:23], v[56:59], v[140:143], v[20:23]\n"
    "ds_read_b64_tr_b16 v[54:55], v8 offset:12800\n"
    "v_mul_f32 v104, v96, v104\n"
    "v_exp_f32 v89, v89\n"
    "s_waitcnt vmcnt(0)\n"
    "s_barrier\n"
    "s_setprio 2\n"
    "s_waitcnt lgkmcnt(4)\n"
    "v_mfma_f32_16x16x32_bf16 v[16:19], v[64:67], v[144:147], v[16:19]\n"
    "s_cmp_lg_u32 s25, 4\n"
    "ds_read_b64_tr_b16 v[56:57], v9 offset:49152\n"
    "s_cbranch_scc1 Lpub_20\n"
    "s_mov_b64 exec, 1\n"
    "buffer_store_dwordx2 v[252:253], off, s[56:59], s77 sc1\n"
    "buffer_store_dwordx2 v[252:253], off, s[56:59], s78 sc1\n"
    "s_mov_b64 exec, -1\n"
    "Lpub_20:\n"
    "v_mfma_f32_16x16x32_bf16 v[24:27], v[68:71], v[144:147], v[24:27]\n"
    "ds_read_b64_tr_b16 v[58:59], v9 offset:49664\n"
    "v_mul_f32 v105, v97, v105\n"
    "v_exp_f32 v90, v90\n"
    "v_mfma_f32_16x16x32_bf16 v[28:31], v[68:71], v[148:151], v[28:31]\n"
    "ds_read_b64_tr_b16 v[60:61], v9 offset:57344\n"
    "v_mfma_f32_16x16x32_bf16 v[20:23], v[64:67], v[148:151], v[20:23]\n"
    "s_waitcnt lgkmcnt(3)\n"
    "ds_read_b64_tr_b16 v[62:63], v9 offset:57856\n"
    "v_mul_f32 v106, v98, v106\n"
    "v_exp_f32 v91, v91\n"
    "v_mfma_f32_16x16x32_bf16 v[16:19], v[48:51], v[152:155], v[16:19]\n"
    "ds_read_b64_tr_b16 v[64:65], v9 offset:53248\n"
    "v_mfma_f32_16x16x32_bf16 v[24:27], v[52:55], v[152:155], v[24:27]\n"
    "ds_read_b64_tr_b16 v[66:67], v9 offset:53760\n"
    "v_mul_f32 v107, v99, v107\n"
    "v_mfma_f32_16x16x32_bf16 v[28:31], v[52:55], v[156:159], v[28:31]\n"
    "ds_read_b64_tr_b16 v[68:69], v9 offset:61440\n"
    "v_fma_f32 v88, v88, v114, v114\n"
    "v_fma_f32 v89, v89, v114, v114\n"
    "v_mfma_f32_16x16x32_bf16 v[20:23], v[48:51], v[156:159], v[20:23]\n"
    "s_waitcnt lgkmcnt(3)\n"
    "ds_read_b64_tr_b16 v[70:71], v9 offset:61952\n"
    "v_fma_f32 v90, v90, v114, v114\n"
    "v_mfma_f32_16x16x32_bf16 v[32:35], v[56:59], v[128:131], 0\n"
    "s_setprio 1\n"
    "v_add_u32 v9, 0x10000, v9\n"
    "ds_read_b64_tr_b16 v[48:49], v9 offset:0\n"
    "v_fma_f32 v91, v91, v114, v114\n"
    "v_rcp_f32 v88, v88\n"
    "v_mfma_f32_16x16x32_bf16 v[40:43], v[60:63], v[128:131], 0\n"
    "ds_read_b64_tr_b16 v[50:51], v9 offset:512\n"
    "v_mfma_f32_16x16x32_bf16 v[44:47], v[60:63], v[132:135], 0\n"
    "ds_read_b64_tr_b16 v[52:53], v9 offset:8192\n"
    "v_rcp_f32 v89, v89\n"
    "v_mfma_f32_16x16x32_bf16 v[36:39], v[56:59], v[132:135], 0\n"
    "s_waitcnt lgkmcnt(3)\n"
    "ds_read_b64_tr_b16 v[54:55], v9 offset:8704\n"
    "v_rcp_f32 v90, v90\n"
    "v_mfma_f32_16x16x32_bf16 v[32:35], v[64:67], v[136:139], v[32:35]\n"
    "ds_read_b64_tr_b16 v[56:57], v9 offset:4096\n"
    "v_rcp_f32 v91, v91\n"
    "v_mfma_f32_16x16x32_bf16 v[40:43], v[68:71], v[136:139], v[40:43]\n"
    "ds_read_b64_tr_b16 v[58:59], v9 offset:4608\n"
    "v_mfma_f32_16x16x32_bf16 v[44:47], v[68:71], v[140:143], v[44:47]\n"
    "s_setprio 0\n"
    "ds_read_b64_tr_b16 v[60:61], v9 offset:12288\n"
    "v_mul_f32 v104, v104, v88\n"
    "v_mfma_f32_16x16x32_bf16 v[36:39], v[64:67], v[140:143], v[36:39]\n"
    "ds_read_b64_tr_b16 v[62:63], v9 offset:12800\n"
    "v_mul_f32 v105, v105, v89\n"
    "v_mul_f32 v106, v106, v90\n"
    "s_barrier\n"
    "s_setprio 2\n"
    "s_waitcnt lgkmcnt(4)\n"
    "v_mfma_f32_16x16x32_bf16 v[32:35], v[48:51], v[144:147], v[32:35]\n"
    "ds_read_b64_tr_b16 v[64:65], v8 offset:16384\n"
    "v_mul_f32 v107, v107, v91\n"
    "v_mfma_f32_16x16x32_bf16 v[40:43], v[52:55], v[144:147], v[40:43]\n"
    "ds_read_b64_tr_b16 v[66:67], v8 offset:16896\n"
    "v_cvt_pk_bf16_f32 v122, v104, v105\n"
    "v_cvt_pk_bf16_f32 v123, v106, v107\n"
    "v_mfma_f32_16x16x32_bf16 v[44:47], v[52:55], v[148:151], v[44:47]\n"
    "ds_read_b64_tr_b16 v[68:69], v8 offset:24576\n"
    "v_mul_f32 v92, v100, v113\n"
    "v_mfma_f32_16x16x32_bf16 v[36:39], v[48:51], v[148:151], v[36:39]\n"
    "s_waitcnt lgkmcnt(3)\n"
    "ds_read_b64_tr_b16 v[70:71], v8 offset:25088\n"
    "v_mul_f32 v93, v101, v113\n"
    "v_mul_f32 v94, v102, v113\n"
    "v_mfma_f32_16x16x32_bf16 v[32:35], v[56:59], v[152:155], v[32:35]\n"
    "ds_read_b64_tr_b16 v[48:49], v8 offset:20480\n"
    "v_mul_f32 v95, v103, v113\n"
    "v_mfma_f32_16x16x32_bf16 v[40:43], v[60:63], v[152:155], v[40:43]\n"
    "ds_read_b64_tr_b16 v[50:51], v8 offset:20992\n"
    "v_exp_f32 v92, v92\n"
    "v_mfma_f32_16x16x32_bf16 v[44:47], v[60:63], v[156:159], v[44:47]\n"
    "ds_read_b64_tr_b16 v[52:53], v8 offset:28672\n"
    "v_mul_f32 v108, v100, v108\n"
    "v_mfma_f32_16x16x32_bf16 v[36:39], v[56:59], v[156:159], v[36:39]\n"
    "s_waitcnt lgkmcnt(3)\n"
    "ds_read_b64_tr_b16 v[54:55], v8 offset:29184\n"
    "v_exp_f32 v93, v93\n"
    "v_mfma_f32_16x16x32_bf16 v[16:19], v[64:67], v[160:163], v[16:19]\n"
    "Lst1_4:\n"
    "s_cbranch_execz Lr1_4\n"
    "s_setprio 1\n"
    "v_add_u32 v8, 0x8000, v8\n"
    "ds_read_b64_tr_b16 v[56:57], v8 offset:32768\n"
    "v_mul_f32 v109, v101, v109\n"
    "v_mfma_f32_16x16x32_bf16 v[24:27], v[68:71], v[160:163], v[24:27]\n"
    "ds_read_b64_tr_b16 v[58:59], v8 offset:33280\n"
    "v_exp_f32 v94, v94\n"
    "v_mfma_f32_16x16x32_bf16 v[28:31], v[68:71], v[164:167], v[28:31]\n"
    "ds_read_b64_tr_b16 v[60:61], v8 offset:40960\n"
    "v_mul_f32 v110, v102, v110\n"
    "v_mfma_f32_16x16x32_bf16 v[20:23], v[64:67], v[164:167], v[20:23]\n"
    "s_waitcnt lgkmcnt(3)\n"
    "ds_read_b64_tr_b16 v[62:63], v8 offset:41472\n"
    "v_exp_f32 v95, v95\n"
    "v_mfma_f32_16x16x32_bf16 v[16:19], v[48:51], v[168:171], v[16:19]\n"
    "ds_read_b64_tr_b16 v[64:65], v8 offset:36864\n"
    "v_mul_f32 v111, v103, v111\n"
    "v_mfma_f32_16x16x32_bf16 v[24:27], v[52:55], v[168:171], v[24:27]\n"
    "ds_read_b64_tr_b16 v[66:67], v8 offset:37376\n"
    "v_fma_f32 v92, v92, v115, v115\n"
    "v_mfma_f32_16x16x32_bf16 v[28:31], v[52:55], v[172:175], v[28:31]\n"
    "s_setprio 0\n"
    "ds_read_b64_tr_b16 v[68:69], v8 offset:45056\n"
    "v_fma_f32 v93, v93, v115, v115\n"
    "v_fma_f32 v94, v94, v115, v115\n"
    "v_mfma_f32_16x16x32_bf16 v[20:23], v[48:51], v[172:175], v[20:23]\n"
    "ds_read_b64_tr_b16 v[70:71], v8 offset:45568\n"
    "v_fma_f32 v95, v95, v115, v115\n"
    "s_barrier\n"
    "s_setprio 2\n"
    "s_waitcnt lgkmcnt(4)\n"
    "v_mfma_f32_16x16x32_bf16 v[16:19], v[56:59], v[176:179], v[16:19]\n"
    "ds_read_b64_tr_b16 v[48:49], v8 offset:49152\n"
    "v_rcp_f32 v92, v92\n"
    "v_mfma_f32_16x16x32_bf16 v[24:27], v[60:63], v[176:179], v[24:27]\n"
    "ds_read_b64_tr_b16 v[50:51], v8 offset:49664\n"
    "v_rcp_f32 v93, v93\n"
    "v_mfma_f32_16x16x32_bf16 v[28:31], v[60:63], v[180:183], v[28:31]\n"
    "ds_read_b64_tr_b16 v[52:53], v8 offset:57344\n"
    "v_mfma_f32_16x16x32_bf16 v[20:23], v[56:59], v[180:183], v[20:23]\n"
    "s_waitcnt lgkmcnt(3)\n"
    "ds_read_b64_tr_b16 v[54:55], v8 offset:57856\n"
    "v_rcp_f32 v94, v94\n"
    "v_mfma_f32_16x16x32_bf16 v[16:19], v[64:67], v[184:187], v[16:19]\n"
    "ds_read_b64_tr_b16 v[56:57], v8 offset:53248\n"
    "buffer_load_dwordx2 v[250:251], v248, s[56:59], 0 offen sc1\n"
    "v_rcp_f32 v95, v95\n"
    "v_mfma_f32_16x16x32_bf16 v[24:27], v[68:71], v[184:187], v[24:27]\n"
    "ds_read_b64_tr_b16 v[58:59], v8 offset:53760\n"
    "v_mul_f32 v108, v108, v92\n"
    "v_mfma_f32_16x16x32_bf16 v[28:31], v[68:71], v[188:191], v[28:31]\n"
    "ds_read_b64_tr_b16 v[60:61], v8 offset:61440\n"
    "v_mul_f32 v109, v109, v93\n"
    "v_mfma_f32_16x16x32_bf16 v[20:23], v[64:67], v[188:191], v[20:23]\n"
    "s_waitcnt lgkmcnt(3)\n"
    "ds_read_b64_tr_b16 v[62:63], v8 offset:61952\n"
    "v_mul_f32 v110, v110, v94\n"
    "v_mul_f32 v111, v111, v95\n"
    "v_mfma_f32_16x16x32_bf16 v[16:19], v[48:51], v[192:195], v[16:19]\n"
    "s_setprio 1\n"
    "ds_read_b64_tr_b16 v[64:65], v8 offset:0\n"
    "v_cvt_pk_bf16_f32 v120, v108, v109\n"
    "v_mfma_f32_16x16x32_bf16 v[24:27], v[52:55], v[192:195], v[24:27]\n"
    "ds_read_b64_tr_b16 v[66:67], v8 offset:512\n"
    "v_cvt_pk_bf16_f32 v121, v110, v111\n"
    "v_mfma_f32_16x16x32_bf16 v[28:31], v[52:55], v[196:199], v[28:31]\n"
    "ds_read_b64_tr_b16 v[68:69], v8 offset:8192\n"
    "v_mfma_f32_16x16x32_bf16 v[20:23], v[48:51], v[196:199], v[20:23]\n"
    "s_waitcnt lgkmcnt(3)\n"
    "ds_read_b64_tr_b16 v[70:71], v8 offset:8704\n"
    "ds_write_b64 v79, v[120:121] offset:49664\n"
    "v_mfma_f32_16x16x32_bf16 v[16:19], v[56:59], v[200:203], v[16:19]\n"
    "ds_read_b64_tr_b16 v[48:49], v8 offset:4096\n"
    "v_mfma_f32_16x16x32_bf16 v[24:27], v[60:63], v[200:203], v[24:27]\n"
    "ds_read_b64_tr_b16 v[50:51], v8 offset:4608\n"
    "v_mfma_f32_16x16x32_bf16 v[28:31], v[60:63], v[204:207], v[28:31]\n"
    "s_setprio 0\n"
    "ds_read_b64_tr_b16 v[52:53], v8 offset:12288\n"
    "v_mfma_f32_16x16x32_bf16 v[20:23], v[56:59], v[204:207], v[20:23]\n"
    "s_waitcnt lgkmcnt(3)\n"
    "ds_read_b64_tr_b16 v[54:55], v8 offset:12800\n"
    "s_barrier\n"
    "s_setprio 2\n"
    "v_mfma_f32_16x16x32_bf16 v[16:19], v[64:67], v[208:211], v[16:19]\n"
    "ds_read_b64_tr_b16 v[56:57], v8 offset:16384\n"
    "v_mfma_f32_16x16x32_bf16 v[24:27], v[68:71], v[208:211], v[24:27]\n"
    "ds_read_b64_tr_b16 v[58:59], v8 offset:16896\n"
    "ds_read_b64 v[120:121], v79 offset:49152\n"
    "v_mfma_f32_16x16x32_bf16 v[28:31], v[68:71], v[212:215], v[28:31]\n"
    "ds_read_b64_tr_b16 v[60:61], v8 offset:24576\n"
    "v_mfma_f32_16x16x32_bf16 v[20:23], v[64:67], v[212:215], v[20:23]\n"
    "s_waitcnt lgkmcnt(4)\n"
    "ds_read_b64_tr_b16 v[62:63], v8 offset:25088\n"
    "v_mfma_f32_16x16x32_bf16 v[16:19], v[48:51], v[216:219], v[16:19]\n"
    "ds_read_b64_tr_b16 v[64:65], v8 offset:20480\n"
    "v_mfma_f32_16x16x32_bf16 v[24:27], v[52:55], v[216:219], v[24:27]\n"
    "ds_read_b64_tr_b16 v[66:67], v8 offset:20992\n"
    "v_mfma_f32_16x16x32_bf16 v[28:31], v[52:55], v[220:223], v[28:31]\n"
    "ds_read_b64_tr_b16 v[68:69], v8 offset:28672\n"
    "v_mfma_f32_16x16x32_bf16 v[20:23], v[48:51], v[220:223], v[20:23]\n"
    "s_waitcnt lgkmcnt(3)\n"
    "ds_read_b64_tr_b16 v[70:71], v8 offset:29184\n"
    "v_mfma_f32_16x16x32_bf16 v[16:19], v[56:59], v[224:227], v[16:19]\n"
    "ds_read_b64_tr_b16 v[48:49], v9 offset:16384\n"
    "v_mfma_f32_16x16x32_bf16 v[24:27], v[60:63], v[224:227], v[24:27]\n"
    "ds_read_b64_tr_b16 v[50:51], v9 offset:16896\n"
    "v_mfma_f32_16x16x32_bf16 v[28:31], v[60:63], v[228:231], v[28:31]\n"
    "ds_read_b64_tr_b16 v[52:53], v9 offset:24576\n"
    "v_mfma_f32_16x16x32_bf16 v[20:23], v[56:59], v[228:231], v[20:23]\n"
    "s_waitcnt lgkmcnt(3)\n"
    "ds_read_b64_tr_b16 v[54:55], v9 offset:25088\n"
    "v_mfma_f32_16x16x32_bf16 v[16:19], v[64:67], v[232:235], v[16:19]\n"
    "ds_read_b64_tr_b16 v[56:57], v9 offset:20480\n"
    "v_mfma_f32_16x16x32_bf16 v[24:27], v[68:71], v[232:235], v[24:27]\n"
    "s_setprio 1\n"
    "ds_read_b64_tr_b16 v[58:59], v9 offset:20992\n"
    "v_mfma_f32_16x16x32_bf16 v[28:31], v[68:71], v[236:239], v[28:31]\n"
    "ds_read_b64_tr_b16 v[60:61], v9 offset:28672\n"
    "v_mfma_f32_16x16x32_bf16 v[20:23], v[64:67], v[236:239], v[20:23]\n"
    "s_waitcnt lgkmcnt(3)\n"
    "ds_read_b64_tr_b16 v[62:63], v9 offset:29184\n"
    "v_mfma_f32_16x16x32_bf16 v[32:35], v[48:51], v[160:163], v[32:35]\n"
    "v_add_u32 v9, 0x8000, v9\n"
    "ds_read_b64_tr_b16 v[64:65], v9 offset:32768\n"
    "v_mfma_f32_16x16x32_bf16 v[40:43], v[52:55], v[160:163], v[40:43]\n"
    "ds_read_b64_tr_b16 v[66:67], v9 offset:33280\n"
    "v_mfma_f32_16x16x32_bf16 v[44:47], v[52:55], v[164:167], v[44:47]\n"
    "ds_read_b64_tr_b16 v[68:69], v9 offset:40960\n"
    "v_mfma_f32_16x16x32_bf16 v[36:39], v[48:51], v[164:167], v[36:39]\n"
    "s_waitcnt lgkmcnt(3)\n"
    "ds_read_b64_tr_b16 v[70:71], v9 offset:41472\n"
    "v_mul_f32 v88, v16, v112\n"
    "v_mul_f32 v89, v17, v112\n"
    "v_mfma_f32_16x16x32_bf16 v[32:35], v[56:59], v[168:171], v[32:35]\n"
    "ds_read_b64_tr_b16 v[48:49], v9 offset:36864\n"
    "v_mul_f32 v90, v18, v112\n"
    "v_mul_f32 v91, v19, v112\n"
    "v_mfma_f32_16x16x32_bf16 v[40:43], v[60:63], v[168:171], v[40:43]\n"
    "ds_read_b64_tr_b16 v[50:51], v9 offset:37376\n"
    "v_exp_f32 v88, v88\n"
    "v_mfma_f32_16x16x32_bf16 v[44:47], v[60:63], v[172:175], v[44:47]\n"
    "s_setprio 0\n"
    "ds_read_b64_tr_b16 v[52:53], v9 offset:45056\n"
    "v_mul_f32 v24, v16, v24\n"
    "v_exp_f32 v89, v89\n"
    "v_mfma_f32_16x16x32_bf16 v[36:39], v[56:59], v[172:175], v[36:39]\n"
    "ds_read_b64_tr_b16 v[54:55], v9 offset:45568\n"
    "v_mul_f32 v25, v17, v25\n"
    "s_waitcnt vmcnt(0)\n"
    "v_cmp_ne_u64 vcc, s[60:61], v[250:251]\n"
    "s_nop 0\n"
    "s_cbranch_vccz Lok_21\n"
    "s_mov_b32 s10, 0x8000\n"
    "Lspin_22:\n"
    "buffer_load_dwordx2 v[250:251], v248, s[56:59], 0 offen sc1\n"
    "s_sub_u32 s10, s10, 1\n"
    "s_waitcnt vmcnt(0)\n"
    "s_cbranch_scc1 Lok_21\n"
    "v_cmp_ne_u64 vcc, s[60:61], v[250:251]\n"
    "s_nop 0\n"
    "s_cbranch_vccnz Lspin_22\n"
    "Lok_21:\n"
    "s_barrier\n"
    "s_setprio 2\n"
    "s_waitcnt lgkmcnt(4)\n"
    "v_mfma_f32_16x16x32_bf16 v[32:35], v[64:67], v[176:179], v[32:35]\n"
    "ds_read_b64_tr_b16 v[56:57], v9 offset:49152\n"
    "buffer_load_dwordx4 v[104:107], v1, s[56:59], s92 offen offset:0 sc1\n"
    "v_exp_f32 v90, v90\n"
    "v_mfma_f32_16x16x32_bf16 v[40:43], v[68:71], v[176:179], v[40:43]\n"
    "ds_read_b64_tr_b16 v[58:59], v9 offset:49664\n"
    "v_mul_f32 v26, v18, v26\n"
    "v_mfma_f32_16x16x32_bf16 v[44:47], v[68:71], v[180:183], v[44:47]\n"
    "ds_read_b64_tr_b16 v[60:61], v9 offset:57344\n"
    "v_exp_f32 v91, v91\n"
    "v_mfma_f32_16x16x32_bf16 v[36:39], v[64:67], v[180:183], v[36:39]\n"
    "s_waitcnt lgkmcnt(3)\n"
    "ds_read_b64_tr_b16 v[62:63], v9 offset:57856\n"
    "v_mul_f32 v27, v19, v27\n"
    "v_fma_f32 v88, v88, v114, v114\n"
    "v_mfma_f32_16x16x32_bf16 v[32:35], v[48:51], v[184:187], v[32:35]\n"
    "ds_read_b64_tr_b16 v[64:65], v9 offset:53248\n"
    "v_fma_f32 v89, v89, v114, v114\n"
    "v_fma_f32 v90, v90, v114, v114\n"
    "v_mfma_f32_16x16x32_bf16 v[40:43], v[52:55], v[184:187], v[40:43]\n"
    "ds_read_b64_tr_b16 v[66:67], v9 offset:53760\n"
    "v_fma_f32 v91, v91, v114, v114\n"
    "v_rcp_f32 v88, v88\n"
    "v_mfma_f32_16x16x32_bf16 v[44:47], v[52:55], v[188:191], v[44:47]\n"
    "ds_read_b64_tr_b16 v[68:69], v9 offset:61440\n"
    "v_rcp_f32 v89, v89\n"
    "v_mfma_f32_16x16x32_bf16 v[36:39], v[48:51], v[188:191], v[36:39]\n"
    "s_waitcnt lgkmcnt(3)\n"
    "ds_read_b64_tr_b16 v[70:71], v9 offset:61952\n"
    "v_mfma_f32_16x16x32_bf16 v[32:35], v[56:59], v[192:195], v[32:35]\n"
    "ds_read_b64_tr_b16 v[48:49], v9 offset:0\n"
    "buffer_load_dwordx4 v[108:111], v1, s[56:59], s92 offen offset:1024 sc1\n"
    "v_rcp_f32 v90, v90\n"
    "v_mfma_f32_16x16x32_bf16 v[40:43], v[60:63], v[192:195], v[40:43]\n"
    "ds_read_b64_tr_b16 v[50:51], v9 offset:512\n"
    "v_rcp_f32 v91, v91\n"
    "v_mfma_f32_16x16x32_bf16 v[44:47], v[60:63], v[196:199], v[44:47]\n"
    "ds_read_b64_tr_b16 v[52:53], v9 offset:8192\n"
    "v_mul_f32 v24, v24, v88\n"
    "v_mul_f32 v25, v25, v89\n"
    "v_mfma_f32_16x16x32_bf16 v[36:39], v[56:59], v[196:199], v[36:39]\n"
    "s_waitcnt lgkmcnt(3)\n"
    "ds_read_b64_tr_b16 v[54:55], v9 offset:8704\n"
    "v_mul_f32 v26, v26, v90\n"
    "v_mul_f32 v27, v27, v91\n"
    "v_mfma_f32_16x16x32_bf16 v[32:35], v[64:67], v[200:203], v[32:35]\n"
    "ds_read_b64_tr_b16 v[56:57], v9 offset:4096\n"
    "v_cvt_pk_bf16_f32 v240, v24, v25\n"
    "v_cvt_pk_bf16_f32 v241, v26, v27\n"
    "v_mfma_f32_16x16x32_bf16 v[40:43], v[68:71], v[200:203], v[40:43]\n"
    "ds_read_b64_tr_b16 v[58:59], v9 offset:4608\n"
    "v_mul_f32 v92, v20, v113\n"
    "v_mfma_f32_16x16x32_bf16 v[44:47], v[68:71], v[204:207], v[44:47]\n"
    "ds_read_b64_tr_b16 v[60:61], v9 offset:12288\n"
    "v_mul_f32 v93, v21, v113\n"
    "v_mul_f32 v94, v22, v113\n"
    "v_mfma_f32_16x16x32_bf16 v[36:39], v[64:67], v[204:207], v[36:39]\n"
    "s_waitcnt lgkmcnt(3)\n"
    "ds_read_b64_tr_b16 v[62:63], v9 offset:12800\n"
    "v_mul_f32 v95, v23, v113\n"
    "v_exp_f32 v92, v92\n"
    "v_mfma_f32_16x16x32_bf16 v[32:35], v[48:51], v[208:211], v[32:35]\n"
    "ds_read_b64_tr_b16 v[64:65], v9 offset:16384\n"
    "buffer_store_dwordx4 v[120:123], v1, s[56:59], s82 offen sc1\n"
    "v_mul_f32 v28, v20, v28\n"
    "v_mfma_f32_16x16x32_bf16 v[40:43], v[52:55], v[208:211], v[40:43]\n"
    "ds_read_b64_tr_b16 v[66:67], v9 offset:16896\n"
    "v_exp_f32 v93, v93\n"
    "v_mfma_f32_16x16x32_bf16 v[44:47], v[52:55], v[212:215], v[44:47]\n"
    "ds_read_b64_tr_b16 v[68:69], v9 offset:24576\n"
    "v_mul_f32 v29, v21, v29\n"
    "v_exp_f32 v94, v94\n"
    "v_mfma_f32_16x16x32_bf16 v[36:39], v[48:51], v[212:215], v[36:39]\n"
    "s_waitcnt lgkmcnt(3)\n"
    "ds_read_b64_tr_b16 v[70:71], v9 offset:25088\n"
    "v_mfma_f32_16x16x32_bf16 v[32:35], v[56:59], v[216:219], v[32:35]\n"
    "ds_read_b64_tr_b16 v[48:49], v9 offset:20480\n"
    "v_mul_f32 v30, v22, v30\n"
    "v_exp_f32 v95, v95\n"
    "v_mfma_f32_16x16x32_bf16 v[40:43], v[60:63], v[216:219], v[40:43]\n"
    "ds_read_b64_tr_b16 v[50:51], v9 offset:20992\n"
    "v_mul_f32 v31, v23, v31\n"
    "v_mfma_f32_16x16x32_bf16 v[44:47], v[60:63], v[220:223], v[44:47]\n"
    "ds_read_b64_tr_b16 v[52:53], v9 offset:28672\n"
    "v_fma_f32 v92, v92, v115, v115\n"
    "v_fma_f32 v93, v93, v115, v115\n"
    "v_mfma_f32_16x16x32_bf16 v[36:39], v[56:59], v[220:223], v[36:39]\n"
    "s_waitcnt lgkmcnt(3)\n"
    "ds_read_b64_tr_b16 v[54:55], v9 offset:29184\n"
    "v_fma_f32 v94, v94, v115, v115\n"
    "v_fma_f32 v95, v95, v115, v115\n"
    "v_mfma_f32_16x16x32_bf16 v[32:35], v[64:67], v[224:227], v[32:35]\n"
    "v_rcp_f32 v92, v92\n"
    "v_mfma_f32_16x16x32_bf16 v[40:43], v[68:71], v[224:227], v[40:43]\n"
    "s_setprio 1\n"
    "v_rcp_f32 v93, v93\n"
    "v_mfma_f32_16x16x32_bf16 v[44:47], v[68:71], v[228:231], v[44:47]\n"
    "v_rcp_f32 v94, v94\n"
    "v_mfma_f32_16x16x32_bf16 v[36:39], v[64:67], v[228:231], v[36:39]\n"
    "s_waitcnt lgkmcnt(0)\n"
    "v_rcp_f32 v95, v95\n"
    "v_mfma_f32_16x16x32_bf16 v[32:35], v[48:51], v[232:235], v[32:35]\n"
    "v_mul_f32 v28, v28, v92\n"
    "v_mfma_f32_16x16x32_bf16 v[40:43], v[52:55], v[232:235], v[40:43]\n"
    "v_mul_f32 v29, v29, v93\n"
    "v_mul_f32 v30, v30, v94\n"
    "v_mfma_f32_16x16x32_bf16 v[44:47], v[52:55], v[236:239], v[44:47]\n"
    "v_mul_f32 v31, v31, v95\n"
    "v_cvt_pk_bf16_f32 v244, v28, v29\n"
    "v_mfma_f32_16x16x32_bf16 v[36:39], v[48:51], v[236:239], v[36:39]\n"
    "v_cvt_pk_bf16_f32 v245, v30, v31\n"
    "Lattn_h1:\n"
    "Lst1_5:\n"
    "s_cbranch_execz Lr1_5\n"
    "s_setprio 0\n"
    "s_nop 0\n"
    "v_mul_f32 v88, v32, v112\n"
    "v_mul_f32 v89, v33, v112\n"
    "v_mul_f32 v90, v34, v112\n"
    "v_mul_f32 v91, v35, v112\n"
    "v_exp_f32 v88, v88\n"
    "v_mul_f32 v40, v32, v40\n"
    "v_exp_f32 v89, v89\n"
    "v_mul_f32 v41, v33, v41\n"
    "v_exp_f32 v90, v90\n"
    "v_mul_f32 v42, v34, v42\n"
    "v_exp_f32 v91, v91\n"
    "v_mul_f32 v43, v35, v43\n"
    "v_fma_f32 v88, v88, v114, v114\n"
    "v_fma_f32 v89, v89, v114, v114\n"
    "v_fma_f32 v90, v90, v114, v114\n"
    "v_fma_f32 v91, v91, v114, v114\n"
    "v_rcp_f32 v88, v88\n"
    "v_rcp_f32 v89, v89\n"
    "v_rcp_f32 v90, v90\n"
    "v_rcp_f32 v91, v91\n"
    "v_mul_f32 v40, v40, v88\n"
    "v_mul_f32 v41, v41, v89\n"
    "v_mul_f32 v42, v42, v90\n"
    "v_mul_f32 v43, v43, v91\n"
    "v_cvt_pk_bf16_f32 v242, v40, v41\n"
    "v_cvt_pk_bf16_f32 v243, v42, v43\n"
    "buffer_store_dwordx4 v[240:243], v1, s[56:59], s83 offen sc1\n"
    "v_mul_f32 v92, v36, v113\n"
    "v_mul_f32 v93, v37, v113\n"
    "v_mul_f32 v94, v38, v113\n"
    "v_mul_f32 v95, v39, v113\n"
    "v_exp_f32 v92, v92\n"
    "v_mul_f32 v44, v36, v44\n"
    "v_exp_f32 v93, v93\n"
    "v_mul_f32 v45, v37, v45\n"
    "v_exp_f32 v94, v94\n"
    "v_mul_f32 v46, v38, v46\n"
    "v_exp_f32 v95, v95\n"
    "v_mul_f32 v47, v39, v47\n"
    "v_fma_f32 v92, v92, v115, v115\n"
    "v_fma_f32 v93, v93, v115, v115\n"
    "v_fma_f32 v94, v94, v115, v115\n"
    "v_fma_f32 v95, v95, v115, v115\n"
    "v_rcp_f32 v92, v92\n"
    "v_rcp_f32 v93, v93\n"
    "v_rcp_f32 v94, v94\n"
    "v_rcp_f32 v95, v95\n"
    "v_mul_f32 v44, v44, v92\n"
    "v_mul_f32 v45, v45, v93\n"
    "v_mul_f32 v46, v46, v94\n"
    "s_barrier\n"
    "s_mov_b64 s[84:85], s[56:57]\n"
    "s_mov_b32 s87, 0x20000\n"
    "s_cmp_ge_u32 s27, 28\n"
    "s_cselect_b32 s50, 0, s50\n"
    "s_cselect_b32 s86, 0, s58\n"
    "s_setprio 3\n"
    "ds_read_b128 v[64:67], v1 offset:49152\n"
    "ds_read_b128 v[68:71], v1 offset:50176\n"
    "ds_read_b128 v[80:83], v73 offset:0\n"
    "ds_read_b128 v[84:87], v73 offset:1024\n"
    "ds_read_b128 v[96:99], v73 offset:2048\n"
    "ds_read_b128 v[120:123], v73 offset:3072\n"
    "ds_read_b128 v[100:103], v73 offset:6144\n"
    "ds_read_b128 v[8:11], v73 offset:7168\n"
    "v_mul_f32 v47, v47, v95\n"
    "v_cvt_pk_bf16_f32 v246, v44, v45\n"
    "v_cvt_pk_bf16_f32 v247, v46, v47\n"
    "s_add_u32 s83, s83, s76\n"
    "buffer_store_dwordx4 v[244:247], v1, s[56:59], s83 offen sc1\n"
    "s_waitcnt vmcnt(3) lgkmcnt(7)\n"
    "v_mfma_f32_32x32x16_bf16 v[16:31], v[64:67], v[104:107], 0\n"
    "s_waitcnt vmcnt(2) lgkmcnt(6)\n"
    "v_mfma_f32_32x32x16_bf16 v[16:31], v[68:71], v[108:111], v[16:31]\n"
    "s_waitcnt lgkmcnt(5)\n"
    "v_mfma_f32_32x32x16_bf16 v[48:63], v[80:83], v[104:107], 0\n"
    "s_waitcnt lgkmcnt(4)\n"
    "ds_read_b128 v[80:83], v73 offset:4096\n"
    "s_nop 7\n"
    "v_max3_f32 v116, v16, v17, v18\n"
    "v_max3_f32 v117, v19, v20, v21\n"
    "v_max3_f32 v116, v116, v22, v23\n"
    "v_max3_f32 v117, v117, v24, v25\n"
    "v_max3_f32 v116, v116, v26, v27\n"
    "v_max3_f32 v117, v117, v28, v29\n"
    "v_max3_f32 v116, v116, v30, v31\n"
    "v_cmp_lt_u32 vcc, 31, v0\n"
    "v_max_f32 v116, v116, v117\n"
    "v_mov_b32 v117, v116\n"
    "s_nop 1\n"
    "v_permlane32_swap_b32 v116, v117\n"
    "v_max_f32 v116, v116, v117\n"
    "v_sub_f32 v116, 0xc2cc0000, v116\n"
    "v_and_b32 v117, 0xffff0000, v116\n"
    "v_sub_f32 v116, v116, v117\n"
    "v_cvt_pk_bf16_f32 v116, v116, v117\n"
    "v_cndmask_b32 v110, v110, v116, vcc\n"
    "v_mov_b32 v72, 0xdddddddd\n"
    "v_mov_b32 v64, 0\n"
    "v_mov_b32 v65, 0\n"
    "v_mov_b32 v66, 0\n"
    "v_mov_b32 v67, 0\n"
    "v_add_u32 v3, 6144, v2\n"
    "v_mfma_f32_32x32x16_bf16 v[48:63], v[84:87], v[108:111], v[48:63]\n"
    "s_setprio 2\n"
    "ds_read_b128 v[84:87], v73 offset:5120\n"
    "v_mfma_f32_32x32x16_bf16 v[16:31], v[64:67], v[64:67], 0\n"
    "s_setprio 0\n"
    "s_nop 7\n"
    "v_exp_f32 v48, v48\n"
    "v_exp_f32 v49, v49\n"
    "s_waitcnt lgkmcnt(0)\n"
    "s_barrier\n"
    "s_setprio 3\n"
    "ds_read_b128 v[88:91], v73 offset:19456\n"
    "v_exp_f32 v50, v50\n"
    "v_exp_f32 v51, v51\n"
    "v_exp_f32 v52, v52\n"
    "v_mfma_f32_32x32x16_bf16 v[32:47], v[80:83], v[104:107], 0\n"
    "ds_read_b128 v[80:83], v73 offset:16384\n"
    "v_exp_f32 v53, v53\n"
    "v_exp_f32 v54, v54\n"
    "v_exp_f32 v55, v55\n"
    "v_exp_f32 v56, v56\n"
    "v_mfma_f32_32x32x16_bf16 v[32:47], v[84:87], v[108:111], v[32:47]\n"
    "ds_read_b128 v[84:87], v73 offset:17408\n"
    "v_exp_f32 v57, v57\n"
    "v_exp_f32 v58, v58\n"
    "v_exp_f32 v59, v59\n"
    "v_smfmac_f32_32x32x32_bf16 v[16:31], v[96:99], v[48:55], v72\n"
    "ds_read_b128 v[96:99], v73 offset:18432\n"
    "v_exp_f32 v60, v60\n"
    "v_exp_f32 v61, v61\n"
    "v_exp_f32 v62, v62\n"
    "v_exp_f32 v63, v63\n"
    "s_nop 1\n"
    "v_exp_f32 v32, v32\n"
    "v_exp_f32 v33, v33\n"
    "v_smfmac_f32_32x32x32_bf16 v[16:31], v[120:123], v[56:63], v72\n"
    "s_waitcnt lgkmcnt(1)\n"
    "ds_read_b128 v[120:123], v73 offset:23552\n"
    "v_exp_f32 v34, v34\n"
    "v_exp_f32 v35, v35\n"
    "v_exp_f32 v36, v36\n"
    "v_mfma_f32_32x32x16_bf16 v[48:63], v[80:83], v[104:107], 0\n"
    "ds_read_b128 v[80:83], v73 offset:20480\n"
    "v_exp_f32 v37, v37\n"
    "v_exp_f32 v38, v38\n"
    "v_exp_f32 v39, v39\n"
    "v_exp_f32 v40, v40\n"
    "v_mfma_f32_32x32x16_bf16 v[48:63], v[84:87], v[108:111], v[48:63]\n"
    "ds_read_b128 v[84:87], v73 offset:21504\n"
    "v_exp_f32 v41, v41\n"
    "v_exp_f32 v42, v42\n"
    "v_exp_f32 v43, v43\n"
    "v_smfmac_f32_32x32x32_bf16 v[16:31], v[100:103], v[32:39], v72\n"
    "ds_read_b128 v[100:103], v73 offset:22528\n"
    "v_exp_f32 v44, v44\n"
    "v_exp_f32 v45, v45\n"
    "v_exp_f32 v46, v46\n"
    "v_exp_f32 v47, v47\n"
    "s_nop 1\n"
    "v_exp_f32 v48, v48\n"
    "v_exp_f32 v49, v49\n"
    "v_smfmac_f32_32x32x32_bf16 v[16:31], v[8:11], v[40:47], v72\n"
    "s_waitcnt lgkmcnt(1)\n"
    "ds_read_b128 v[8:11], v73 offset:35840\n"
    "v_exp_f32 v50, v50\n"
    "v_exp_f32 v51, v51\n"
    "v_exp_f32 v52, v52\n"
    "v_mfma_f32_32x32x16_bf16 v[32:47], v[80:83], v[104:107], 0\n"
    "ds_read_b128 v[80:83], v73 offset:32768\n"
    "v_exp_f32 v53, v53\n"
    "v_exp_f32 v54, v54\n"
    "v_exp_f32 v55, v55\n"
    "v_exp_f32 v56, v56\n"
    "v_mfma_f32_32x32x16_bf16 v[32:47], v[84:87], v[108:111], v[32:47]\n"
    "s_setprio 2\n"
    "ds_read_b128 v[84:87], v73 offset:33792\n"
    "v_exp_f32 v57, v57\n"
    "v_exp_f32 v58, v58\n"
    "v_exp_f32 v59, v59\n"
    "v_smfmac_f32_32x32x32_bf16 v[16:31], v[96:99], v[48:55], v72\n"
    "ds_read_b128 v[96:99], v73 offset:34816\n"
    "v_exp_f32 v60, v60\n"
    "v_exp_f32 v61, v61\n"
    "v_exp_f32 v62, v62\n"
    "v_exp_f32 v63, v63\n"
    "s_nop 0\n"
    "v_exp_f32 v32, v32\n"
    "v_exp_f32 v33, v33\n"
    "v_smfmac_f32_32x32x32_bf16 v[16:31], v[88:91], v[56:63], v72\n"
    "s_waitcnt lgkmcnt(1)\n"
    "ds_read_b128 v[88:91], v73 offset:39936\n"
    "v_exp_f32 v34, v34\n"
    "v_exp_f32 v35, v35\n"
    "v_exp_f32 v36, v36\n"
    "v_mfma_f32_32x32x16_bf16 v[48:63], v[80:83], v[104:107], 0\n"
    "ds_read_b128 v[80:83], v73 offset:36864\n"
    "v_exp_f32 v37, v37\n"
    "v_exp_f32 v38, v38\n"
    "v_exp_f32 v39, v39\n"
    "v_exp_f32 v40, v40\n"
    "v_mfma_f32_32x32x16_bf16 v[48:63], v[84:87], v[108:111], v[48:63]\n"
    "ds_read_b128 v[84:87], v73 offset:37888\n"
    "v_exp_f32 v41, v41\n"
    "v_exp_f32 v42, v42\n"
    "v_exp_f32 v43, v43\n"
    "v_smfmac_f32_32x32x32_bf16 v[16:31], v[100:103], v[32:39], v72\n"
    "s_setprio 1\n"
    "s_setprio 0\n"
    "ds_read_b128 v[100:103], v73 offset:38912\n"
    "buffer_load_dwordx4 v[244:247], v6, s[28:31], s99 offen\n"
    "v_exp_f32 v44, v44\n"
    "v_exp_f32 v45, v45\n"
    "v_exp_f32 v46, v46\n"
    "v_exp_f32 v47, v47\n"
    "v_exp_f32 v48, v48\n"
    "v_exp_f32 v49, v49\n"
    "v_smfmac_f32_32x32x32_bf16 v[16:31], v[120:123], v[40:47], v72\n"
    "s_waitcnt vmcnt(1) lgkmcnt(0)\n"
    "s_barrier\n"
    "s_setprio 3\n"
    "ds_read_b128 v[120:123], v74 offset:3072\n"
    "v_exp_f32 v50, v50\n"
    "v_exp_f32 v51, v51\n"
    "v_exp_f32 v52, v52\n"
    "v_mfma_f32_32x32x16_bf16 v[32:47], v[80:83], v[104:107], 0\n"
    "ds_read_b128 v[80:83], v74 offset:0\n"
    "v_exp_f32 v53, v53\n"
    "v_exp_f32 v54, v54\n"
    "v_exp_f32 v55, v55\n"
    "v_exp_f32 v56, v56\n"
    "v_mfma_f32_32x32x16_bf16 v[32:47], v[84:87], v[108:111], v[32:47]\n"
    "ds_read_b128 v[84:87], v74 offset:1024\n"
    "v_exp_f32 v57, v57\n"
    "v_exp_f32 v58, v58\n"
    "v_exp_f32 v59, v59\n"
    "v_smfmac_f32_32x32x32_bf16 v[16:31], v[96:99], v[48:55], v72\n"
    "s_cmp_lg_u32 s25, 4\n"
    "ds_read_b128 v[96:99], v74 offset:2048\n"
    "s_cbranch_scc1 Lpub_23\n"
    "s_mov_b64 exec, 1\n"
    "buffer_store_dwordx2 v[252:253], off, s[56:59], s95 sc1\n"
    "s_mov_b64 exec, -1\n"
    "Lpub_23:\n"
    "s_add_u32 m0, s16, 0x2000\n"
    "s_add_u32 s18, s97, 0xc4000\n"
    "buffer_load_dwordx4 v2, s[48:51], s18 offen lds\n"
    "v_exp_f32 v60, v60\n"
    "v_exp_f32 v61, v61\n"
    "v_exp_f32 v62, v62\n"
    "v_exp_f32 v63, v63\n"
    "v_exp_f32 v32, v32\n"
    "v_exp_f32 v33, v33\n"
    "v_smfmac_f32_32x32x32_bf16 v[16:31], v[8:11], v[56:63], v72\n"
    "s_waitcnt lgkmcnt(1)\n"
    "ds_read_b128 v[8:11], v74 offset:7168\n"
    "v_exp_f32 v34, v34\n"
    "v_exp_f32 v35, v35\n"
    "v_exp_f32 v36, v36\n"
    "v_mfma_f32_32x32x16_bf16 v[48:63], v[80:83], v[104:107], 0\n"
    "ds_read_b128 v[80:83], v74 offset:4096\n"
    "v_exp_f32 v37, v37\n"
    "v_exp_f32 v38, v38\n"
    "v_exp_f32 v39, v39\n"
    "v_exp_f32 v40, v40\n"
    "v_mfma_f32_32x32x16_bf16 v[48:63], v[84:87], v[108:111], v[48:63]\n"
    "ds_read_b128 v[84:87], v74 offset:5120\n"
    "v_exp_f32 v41, v41\n"
    "v_exp_f32 v42, v42\n"
    "v_exp_f32 v43, v43\n"
    "v_smfmac_f32_32x32x32_bf16 v[16:31], v[100:103], v[32:39], v72\n"
    "ds_read_b128 v[100:103], v74 offset:6144\n"
    "buffer_load_dwordx4 v3, s[48:51], s18 offen offset:1024 lds\n"
    "v_exp_f32 v44, v44\n"
    "v_exp_f32 v45, v45\n"
    "v_exp_f32 v46, v46\n"
    "v_exp_f32 v47, v47\n"
    "s_nop 0\n"
    "v_exp_f32 v48, v48\n"
    "v_exp_f32 v49, v49\n"
    "v_smfmac_f32_32x32x32_bf16 v[16:31], v[88:91], v[40:47], v72\n"
    "s_waitcnt lgkmcnt(1)\n"
    "ds_read_b128 v[88:91], v74 offset:19456\n"
    "v_exp_f32 v50, v50\n"
    "v_exp_f32 v51, v51\n"
    "v_exp_f32 v52, v52\n"
    "v_mfma_f32_32x32x16_bf16 v[32:47], v[80:83], v[104:107], 0\n"
    "ds_read_b128 v[80:83], v74 offset:16384\n"
    "v_exp_f32 v53, v53\n"
    "v_exp_f32 v54, v54\n"
    "v_exp_f32 v55, v55\n"
    "v_exp_f32 v56, v56\n"
    "v_mfma_f32_32x32x16_bf16 v[32:47], v[84:87], v[108:111], v[32:47]\n"
    "ds_read_b128 v[84:87], v74 offset:17408\n"
    "v_exp_f32 v57, v57\n"
    "v_exp_f32 v58, v58\n"
    "v_exp_f32 v59, v59\n"
    "v_smfmac_f32_32x32x32_bf16 v[16:31], v[96:99], v[48:55], v72\n"
    "s_add_u32 m0, s16, 0x6000\n"
    "s_add_u32 s18, s97, 0xcb000\n"
    "ds_read_b128 v[96:99], v74 offset:18432\n"
    "buffer_load_dwordx4 v2, s[48:51], s18 offen lds\n"
    "v_exp_f32 v60, v60\n"
    "v_exp_f32 v61, v61\n"
    "v_exp_f32 v62, v62\n"
    "v_exp_f32 v63, v63\n"
    "v_exp_f32 v32, v32\n"
    "v_exp_f32 v33, v33\n"
    "v_smfmac_f32_32x32x32_bf16 v[16:31], v[120:123], v[56:63], v72\n"
    "s_waitcnt lgkmcnt(1)\n"
    "ds_read_b128 v[120:123], v74 offset:23552\n"
    "v_exp_f32 v34, v34\n"
    "v_exp_f32 v35, v35\n"
    "v_exp_f32 v36, v36\n"
    "v_mfma_f32_32x32x16_bf16 v[48:63], v[80:83], v[104:107], 0\n"
    "ds_read_b128 v[80:83], v74 offset:20480\n"
    "v_exp_f32 v37, v37\n"
    "v_exp_f32 v38, v38\n"
    "v_exp_f32 v39, v39\n"
    "v_exp_f32 v40, v40\n"
    "v_mfma_f32_32x32x16_bf16 v[48:63], v[84:87], v[108:111], v[48:63]\n"
    "ds_read_b128 v[84:87], v74 offset:21504\n"
    "v_exp_f32 v41, v41\n"
    "v_exp_f32 v42, v42\n"
    "v_exp_f32 v43, v43\n"
    "v_smfmac_f32_32x32x32_bf16 v[16:31], v[100:103], v[32:39], v72\n"
    "s_setprio 2\n"
    "ds_read_b128 v[100:103], v74 offset:22528\n"
    "buffer_load_dwordx4 v3, s[48:51], s18 offen offset:1024 lds\n"
    "v_exp_f32 v44, v44\n"
    "v_exp_f32 v45, v45\n"
    "v_exp_f32 v46, v46\n"
    "v_exp_f32 v47, v47\n"
    "v_exp_f32 v48, v48\n"
    "v_exp_f32 v49, v49\n"
    "v_smfmac_f32_32x32x32_bf16 v[16:31], v[8:11], v[40:47], v72\n"
    "s_waitcnt lgkmcnt(1)\n"
    "ds_read_b128 v[8:11], v74 offset:35840\n"
    "v_exp_f32 v50, v50\n"
    "v_exp_f32 v51, v51\n"
    "v_exp_f32 v52, v52\n"
    "v_mfma_f32_32x32x16_bf16 v[32:47], v[80:83], v[104:107], 0\n"
    "ds_read_b128 v[80:83], v74 offset:32768\n"
    "v_exp_f32 v53, v53\n"
    "v_exp_f32 v54, v54\n"
    "v_exp_f32 v55, v55\n"
    "v_exp_f32 v56, v56\n"
    "v_mfma_f32_32x32x16_bf16 v[32:47], v[84:87], v[108:111], v[32:47]\n"
    "ds_read_b128 v[84:87], v74 offset:33792\n"
    "v_exp_f32 v57, v57\n"
    "v_exp_f32 v58, v58\n"
    "v_exp_f32 v59, v59\n"
    "v_smfmac_f32_32x32x32_bf16 v[16:31], v[96:99], v[48:55], v72\n"
    "s_add_u32 m0, s16, 0xa000\n"
    "s_add_u32 s18, s97, 0xd2000\n"
    "ds_read_b128 v[96:99], v74 offset:34816\n"
    "buffer_load_dwordx4 v2, s[48:51], s18 offen lds\n"
    "v_exp_f32 v60, v60\n"
    "v_exp_f32 v61, v61\n"
    "v_exp_f32 v62, v62\n"
    "v_exp_f32 v63, v63\n"
    "v_exp_f32 v32, v32\n"
    "v_exp_f32 v33, v33\n"
    "v_smfmac_f32_32x32x32_bf16 v[16:31], v[88:91], v[56:63], v72\n"
    "s_waitcnt lgkmcnt(1)\n"
    "ds_read_b128 v[88:91], v74 offset:39936\n"
    "v_exp_f32 v34, v34\n"
    "v_exp_f32 v35, v35\n"
    "v_exp_f32 v36, v36\n"
    "v_mfma_f32_32x32x16_bf16 v[48:63], v[80:83], v[104:107], 0\n"
    "ds_read_b128 v[80:83], v74 offset:36864\n"
    "buffer_load_dwordx4 v3, s[48:51], s18 offen offset:1024 lds\n"
    "v_exp_f32 v37, v37\n"
    "v_exp_f32 v38, v38\n"
    "v_exp_f32 v39, v39\n"
    "v_exp_f32 v40, v40\n"
    "v_mfma_f32_32x32x16_bf16 v[48:63], v[84:87], v[108:111], v[48:63]\n"
    "s_setprio 1\n"
    "ds_read_b128 v[84:87], v74 offset:37888\n"
    "v_exp_f32 v41, v41\n"
    "v_exp_f32 v42, v42\n"
    "v_exp_f32 v43, v43\n"
    "v_smfmac_f32_32x32x32_bf16 v[16:31], v[100:103], v[32:39], v72\n"
    "s_setprio 0\n"
    "ds_read_b128 v[100:103], v74 offset:38912\n"
    "buffer_load_dwordx2 v[250:251], v75, s[56:59], 0 offen sc1\n"
    "v_exp_f32 v44, v44\n"
    "v_exp_f32 v45, v45\n"
    "v_exp_f32 v46, v46\n"
    "v_exp_f32 v47, v47\n"
    "v_exp_f32 v48, v48\n"
    "v_exp_f32 v49, v49\n"
    "v_smfmac_f32_32x32x32_bf16 v[16:31], v[120:123], v[40:47], v72\n"
    "s_waitcnt lgkmcnt(0)\n"
    "s_barrier\n"
    "s_setprio 3\n"
    "ds_read_b128 v[120:123], v73 offset:3072\n"
    "v_exp_f32 v50, v50\n"
    "v_exp_f32 v51, v51\n"
    "v_exp_f32 v52, v52\n"
    "v_mfma_f32_32x32x16_bf16 v[32:47], v[80:83], v[104:107], 0\n"
    "ds_read_b128 v[80:83], v73 offset:0\n"
    "v_exp_f32 v53, v53\n"
    "v_exp_f32 v54, v54\n"
    "v_exp_f32 v55, v55\n"
    "v_exp_f32 v56, v56\n"
    "v_mfma_f32_32x32x16_bf16 v[32:47], v[84:87], v[108:111], v[32:47]\n"
    "ds_read_b128 v[84:87], v73 offset:1024\n"
    "v_exp_f32 v57, v57\n"
    "v_exp_f32 v58, v58\n"
    "v_exp_f32 v59, v59\n"
    "v_smfmac_f32_32x32x32_bf16 v[16:31], v[96:99], v[48:55], v72\n"
    "s_add_u32 m0, s16, 0x26000\n"
    "s_add_u32 s18, s97, 0xd9000\n"
    "ds_read_b128 v[96:99], v73 offset:2048\n"
    "buffer_load_dwordx4 v2, s[48:51], s18 offen lds\n"
    "v_exp_f32 v60, v60\n"
    "v_exp_f32 v61, v61\n"
    "Lst1_6:\n"
    "s_cbranch_execz Lr1_6\n"
    "v_exp_f32 v62, v62\n"
    "v_exp_f32 v63, v63\n"
    "v_exp_f32 v32, v32\n"
    "v_exp_f32 v33, v33\n"
    "v_smfmac_f32_32x32x32_bf16 v[16:31], v[8:11], v[56:63], v72\n"
    "s_waitcnt lgkmcnt(1)\n"
    "ds_read_b128 v[8:11], v73 offset:7168\n"
    "v_exp_f32 v34, v34\n"
    "v_exp_f32 v35, v35\n"
    "v_exp_f32 v36, v36\n"
    "v_mfma_f32_32x32x16_bf16 v[48:63], v[80:83], v[104:107], 0\n"
    "ds_read_b128 v[80:83], v73 offset:4096\n"
    "v_exp_f32 v37, v37\n"
    "v_exp_f32 v38, v38\n"
    "v_exp_f32 v39, v39\n"
    "v_exp_f32 v40, v40\n"
    "v_mfma_f32_32x32x16_bf16 v[48:63], v[84:87], v[108:111], v[48:63]\n"
    "ds_read_b128 v[84:87], v73 offset:5120\n"
    "v_exp_f32 v41, v41\n"
    "v_exp_f32 v42, v42\n"
    "v_exp_f32 v43, v43\n"
    "v_smfmac_f32_32x32x32_bf16 v[16:31], v[100:103], v[32:39], v72\n"
    "ds_read_b128 v[100:103], v73 offset:6144\n"
    "buffer_load_dwordx4 v3, s[48:51], s18 offen offset:1024 lds\n"
    "v_exp_f32 v44, v44\n"
    "v_exp_f32 v45, v45\n"
    "v_exp_f32 v46, v46\n"
    "v_exp_f32 v47, v47\n"
    "s_nop 0\n"
    "v_exp_f32 v48, v48\n"
    "v_exp_f32 v49, v49\n"
    "v_smfmac_f32_32x32x32_bf16 v[16:31], v[88:91], v[40:47], v72\n"
    "s_waitcnt lgkmcnt(1)\n"
    "ds_read_b128 v[88:91], v73 offset:19456\n"
    "v_exp_f32 v50, v50\n"
    "v_exp_f32 v51, v51\n"
    "v_exp_f32 v52, v52\n"
    "v_mfma_f32_32x32x16_bf16 v[32:47], v[80:83], v[104:107], 0\n"
    "ds_read_b128 v[80:83], v73 offset:16384\n"
    "v_exp_f32 v53, v53\n"
    "v_exp_f32 v54, v54\n"
    "v_exp_f32 v55, v55\n"
    "v_exp_f32 v56, v56\n"
    "v_mfma_f32_32x32x16_bf16 v[32:47], v[84:87], v[108:111], v[32:47]\n"
    "ds_read_b128 v[84:87], v73 offset:17408\n"
    "v_exp_f32 v57, v57\n"
    "v_exp_f32 v58, v58\n"
    "v_exp_f32 v59, v59\n"
    "v_smfmac_f32_32x32x32_bf16 v[16:31], v[96:99], v[48:55], v72\n"
    "ds_read_b128 v[96:99], v73 offset:18432\n"
    "v_exp_f32 v60, v60\n"
    "v_exp_f32 v61, v61\n"
    "v_exp_f32 v62, v62\n"
    "v_exp_f32 v63, v63\n"
    "s_waitcnt vmcnt(2)\n"
    "v_cmp_ne_u64 vcc, s[60:61], v[250:251]\n"
    "s_nop 0\n"
    "s_cbranch_vccz Lok_24\n"
    "s_mov_b32 s10, 0x8000\n"
    "Lspin_25:\n"
    "buffer_load_dwordx2 v[250:251], v75, s[56:59], 0 offen sc1\n"
    "s_sub_u32 s10, s10, 1\n"
    "s_waitcnt vmcnt(0)\n"
    "s_cbranch_scc1 Lok_24\n"
    "v_cmp_ne_u64 vcc, s[60:61], v[250:251]\n"
    "s_nop 0\n"
    "s_cbranch_vccnz Lspin_25\n"
    "Lok_24:\n"
    "v_exp_f32 v32, v32\n"
    "v_exp_f32 v33, v33\n"
    "v_smfmac_f32_32x32x32_bf16 v[16:31], v[120:123], v[56:63], v72\n"
    "s_waitcnt lgkmcnt(1)\n"
    "ds_read_b128 v[120:123], v73 offset:23552\n"
    "v_exp_f32 v34, v34\n"
    "v_exp_f32 v35, v35\n"
    "v_exp_f32 v36, v36\n"
    "v_mfma_f32_32x32x16_bf16 v[48:63], v[80:83], v[104:107], 0\n"
    "ds_read_b128 v[80:83], v73 offset:20480\n"
    "v_exp_f32 v37, v37\n"
    "v_exp_f32 v38, v38\n"
    "v_exp_f32 v39, v39\n"
    "v_exp_f32 v40, v40\n"
    "v_mfma_f32_32x32x16_bf16 v[48:63], v[84:87], v[108:111], v[48:63]\n"
    "ds_read_b128 v[84:87], v73 offset:21504\n"
    "v_exp_f32 v41, v41\n"
    "v_exp_f32 v42, v42\n"
    "v_exp_f32 v43, v43\n"
    "v_smfmac_f32_32x32x32_bf16 v[16:31], v[100:103], v[32:39], v72\n"
    "s_add_u32 s19, s96, 0\n"
    "s_setprio 2\n"
    "ds_read_b128 v[100:103], v73 offset:22528\n"
    "buffer_load_dwordx4 v[128:131], v1, s[84:87], s19 offen offset:0 sc1\n"
    "v_exp_f32 v44, v44\n"
    "v_exp_f32 v45, v45\n"
    "v_exp_f32 v46, v46\n"
    "v_exp_f32 v47, v47\n"
    "v_exp_f32 v48, v48\n"
    "v_exp_f32 v49, v49\n"
    "v_smfmac_f32_32x32x32_bf16 v[16:31], v[8:11], v[40:47], v72\n"
    "s_waitcnt lgkmcnt(1)\n"
    "ds_read_b128 v[8:11], v73 offset:35840\n"
    "v_exp_f32 v50, v50\n"
    "v_exp_f32 v51, v51\n"
    "v_exp_f32 v52, v52\n"
    "v_mfma_f32_32x32x16_bf16 v[32:47], v[80:83], v[104:107], 0\n"
    "s_add_u32 s20, s21, 0\n"
    "ds_read_b128 v[80:83], v73 offset:32768\n"
    "buffer_load_dwordx4 v[132:135], v1, s[84:87], s20 offen offset:0 sc1\n"
    "v_exp_f32 v53, v53\n"
    "v_exp_f32 v54, v54\n"
    "v_exp_f32 v55, v55\n"
    "v_exp_f32 v56, v56\n"
    "v_mfma_f32_32x32x16_bf16 v[32:47], v[84:87], v[108:111], v[32:47]\n"
    "ds_read_b128 v[84:87], v73 offset:33792\n"
    "buffer_load_dwordx4 v[136:139], v1, s[84:87], s19 offen offset:1024 sc1\n"
    "v_exp_f32 v57, v57\n"
    "v_exp_f32 v58, v58\n"
    "v_exp_f32 v59, v59\n"
    "v_smfmac_f32_32x32x32_bf16 v[16:31], v[96:99], v[48:55], v72\n"
    "ds_read_b128 v[96:99], v73 offset:34816\n"
    "buffer_load_dwordx4 v[140:143], v1, s[84:87], s20 offen offset:1024 sc1\n"
    "v_exp_f32 v60, v60\n"
    "v_exp_f32 v61, v61\n"
    "v_exp_f32 v62, v62\n"
    "v_exp_f32 v63, v63\n"
    "v_exp_f32 v32, v32\n"
    "v_exp_f32 v33, v33\n"
    "v_smfmac_f32_32x32x32_bf16 v[16:31], v[88:91], v[56:63], v72\n"
    "s_waitcnt lgkmcnt(1)\n"
    "ds_read_b128 v[88:91], v73 offset:39936\n"
    "v_exp_f32 v34, v34\n"
    "v_exp_f32 v35, v35\n"
    "v_exp_f32 v36, v36\n"
    "v_mfma_f32_32x32x16_bf16 v[48:63], v[80:83], v[104:107], 0\n"
    "ds_read_b128 v[80:83], v73 offset:36864\n"
    "buffer_load_dwordx4 v[144:147], v1, s[84:87], s19 offen offset:2048 sc1\n"
    "v_exp_f32 v37, v37\n"
    "v_exp_f32 v38, v38\n"
    "v_exp_f32 v39, v39\n"
    "v_exp_f32 v40, v40\n"
    "v_mfma_f32_32x32x16_bf16 v[48:63], v[84:87], v[108:111], v[48:63]\n"
    "s_setprio 1\n"
    "ds_read_b128 v[84:87], v73 offset:37888\n"
    "buffer_load_dwordx4 v[148:151], v1, s[84:87], s20 offen offset:2048 sc1\n"
    "v_exp_f32 v41, v41\n"
    "v_exp_f32 v42, v42\n"
    "v_exp_f32 v43, v43\n"
    "v_smfmac_f32_32x32x32_bf16 v[16:31], v[100:103], v[32:39], v72\n"
    "s_setprio 0\n"
    "ds_read_b128 v[100:103], v73 offset:38912\n"
    "buffer_load_dwordx4 v[152:155], v1, s[84:87], s19 offen offset:3072 sc1\n"
    "v_exp_f32 v44, v44\n"
    "v_exp_f32 v45, v45\n"
    "v_exp_f32 v46, v46\n"
    "v_exp_f32 v47, v47\n"
    "v_exp_f32 v48, v48\n"
    "v_exp_f32 v49, v49\n"
    "v_smfmac_f32_32x32x32_bf16 v[16:31], v[120:123], v[40:47], v72\n"
    "s_waitcnt lgkmcnt(0)\n"
    "s_barrier\n"
    "s_setprio 3\n"
    "ds_read_b128 v[120:123], v74 offset:3072\n"
    "v_exp_f32 v50, v50\n"
    "v_exp_f32 v51, v51\n"
    "v_exp_f32 v52, v52\n"
    "v_mfma_f32_32x32x16_bf16 v[32:47], v[80:83], v[104:107], 0\n"
    "ds_read_b128 v[80:83], v74 offset:0\n"
    "v_exp_f32 v53, v53\n"
    "v_exp_f32 v54, v54\n"
    "v_exp_f32 v55, v55\n"
    "v_exp_f32 v56, v56\n"
    "v_mfma_f32_32x32x16_bf16 v[32:47], v[84:87], v[108:111], v[32:47]\n"
    "ds_read_b128 v[84:87], v74 offset:1024\n"
    "v_exp_f32 v57, v57\n"
    "v_exp_f32 v58, v58\n"
    "v_exp_f32 v59, v59\n"
    "v_smfmac_f32_32x32x32_bf16 v[16:31], v[96:99], v[48:55], v72\n"
    "ds_read_b128 v[96:99], v74 offset:2048\n"
    "buffer_load_dwordx4 v[156:159], v1, s[84:87], s20 offen offset:3072 sc1\n"
    "v_exp_f32 v60, v60\n"
    "v_exp_f32 v61, v61\n"
    "v_exp_f32 v62, v62\n"
    "v_exp_f32 v63, v63\n"
    "s_nop 0\n"
    "v_exp_f32 v32, v32\n"
    "v_exp_f32 v33, v33\n"
    "v_smfmac_f32_32x32x32_bf16 v[16:31], v[8:11], v[56:63], v72\n"
    "s_waitcnt lgkmcnt(1)\n"
    "ds_read_b128 v[8:11], v74 offset:7168\n"
    "v_exp_f32 v34, v34\n"
    "v_exp_f32 v35, v35\n"
    "v_exp_f32 v36, v36\n"
    "v_mfma_f32_32x32x16_bf16 v[48:63], v[80:83], v[104:107], 0\n"
    "ds_read_b128 v[80:83], v74 offset:4096\n"
    "v_exp_f32 v37, v37\n"
    "v_exp_f32 v38, v38\n"
    "v_exp_f32 v39, v39\n"
    "v_exp_f32 v40, v40\n"
    "v_mfma_f32_32x32x16_bf16 v[48:63], v[84:87], v[108:111], v[48:63]\n"
    "ds_read_b128 v[84:87], v74 offset:5120\n"
    "v_exp_f32 v41, v41\n"
    "v_exp_f32 v42, v42\n"
    "v_exp_f32 v43, v43\n"
    "v_smfmac_f32_32x32x32_bf16 v[16:31], v[100:103], v[32:39], v72\n"
    "s_add_u32 s19, s96, 4096\n"
    "ds_read_b128 v[100:103], v74 offset:6144\n"
    "buffer_load_dwordx4 v[160:163], v1, s[84:87], s19 offen offset:0 sc1\n"
    "v_exp_f32 v44, v44\n"
    "v_exp_f32 v45, v45\n"
    "v_exp_f32 v46, v46\n"
    "v_exp_f32 v47, v47\n"
    "v_exp_f32 v48, v48\n"
    "v_exp_f32 v49, v49\n"
    "v_smfmac_f32_32x32x32_bf16 v[16:31], v[88:91], v[40:47], v72\n"
    "s_waitcnt lgkmcnt(1)\n"
    "ds_read_b128 v[88:91], v74 offset:19456\n"
    "v_exp_f32 v50, v50\n"
    "v_exp_f32 v51, v51\n"
    "v_exp_f32 v52, v52\n"
    "v_mfma_f32_32x32x16_bf16 v[32:47], v[80:83], v[104:107], 0\n"
    "ds_read_b128 v[80:83], v74 offset:16384\n"
    "v_exp_f32 v53, v53\n"
    "v_exp_f32 v54, v54\n"
    "v_exp_f32 v55, v55\n"
    "v_exp_f32 v56, v56\n"
    "v_mfma_f32_32x32x16_bf16 v[32:47], v[84:87], v[108:111], v[32:47]\n"
    "ds_read_b128 v[84:87], v74 offset:17408\n"
    "v_exp_f32 v57, v57\n"
    "v_exp_f32 v58, v58\n"
    "v_exp_f32 v59, v59\n"
    "v_smfmac_f32_32x32x32_bf16 v[16:31], v[96:99], v[48:55], v72\n"
    "s_add_u32 s20, s21, 4096\n"
    "ds_read_b128 v[96:99], v74 offset:18432\n"
    "buffer_load_dwordx4 v[164:167], v1, s[84:87], s20 offen offset:0 sc1\n"
    "v_exp_f32 v60, v60\n"
    "v_exp_f32 v61, v61\n"
    "v_exp_f32 v62, v62\n"
    "v_exp_f32 v63, v63\n"
    "v_exp_f32 v32, v32\n"
    "v_exp_f32 v33, v33\n"
    "v_smfmac_f32_32x32x32_bf16 v[16:31], v[120:123], v[56:63], v72\n"
    "s_waitcnt lgkmcnt(1)\n"
    "ds_read_b128 v[120:123], v74 offset:23552\n"
    "v_exp_f32 v34, v34\n"
    "v_exp_f32 v35, v35\n"
    "v_exp_f32 v36, v36\n"
    "v_mfma_f32_32x32x16_bf16 v[48:63], v[80:83], v[104:107], 0\n"
    "ds_read_b128 v[80:83], v74 offset:20480\n"
    "v_exp_f32 v37, v37\n"
    "v_exp_f32 v38, v38\n"
    "v_exp_f32 v39, v39\n"
    "v_exp_f32 v40, v40\n"
    "v_mfma_f32_32x32x16_bf16 v[48:63], v[84:87], v[108:111], v[48:63]\n"
    "ds_read_b128 v[84:87], v74 offset:21504\n"
    "v_exp_f32 v41, v41\n"
    "v_exp_f32 v42, v42\n"
    "v_exp_f32 v43, v43\n"
    "v_smfmac_f32_32x32x32_bf16 v[16:31], v[100:103], v[32:39], v72\n"
    "s_setprio 2\n"
    "ds_read_b128 v[100:103], v74 offset:22528\n"
    "buffer_load_dwordx4 v[168:171], v1, s[84:87], s19 offen offset:1024 sc1\n"
    "v_exp_f32 v44, v44\n"
    "v_exp_f32 v45, v45\n"
    "v_exp_f32 v46, v46\n"
    "v_exp_f32 v47, v47\n"
    "v_exp_f32 v48, v48\n"
    "v_exp_f32 v49, v49\n"
    "v_smfmac_f32_32x32x32_bf16 v[16:31], v[8:11], v[40:47], v72\n"
    "s_waitcnt lgkmcnt(1)\n"
    "ds_read_b128 v[8:11], v74 offset:35840\n"
    "v_exp_f32 v50, v50\n"
    "v_exp_f32 v51, v51\n"
    "v_exp_f32 v52, v52\n"
    "v_mfma_f32_32x32x16_bf16 v[32:47], v[80:83], v[104:107], 0\n"
    "ds_read_b128 v[80:83], v74 offset:32768\n"
    "buffer_load_dwordx4 v[172:175], v1, s[84:87], s20 offen offset:1024 sc1\n"
    "v_exp_f32 v53, v53\n"
    "v_exp_f32 v54, v54\n"
    "v_exp_f32 v55, v55\n"
    "v_exp_f32 v56, v56\n"
    "v_mfma_f32_32x32x16_bf16 v[32:47], v[84:87], v[108:111], v[32:47]\n"
    "ds_read_b128 v[84:87], v74 offset:33792\n"
    "buffer_load_dwordx4 v[176:179], v1, s[84:87], s19 offen offset:2048 sc1\n"
    "v_exp_f32 v57, v57\n"
    "v_exp_f32 v58, v58\n"
    "v_exp_f32 v59, v59\n"
    "v_smfmac_f32_32x32x32_bf16 v[16:31], v[96:99], v[48:55], v72\n"
    "ds_read_b128 v[96:99], v74 offset:34816\n"
    "buffer_load_dwordx4 v[180:183], v1, s[84:87], s20 offen offset:2048 sc1\n"
    "v_exp_f32 v60, v60\n"
    "v_exp_f32 v61, v61\n"
    "v_exp_f32 v62, v62\n"
    "v_exp_f32 v63, v63\n"
    "v_exp_f32 v32, v32\n"
    "v_exp_f32 v33, v33\n"
    "v_smfmac_f32_32x32x32_bf16 v[16:31], v[88:91], v[56:63], v72\n"
    "s_waitcnt lgkmcnt(1)\n"
    "ds_read_b128 v[88:91], v74 offset:39936\n"
    "v_exp_f32 v34, v34\n"
    "v_exp_f32 v35, v35\n"
    "v_exp_f32 v36, v36\n"
    "v_mfma_f32_32x32x16_bf16 v[48:63], v[80:83], v[104:107], 0\n"
    "ds_read_b128 v[80:83], v74 offset:36864\n"
    "buffer_load_dwordx4 v[184:187], v1, s[84:87], s19 offen offset:3072 sc1\n"
    "v_exp_f32 v37, v37\n"
    "v_exp_f32 v38, v38\n"
    "v_exp_f32 v39, v39\n"
    "v_exp_f32 v40, v40\n"
    "v_mfma_f32_32x32x16_bf16 v[48:63], v[84:87], v[108:111], v[48:63]\n"
    "s_setprio 1\n"
    "ds_read_b128 v[84:87], v74 offset:37888\n"
    "v_exp_f32 v41, v41\n"
    "v_exp_f32 v42, v42\n"
    "v_exp_f32 v43, v43\n"
    "v_smfmac_f32_32x32x32_bf16 v[16:31], v[100:103], v[32:39], v72\n"
    "s_setprio 0\n"
    "ds_read_b128 v[100:103], v74 offset:38912\n"
    "buffer_load_dwordx4 v[188:191], v1, s[84:87], s20 offen offset:3072 sc1\n"
    "v_exp_f32 v44, v44\n"
    "v_exp_f32 v45, v45\n"
    "v_exp_f32 v46, v46\n"
    "v_exp_f32 v47, v47\n"
    "v_exp_f32 v48, v48\n"
    "v_exp_f32 v49, v49\n"
    "v_smfmac_f32_32x32x32_bf16 v[16:31], v[120:123], v[40:47], v72\n"
    "s_waitcnt lgkmcnt(0)\n"
    "s_barrier\n"
    "s_setprio 3\n"
    "ds_read_b128 v[120:123], v73 offset:3072\n"
    "v_exp_f32 v50, v50\n"
    "v_exp_f32 v51, v51\n"
    "v_exp_f32 v52, v52\n"
    "v_mfma_f32_32x32x16_bf16 v[32:47], v[80:83], v[104:107], 0\n"
    "ds_read_b128 v[80:83], v73 offset:0\n"
    "v_exp_f32 v53, v53\n"
    "v_exp_f32 v54, v54\n"
    "v_exp_f32 v55, v55\n"
    "v_exp_f32 v56, v56\n"
    "v_mfma_f32_32x32x16_bf16 v[32:47], v[84:87], v[108:111], v[32:47]\n"
    "ds_read_b128 v[84:87], v73 offset:1024\n"
    "v_exp_f32 v57, v57\n"
    "v_exp_f32 v58, v58\n"
    "v_exp_f32 v59, v59\n"
    "v_smfmac_f32_32x32x32_bf16 v[16:31], v[96:99], v[48:55], v72\n"
    "s_add_u32 m0, s16, 0x1e000\n"
    "s_add_u32 s18, s97, 0xe0000\n"
    "ds_read_b128 v[96:99], v73 offset:2048\n"
    "buffer_load_dwordx4 v2, s[48:51], s18 offen lds\n"
    "v_exp_f32 v60, v60\n"
    "v_exp_f32 v61, v61\n"
    "v_exp_f32 v62, v62\n"
    "v_exp_f32 v63, v63\n"
    "v_exp_f32 v32, v32\n"
    "v_exp_f32 v33, v33\n"
    "v_smfmac_f32_32x32x32_bf16 v[16:31], v[8:11], v[56:63], v72\n"
    "s_waitcnt lgkmcnt(1)\n"
    "ds_read_b128 v[8:11], v73 offset:7168\n"
    "v_exp_f32 v34, v34\n"
    "v_exp_f32 v35, v35\n"
    "v_exp_f32 v36, v36\n"
    "v_mfma_f32_32x32x16_bf16 v[48:63], v[80:83], v[104:107], 0\n"
    "ds_read_b128 v[80:83], v73 offset:4096\n"
    "buffer_load_dwordx4 v3, s[48:51], s18 offen offset:1024 lds\n"
    "v_exp_f32 v37, v37\n"
    "v_exp_f32 v38, v38\n"
    "v_exp_f32 v39, v39\n"
    "v_exp_f32 v40, v40\n"
    "v_mfma_f32_32x32x16_bf16 v[48:63], v[84:87], v[108:111], v[48:63]\n"
    "ds_read_b128 v[84:87], v73 offset:5120\n"
    "v_exp_f32 v41, v41\n"
    "v_exp_f32 v42, v42\n"
    "v_exp_f32 v43, v43\n"
    "v_smfmac_f32_32x32x32_bf16 v[16:31], v[100:103], v[32:39], v72\n"
    "s_add_u32 s19, s96, 8192\n"
    "ds_read_b128 v[100:103], v73 offset:6144\n"
    "buffer_load_dwordx4 v[192:195], v1, s[84:87], s19 offen offset:0 sc1\n"
    "v_exp_f32 v44, v44\n"
    "v_exp_f32 v45, v45\n"
    "v_exp_f32 v46, v46\n"
    "v_exp_f32 v47, v47\n"
    "v_exp_f32 v48, v48\n"
    "v_exp_f32 v49, v49\n"
    "v_smfmac_f32_32x32x32_bf16 v[16:31], v[88:91], v[40:47], v72\n"
    "s_waitcnt lgkmcnt(1)\n"
    "ds_read_b128 v[88:91], v73 offset:19456\n"
    "v_exp_f32 v50, v50\n"
    "v_exp_f32 v51, v51\n"
    "v_exp_f32 v52, v52\n"
    "v_mfma_f32_32x32x16_bf16 v[32:47], v[80:83], v[104:107], 0\n"
    "s_add_u32 s20, s21, 8192\n"
    "ds_read_b128 v[80:83], v73 offset:16384\n"
    "buffer_load_dwordx4 v[196:199], v1, s[84:87], s20 offen offset:0 sc1\n"
    "v_exp_f32 v53, v53\n"
    "v_exp_f32 v54, v54\n"
    "v_exp_f32 v55, v55\n"
    "v_exp_f32 v56, v56\n"
    "v_mfma_f32_32x32x16_bf16 v[32:47], v[84:87], v[108:111], v[32:47]\n"
    "ds_read_b128 v[84:87], v73 offset:17408\n"
    "v_exp_f32 v57, v57\n"
    "v_exp_f32 v58, v58\n"
    "v_exp_f32 v59, v59\n"
    "v_smfmac_f32_32x32x32_bf16 v[16:31], v[96:99], v[48:55], v72\n"
    "ds_read_b128 v[96:99], v73 offset:18432\n"
    "buffer_load_dwordx4 v[200:203], v1, s[84:87], s19 offen offset:1024 sc1\n"
    "v_exp_f32 v60, v60\n"
    "v_exp_f32 v61, v61\n"
    "v_exp_f32 v62, v62\n"
    "v_exp_f32 v63, v63\n"
    "s_nop 0\n"
    "v_exp_f32 v32, v32\n"
    "v_exp_f32 v33, v33\n"
    "v_smfmac_f32_32x32x32_bf16 v[16:31], v[120:123], v[56:63], v72\n"
    "s_waitcnt lgkmcnt(1)\n"
    "ds_read_b128 v[120:123], v73 offset:23552\n"
    "v_exp_f32 v34, v34\n"
    "v_exp_f32 v35, v35\n"
    "v_exp_f32 v36, v36\n"
    "v_mfma_f32_32x32x16_bf16 v[48:63], v[80:83], v[104:107], 0\n"
    "s_add_u32 m0, s16, 0x22000\n"
    "ds_read_b128 v[80:83], v73 offset:20480\n"
    "s_add_u32 s18, s97, 0xe7000\n"
    "buffer_load_dwordx4 v2, s[48:51], s18 offen lds\n"
    "v_exp_f32 v37, v37\n"
    "v_exp_f32 v38, v38\n"
    "v_exp_f32 v39, v39\n"
    "v_exp_f32 v40, v40\n"
    "v_mfma_f32_32x32x16_bf16 v[48:63], v[84:87], v[108:111], v[48:63]\n"
    "ds_read_b128 v[84:87], v73 offset:21504\n"
    "v_exp_f32 v41, v41\n"
    "v_exp_f32 v42, v42\n"
    "v_exp_f32 v43, v43\n"
    "v_smfmac_f32_32x32x32_bf16 v[16:31], v[100:103], v[32:39], v72\n"
    "s_setprio 2\n"
    "ds_read_b128 v[100:103], v73 offset:22528\n"
    "buffer_load_dwordx4 v[204:207], v1, s[84:87], s20 offen offset:1024 sc1\n"
    "v_exp_f32 v44, v44\n"
    "v_exp_f32 v45, v45\n"
    "v_exp_f32 v46, v46\n"
    "v_exp_f32 v47, v47\n"
    "v_exp_f32 v48, v48\n"
    "v_exp_f32 v49, v49\n"
    "v_smfmac_f32_32x32x32_bf16 v[16:31], v[8:11], v[40:47], v72\n"
    "s_waitcnt lgkmcnt(1)\n"
    "ds_read_b128 v[8:11], v73 offset:35840\n"
    "v_exp_f32 v50, v50\n"
    "v_exp_f32 v51, v51\n"
    "v_exp_f32 v52, v52\n"
    "v_mfma_f32_32x32x16_bf16 v[32:47], v[80:83], v[104:107], 0\n"
    "ds_read_b128 v[80:83], v73 offset:32768\n"
    "buffer_load_dwordx4 v3, s[48:51], s18 offen offset:1024 lds\n"
    "v_exp_f32 v53, v53\n"
    "v_exp_f32 v54, v54\n"
    "v_exp_f32 v55, v55\n"
    "v_exp_f32 v56, v56\n"
    "v_mfma_f32_32x32x16_bf16 v[32:47], v[84:87], v[108:111], v[32:47]\n"
    "ds_read_b128 v[84:87], v73 offset:33792\n"
    "buffer_load_dwordx4 v[208:211], v1, s[84:87], s19 offen offset:2048 sc1\n"
    "v_exp_f32 v57, v57\n"
    "v_exp_f32 v58, v58\n"
    "v_exp_f32 v59, v59\n"
    "v_smfmac_f32_32x32x32_bf16 v[16:31], v[96:99], v[48:55], v72\n"
    "ds_read_b128 v[96:99], v73 offset:34816\n"
    "buffer_load_dwordx4 v[212:215], v1, s[84:87], s20 offen offset:2048 sc1\n"
    "v_exp_f32 v60, v60\n"
    "v_exp_f32 v61, v61\n"
    "v_exp_f32 v62, v62\n"
    "v_exp_f32 v63, v63\n"
    "v_exp_f32 v32, v32\n"
    "v_exp_f32 v33, v33\n"
    "v_smfmac_f32_32x32x32_bf16 v[16:31], v[88:91], v[56:63], v72\n"
    "s_waitcnt lgkmcnt(1)\n"
    "ds_read_b128 v[88:91], v73 offset:39936\n"
    "v_exp_f32 v34, v34\n"
    "v_exp_f32 v35, v35\n"
    "v_exp_f32 v36, v36\n"
    "v_mfma_f32_32x32x16_bf16 v[48:63], v[80:83], v[104:107], 0\n"
    "ds_read_b128 v[80:83], v73 offset:36864\n"
    "buffer_load_dwordx4 v[216:219], v1, s[84:87], s19 offen offset:3072 sc1\n"
    "v_exp_f32 v37, v37\n"
    "v_exp_f32 v38, v38\n"
    "v_exp_f32 v39, v39\n"
    "v_exp_f32 v40, v40\n"
    "v_mfma_f32_32x32x16_bf16 v[48:63], v[84:87], v[108:111], v[48:63]\n"
    "s_setprio 1\n"
    "ds_read_b128 v[84:87], v73 offset:37888\n"
    "v_exp_f32 v41, v41\n"
    "v_exp_f32 v42, v42\n"
    "v_exp_f32 v43, v43\n"
    "v_smfmac_f32_32x32x32_bf16 v[16:31], v[100:103], v[32:39], v72\n"
    "s_setprio 0\n"
    "ds_read_b128 v[100:103], v73 offset:38912\n"
    "buffer_load_dwordx4 v[220:223], v1, s[84:87], s20 offen offset:3072 sc1\n"
    "v_exp_f32 v44, v44\n"
    "v_exp_f32 v45, v45\n"
    "v_exp_f32 v46, v46\n"
    "v_exp_f32 v47, v47\n"
    "v_exp_f32 v48, v48\n"
    "v_exp_f32 v49, v49\n"
    "v_smfmac_f32_32x32x32_bf16 v[16:31], v[120:123], v[40:47], v72\n"
    "s_waitcnt lgkmcnt(0)\n"
    "s_barrier\n"
    "s_setprio 3\n"
    "ds_read_b128 v[120:123], v74 offset:3072\n"
    "v_exp_f32 v50, v50\n"
    "v_exp_f32 v51, v51\n"
    "v_exp_f32 v52, v52\n"
    "v_mfma_f32_32x32x16_bf16 v[32:47], v[80:83], v[104:107], 0\n"
    "ds_read_b128 v[80:83], v74 offset:0\n"
    "v_exp_f32 v53, v53\n"
    "v_exp_f32 v54, v54\n"
    "v_exp_f32 v55, v55\n"
    "v_exp_f32 v56, v56\n"
    "v_mfma_f32_32x32x16_bf16 v[32:47], v[84:87], v[108:111], v[32:47]\n"
    "s_add_u32 m0, s16, 0xe000\n"
    "ds_read_b128 v[84:87], v74 offset:1024\n"
    "s_add_u32 s18, s97, 0xee000\n"
    "buffer_load_dwordx4 v2, s[48:51], s18 offen lds\n"
    "v_exp_f32 v57, v57\n"
    "v_exp_f32 v58, v58\n"
    "v_exp_f32 v59, v59\n"
    "v_smfmac_f32_32x32x32_bf16 v[16:31], v[96:99], v[48:55], v72\n"
    "s_add_u32 s19, s96, 12288\n"
    "ds_read_b128 v[96:99], v74 offset:2048\n"
    "buffer_load_dwordx4 v[224:227], v1, s[84:87], s19 offen offset:0 sc1\n"
    "v_exp_f32 v60, v60\n"
    "v_exp_f32 v61, v61\n"
    "v_exp_f32 v62, v62\n"
    "v_exp_f32 v63, v63\n"
    "v_exp_f32 v32, v32\n"
    "v_exp_f32 v33, v33\n"
    "v_smfmac_f32_32x32x32_bf16 v[16:31], v[8:11], v[56:63], v72\n"
    "s_waitcnt lgkmcnt(1)\n"
    "ds_read_b128 v[8:11], v74 offset:7168\n"
    "v_exp_f32 v34, v34\n"
    "v_exp_f32 v35, v35\n"
    "v_exp_f32 v36, v36\n"
    "v_mfma_f32_32x32x16_bf16 v[48:63], v[80:83], v[104:107], 0\n"
    "s_setprio 2\n"
    "ds_read_b128 v[80:83], v74 offset:4096\n"
    "buffer_load_dwordx4 v3, s[48:51], s18 offen offset:1024 lds\n"
    "v_exp_f32 v37, v37\n"
    "v_exp_f32 v38, v38\n"
    "v_exp_f32 v39, v39\n"
    "v_exp_f32 v40, v40\n"
    "v_mfma_f32_32x32x16_bf16 v[48:63], v[84:87], v[108:111], v[48:63]\n"
    "ds_read_b128 v[84:87], v74 offset:5120\n"
    "v_exp_f32 v41, v41\n"
    "v_exp_f32 v42, v42\n"
    "v_exp_f32 v43, v43\n"
    "v_smfmac_f32_32x32x32_bf16 v[16:31], v[100:103], v[32:39], v72\n"
    "s_add_u32 s20, s21, 12288\n"
    "s_setprio 0\n"
    "ds_read_b128 v[100:103], v74 offset:6144\n"
    "buffer_load_dwordx4 v[228:231], v1, s[84:87], s20 offen offset:0 sc1\n"
    "v_exp_f32 v44, v44\n"
    "v_exp_f32 v45, v45\n"
    "v_exp_f32 v46, v46\n"
    "v_exp_f32 v47, v47\n"
    "v_exp_f32 v48, v48\n"
    "v_exp_f32 v49, v49\n"
    "v_smfmac_f32_32x32x32_bf16 v[16:31], v[88:91], v[40:47], v72\n"
    "s_waitcnt lgkmcnt(0)\n"
    "s_barrier\n"
    "s_setprio 0\n"
    "v_exp_f32 v50, v50\n"
    "v_exp_f32 v51, v51\n"
    "v_exp_f32 v52, v52\n"
    "v_mfma_f32_32x32x16_bf16 v[32:47], v[80:83], v[104:107], 0\n"
    "v_exp_f32 v53, v53\n"
    "v_exp_f32 v54, v54\n"
    "v_exp_f32 v55, v55\n"
    "v_exp_f32 v56, v56\n"
    "v_mfma_f32_32x32x16_bf16 v[32:47], v[84:87], v[108:111], v[32:47]\n"
    "s_add_u32 m0, s16, 0x12000\n"
    "s_add_u32 s18, s97, 0xf5000\n"
    "buffer_load_dwordx4 v2, s[48:51], s18 offen lds\n"
    "v_exp_f32 v57, v57\n"
    "v_exp_f32 v58, v58\n"
    "v_exp_f32 v59, v59\n"
    "v_smfmac_f32_32x32x32_bf16 v[16:31], v[96:99], v[48:55], v72\n"
    "buffer_load_dwordx4 v[232:235], v1, s[84:87], s19 offen offset:1024 sc1\n"
    "v_exp_f32 v60, v60\n"
    "v_exp_f32 v61, v61\n"
    "v_exp_f32 v62, v62\n"
    "v_exp_f32 v63, v63\n"
    "v_exp_f32 v32, v32\n"
    "v_exp_f32 v33, v33\n"
    "v_smfmac_f32_32x32x32_bf16 v[16:31], v[120:123], v[56:63], v72\n"
    "buffer_load_dwordx4 v3, s[48:51], s18 offen offset:1024 lds\n"
    "v_exp_f32 v34, v34\n"
    "v_exp_f32 v35, v35\n"
    "v_exp_f32 v36, v36\n"
    "v_exp_f32 v37, v37\n"
    "v_exp_f32 v38, v38\n"
    "v_exp_f32 v39, v39\n"
    "v_exp_f32 v40, v40\n"
    "v_exp_f32 v41, v41\n"
    "v_exp_f32 v42, v42\n"
    "v_exp_f32 v43, v43\n"
    "v_smfmac_f32_32x32x32_bf16 v[16:31], v[100:103], v[32:39], v72\n"
    "buffer_load_dwordx4 v[236:239], v1, s[84:87], s20 offen offset:1024 sc1\n"
    "v_exp_f32 v44, v44\n"
    "v_exp_f32 v45, v45\n"
    "v_exp_f32 v46, v46\n"
    "v_exp_f32 v47, v47\n"
    "s_nop 1\n"
    "v_smfmac_f32_32x32x32_bf16 v[16:31], v[8:11], v[40:47], v72\n"
    "Lc_h1:\n"
    "s_waitcnt vmcnt(18) lgkmcnt(0)\n"
    "s_barrier\n"
    "s_setprio 3\n"
    "ds_read_b128 v[44:47], v76 offset:3072\n"
    "ds_read_b128 v[32:35], v76 offset:0\n"
    "ds_read_b128 v[36:39], v76 offset:1024\n"
    "ds_read_b128 v[40:43], v76 offset:2048\n"
    "v_lshrrev_b32 v13, 11, v77\n"
    "v_and_b32 v12, 0xff, v77\n"
    "v_or_b32 v12, v12, v13\n"
    "s_waitcnt lgkmcnt(3)\n"
    "s_nop 0\n"
    "v_add_f32 v116, v28, v44\n"
    "v_add_f32 v117, v28, v44\n"
    "v_add_f32 v28, v28, v44\n"
    "v_add_f32 v29, v29, v45\n"
    "v_add_f32 v30, v30, v46\n"
    "v_add_f32 v31, v31, v47\n"
    "v_permlane32_swap_b32 v116, v117\n"
    "s_waitcnt lgkmcnt(2)\n"
    "v_add_f32 v16, v16, v32\n"
    "v_add_f32 v17, v17, v33\n"
    "v_add_f32 v18, v18, v34\n"
    "v_add_f32 v19, v19, v35\n"
    "s_waitcnt lgkmcnt(1)\n"
    "v_add_f32 v20, v20, v36\n"
    "v_add_f32 v21, v21, v37\n"
    "v_add_f32 v22, v22, v38\n"
    "v_add_f32 v23, v23, v39\n"
    "s_waitcnt lgkmcnt(0)\n"
    "v_add_f32 v24, v24, v40\n"
    "v_add_f32 v25, v25, v41\n"
    "v_add_f32 v26, v26, v42\n"
    "v_add_f32 v27, v27, v43\n"
    "v_maximum3_f32 v118, |v16|, |v17|, |v31|\n"
    "v_maximum3_f32 v119, |v18|, |v19|, |v20|\n"
    "v_maximum3_f32 v118, v118, |v21|, |v22|\n"
    "v_maximum3_f32 v119, v119, |v23|, |v24|\n"
    "v_maximum3_f32 v118, v118, |v25|, |v26|\n"
    "v_maximum3_f32 v119, v119, |v27|, |v28|\n"
    "v_mul_f32 v116, 0x38000000, v117\n"
    "v_maximum3_f32 v118, v118, |v29|, |v30|\n"
    "v_maximum3_f32 v118, v118, v119, v119\n"
    "v_minimum3_f32 v116, v116, v118, v118\n"
    "v_cmp_ngt_f32 vcc, 0x7d800000, v118\n"
    "s_nop 0\n"
    "s_mov_b64 s[66:67], vcc\n"
    "v_cmp_nlt_f32 vcc, 0x04800000, v116\n"
    "s_nop 0\n"
    "s_or_b64 vcc, vcc, s[66:67]\n"
    "s_cbranch_vccnz Lfallback\n"
    "Lnorm:\n"
    "v_rcp_f32 v117, v117\n"
    "s_nop 0\n"
    "v_mul_f32 v16, v16, v117\n"
    "v_mul_f32 v17, v17, v117\n"
    "v_mul_f32 v18, v18, v117\n"
    "v_mul_f32 v19, v19, v117\n"
    "v_mul_f32 v20, v20, v117\n"
    "v_mul_f32 v21, v21, v117\n"
    "v_mul_f32 v22, v22, v117\n"
    "v_mul_f32 v23, v23, v117\n"
    "v_cvt_pk_bf16_f32 v32, v16, v17\n"
    "v_cvt_pk_bf16_f32 v33, v18, v19\n"
    "v_cvt_pk_bf16_f32 v34, v20, v21\n"
    "v_cvt_pk_bf16_f32 v35, v22, v23\n"
    "buffer_store_dwordx4 v[32:35], v77, s[56:59], s93 offen offset:0 sc1\n"
    "v_mul_f32 v24, v24, v117\n"
    "v_mul_f32 v25, v25, v117\n"
    "v_mul_f32 v26, v26, v117\n"
    "v_mul_f32 v27, v27, v117\n"
    "v_mul_f32 v28, v28, v117\n"
    "v_mul_f32 v29, v29, v117\n"
    "v_mul_f32 v30, v30, v117\n"
    "v_mul_f32 v31, v31, v117\n"
    "v_cvt_pk_bf16_f32 v36, v24, v25\n"
    "v_cvt_pk_bf16_f32 v37, v26, v27\n"
    "v_cvt_pk_bf16_f32 v38, v28, v29\n"
    "v_cvt_pk_bf16_f32 v39, v30, v31\n"
    "s_mov_b32 exec_hi, 0\n"
    "buffer_store_dwordx4 v[36:39], v77, s[56:59], s93 offen offset:512 sc1\n"
    "s_not_b64 exec, exec\n"
    "buffer_store_dwordx2 v[36:37], v12, s[56:59], s7 offen sc1\n"
    "s_mov_b64 exec, -1\n"
    "s_cmp_ge_u32 s27, 28\n"
    "s_cbranch_scc1 Lidle_h1\n"
    "ds_read_b64_tr_b16 v[48:49], v124 offset:0\n"
    "ds_read_b64_tr_b16 v[50:51], v124 offset:2048\n"
    "ds_read_b64_tr_b16 v[52:53], v125 offset:0\n"
    "ds_read_b64_tr_b16 v[54:55], v125 offset:2048\n"
    "ds_read_b64_tr_b16 v[56:57], v126 offset:0\n"
    "ds_read_b64_tr_b16 v[58:59], v126 offset:2048\n"
    "ds_read_b64_tr_b16 v[60:61], v127 offset:0\n"
    "ds_read_b64_tr_b16 v[62:63], v127 offset:2048\n"
    "s_barrier\n"
    "s_setprio 2\n"
    "s_waitcnt lgkmcnt(6)\n"
    "v_mfma_f32_16x16x32_bf16 v[16:19], v[48:51], v[128:131], 0\n"
    "ds_read_b64_tr_b16 v[64:65], v124 offset:16384\n"
    "v_mfma_f32_16x16x32_bf16 v[20:23], v[48:51], v[132:135], 0\n"
    "s_waitcnt lgkmcnt(5)\n"
    "ds_read_b64_tr_b16 v[66:67], v124 offset:18432\n"
    "v_mfma_f32_16x16x32_bf16 v[28:31], v[52:55], v[132:135], 0\n"
    "ds_read_b64_tr_b16 v[68:69], v125 offset:16384\n"
    "v_mfma_f32_16x16x32_bf16 v[24:27], v[52:55], v[128:131], 0\n"
    "s_waitcnt lgkmcnt(5)\n"
    "ds_read_b64_tr_b16 v[70:71], v125 offset:18432\n"
    "v_mfma_f32_16x16x32_bf16 v[32:35], v[56:59], v[128:131], 0\n"
    "ds_read_b64_tr_b16 v[72:73], v126 offset:16384\n"
    "v_mfma_f32_16x16x32_bf16 v[36:39], v[56:59], v[132:135], 0\n"
    "s_waitcnt lgkmcnt(5)\n"
    "ds_read_b64_tr_b16 v[74:75], v126 offset:18432\n"
    "v_mfma_f32_16x16x32_bf16 v[44:47], v[60:63], v[132:135], 0\n"
    "ds_read_b64_tr_b16 v[76:77], v127 offset:16384\n"
    "v_mfma_f32_16x16x32_bf16 v[40:43], v[60:63], v[128:131], 0\n"
    "s_waitcnt lgkmcnt(5)\n"
    "ds_read_b64_tr_b16 v[78:79], v127 offset:18432\n"
    "v_mfma_f32_16x16x32_bf16 v[16:19], v[64:67], v[136:139], v[16:19]\n"
    "ds_read_b64_tr_b16 v[48:49], v124 offset:32768\n"
    "v_mfma_f32_16x16x32_bf16 v[20:23], v[64:67], v[140:143], v[20:23]\n"
    "s_waitcnt lgkmcnt(5)\n"
    "ds_read_b64_tr_b16 v[50:51], v124 offset:34816\n"
    "v_mfma_f32_16x16x32_bf16 v[28:31], v[68:71], v[140:143], v[28:31]\n"
    "ds_read_b64_tr_b16 v[52:53], v125 offset:32768\n"
    "v_mfma_f32_16x16x32_bf16 v[24:27], v[68:71], v[136:139], v[24:27]\n"
    "s_waitcnt lgkmcnt(5)\n"
    "ds_read_b64_tr_b16 v[54:55], v125 offset:34816\n"
    "v_mfma_f32_16x16x32_bf16 v[32:35], v[72:75], v[136:139], v[32:35]\n"
    "ds_read_b64_tr_b16 v[56:57], v126 offset:32768\n"
    "v_mfma_f32_16x16x32_bf16 v[36:39], v[72:75], v[140:143], v[36:39]\n"
    "s_waitcnt lgkmcnt(5)\n"
    "ds_read_b64_tr_b16 v[58:59], v126 offset:34816\n"
    "v_mfma_f32_16x16x32_bf16 v[44:47], v[76:79], v[140:143], v[44:47]\n"
    "ds_read_b64_tr_b16 v[60:61], v127 offset:32768\n"
    "v_mfma_f32_16x16x32_bf16 v[40:43], v[76:79], v[136:139], v[40:43]\n"
    "s_waitcnt lgkmcnt(5)\n"
    "ds_read_b64_tr_b16 v[62:63], v127 offset:34816\n"
    "v_mfma_f32_16x16x32_bf16 v[16:19], v[48:51], v[144:147], v[16:19]\n"
    "Lst1_7:\n"
    "s_cbranch_execz Lr1_7\n"
    "s_setprio 1\n"
    "v_add_u32 v124, 0x18000, v124\n"
    "ds_read_b64_tr_b16 v[64:65], v124 offset:49152\n"
    "v_mfma_f32_16x16x32_bf16 v[20:23], v[48:51], v[148:151], v[20:23]\n"
    "s_waitcnt lgkmcnt(5)\n"
    "ds_read_b64_tr_b16 v[66:67], v124 offset:51200\n"
    "v_mfma_f32_16x16x32_bf16 v[28:31], v[52:55], v[148:151], v[28:31]\n"
    "v_add_u32 v125, 0x18000, v125\n"
    "ds_read_b64_tr_b16 v[68:69], v125 offset:49152\n"
    "v_mfma_f32_16x16x32_bf16 v[24:27], v[52:55], v[144:147], v[24:27]\n"
    "s_waitcnt lgkmcnt(5)\n"
    "ds_read_b64_tr_b16 v[70:71], v125 offset:51200\n"
    "v_mfma_f32_16x16x32_bf16 v[32:35], v[56:59], v[144:147], v[32:35]\n"
    "v_add_u32 v126, 0x18000, v126\n"
    "ds_read_b64_tr_b16 v[72:73], v126 offset:49152\n"
    "v_mfma_f32_16x16x32_bf16 v[36:39], v[56:59], v[148:151], v[36:39]\n"
    "s_waitcnt lgkmcnt(5)\n"
    "ds_read_b64_tr_b16 v[74:75], v126 offset:51200\n"
    "v_mfma_f32_16x16x32_bf16 v[44:47], v[60:63], v[148:151], v[44:47]\n"
    "v_add_u32 v127, 0x18000, v127\n"
    "ds_read_b64_tr_b16 v[76:77], v127 offset:49152\n"
    "v_mfma_f32_16x16x32_bf16 v[40:43], v[60:63], v[144:147], v[40:43]\n"
    "s_waitcnt lgkmcnt(5)\n"
    "ds_read_b64_tr_b16 v[78:79], v127 offset:51200\n"
    "v_mfma_f32_16x16x32_bf16 v[16:19], v[64:67], v[152:155], v[16:19]\n"
    "ds_read_b64_tr_b16 v[48:49], v124 offset:16384\n"
    "v_mfma_f32_16x16x32_bf16 v[20:23], v[64:67], v[156:159], v[20:23]\n"
    "s_waitcnt lgkmcnt(5)\n"
    "ds_read_b64_tr_b16 v[50:51], v124 offset:18432\n"
    "v_mfma_f32_16x16x32_bf16 v[28:31], v[68:71], v[156:159], v[28:31]\n"
    "ds_read_b64_tr_b16 v[52:53], v125 offset:16384\n"
    "v_mfma_f32_16x16x32_bf16 v[24:27], v[68:71], v[152:155], v[24:27]\n"
    "s_waitcnt lgkmcnt(5)\n"
    "ds_read_b64_tr_b16 v[54:55], v125 offset:18432\n"
    "v_mfma_f32_16x16x32_bf16 v[32:35], v[72:75], v[152:155], v[32:35]\n"
    "s_setprio 0\n"
    "ds_read_b64_tr_b16 v[56:57], v126 offset:16384\n"
    "v_mfma_f32_16x16x32_bf16 v[36:39], v[72:75], v[156:159], v[36:39]\n"
    "s_waitcnt lgkmcnt(5)\n"
    "ds_read_b64_tr_b16 v[58:59], v126 offset:18432\n"
    "v_mfma_f32_16x16x32_bf16 v[44:47], v[76:79], v[156:159], v[44:47]\n"
    "ds_read_b64_tr_b16 v[60:61], v127 offset:16384\n"
    "v_mfma_f32_16x16x32_bf16 v[40:43], v[76:79], v[152:155], v[40:43]\n"
    "ds_read_b64_tr_b16 v[62:63], v127 offset:18432\n"
    "s_waitcnt vmcnt(4)\n"
    "s_barrier\n"
    "s_setprio 2\n"
    "s_waitcnt lgkmcnt(6)\n"
    "v_mfma_f32_16x16x32_bf16 v[16:19], v[48:51], v[160:163], v[16:19]\n"
    "ds_read_b64_tr_b16 v[64:65], v124 offset:32768\n"
    "v_mfma_f32_16x16x32_bf16 v[20:23], v[48:51], v[164:167], v[20:23]\n"
    "s_waitcnt lgkmcnt(5)\n"
    "ds_read_b64_tr_b16 v[66:67], v124 offset:34816\n"
    "v_mfma_f32_16x16x32_bf16 v[28:31], v[52:55], v[164:167], v[28:31]\n"
    "ds_read_b64_tr_b16 v[68:69], v125 offset:32768\n"
    "v_mfma_f32_16x16x32_bf16 v[24:27], v[52:55], v[160:163], v[24:27]\n"
    "s_waitcnt lgkmcnt(5)\n"
    "ds_read_b64_tr_b16 v[70:71], v125 offset:34816\n"
    "v_mfma_f32_16x16x32_bf16 v[32:35], v[56:59], v[160:163], v[32:35]\n"
    "ds_read_b64_tr_b16 v[72:73], v126 offset:32768\n"
    "v_mfma_f32_16x16x32_bf16 v[36:39], v[56:59], v[164:167], v[36:39]\n"
    "s_waitcnt lgkmcnt(5)\n"
    "ds_read_b64_tr_b16 v[74:75], v126 offset:34816\n"
    "v_mfma_f32_16x16x32_bf16 v[44:47], v[60:63], v[164:167], v[44:47]\n"
    "ds_read_b64_tr_b16 v[76:77], v127 offset:32768\n"
    "v_mfma_f32_16x16x32_bf16 v[40:43], v[60:63], v[160:163], v[40:43]\n"
    "s_waitcnt lgkmcnt(5)\n"
    "ds_read_b64_tr_b16 v[78:79], v127 offset:34816\n"
    "v_mfma_f32_16x16x32_bf16 v[16:19], v[64:67], v[168:171], v[16:19]\n"
    "v_add_u32 v124, 0xffff4000, v124\n"
    "ds_read_b64_tr_b16 v[48:49], v124 offset:0\n"
    "s_waitcnt vmcnt(0)\n"
    "s_mov_b64 exec, 1\n"
    "buffer_store_dwordx2 v[252:253], off, s[56:59], s94 sc1\n"
    "s_mov_b64 exec, -1\n"
    "v_mfma_f32_16x16x32_bf16 v[20:23], v[64:67], v[172:175], v[20:23]\n"
    "s_waitcnt lgkmcnt(5)\n"
    "ds_read_b64_tr_b16 v[50:51], v124 offset:2048\n"
    "v_mfma_f32_16x16x32_bf16 v[28:31], v[68:71], v[172:175], v[28:31]\n"
    "v_add_u32 v125, 0xffff4000, v125\n"
    "ds_read_b64_tr_b16 v[52:53], v125 offset:0\n"
    "v_lshrrev_b32 v13, 5, v0\n"
    "v_mul_u32_u24 v14, 7168, v13\n"
    "v_mfma_f32_16x16x32_bf16 v[24:27], v[68:71], v[168:171], v[24:27]\n"
    "s_waitcnt lgkmcnt(5)\n"
    "ds_read_b64_tr_b16 v[54:55], v125 offset:2048\n"
    "v_add_u32 v5, v14, v4\n"
    "v_mfma_f32_16x16x32_bf16 v[32:35], v[72:75], v[168:171], v[32:35]\n"
    "v_add_u32 v126, 0xffff4000, v126\n"
    "ds_read_b64_tr_b16 v[56:57], v126 offset:0\n"
    "v_mul_u32_u24 v14, 21504, v13\n"
    "v_mfma_f32_16x16x32_bf16 v[36:39], v[72:75], v[172:175], v[36:39]\n"
    "s_bfe_u32 s65, s25, 0x10001\n"
    "ds_read_b64_tr_b16 v[58:59], v126 offset:2048\n"
    "s_waitcnt lgkmcnt(6)\n"
    "v_mfma_f32_16x16x32_bf16 v[44:47], v[76:79], v[172:175], v[44:47]\n"
    "s_mul_i32 s66, s65, 100352\n"
    "v_add_u32 v127, 0xffff4000, v127\n"
    "ds_read_b64_tr_b16 v[60:61], v127 offset:0\n"
    "s_and_b32 s67, s25, 1\n"
    "v_mfma_f32_16x16x32_bf16 v[40:43], v[76:79], v[168:171], v[40:43]\n"
    "s_mul_i32 s67, s67, 93184\n"
    "ds_read_b64_tr_b16 v[62:63], v127 offset:2048\n"
    "s_waitcnt lgkmcnt(6)\n"
    "v_mfma_f32_16x16x32_bf16 v[16:19], v[48:51], v[176:179], v[16:19]\n"
    "s_setprio 1\n"
    "ds_read_b64_tr_b16 v[64:65], v124 offset:16384\n"
    "s_add_u32 s66, s66, s67\n"
    "v_mfma_f32_16x16x32_bf16 v[20:23], v[48:51], v[180:183], v[20:23]\n"
    "s_waitcnt lgkmcnt(5)\n"
    "ds_read_b64_tr_b16 v[66:67], v124 offset:18432\n"
    "v_add3_u32 v12, v14, s66, v4\n"
    "v_mfma_f32_16x16x32_bf16 v[28:31], v[52:55], v[180:183], v[28:31]\n"
    "s_lshl_b32 s65, s65, 10\n"
    "ds_read_b64_tr_b16 v[68:69], v125 offset:16384\n"
    "s_sub_u32 s7, s98, s65\n"
    "v_mfma_f32_16x16x32_bf16 v[24:27], v[52:55], v[176:179], v[24:27]\n"
    "s_add_u32 s7, s7, 12288\n"
    "ds_read_b64_tr_b16 v[70:71], v125 offset:18432\n"
    "s_waitcnt lgkmcnt(6)\n"
    "v_mfma_f32_16x16x32_bf16 v[32:35], v[56:59], v[176:179], v[32:35]\n"
    "ds_read_b64_tr_b16 v[72:73], v126 offset:16384\n"
    "v_and_b32 v106, 15, v0\n"
    "v_mfma_f32_16x16x32_bf16 v[36:39], v[56:59], v[180:183], v[36:39]\n"
    "s_waitcnt lgkmcnt(5)\n"
    "ds_read_b64_tr_b16 v[74:75], v126 offset:18432\n"
    "v_and_b32 v107, 7, v0\n"
    "v_mfma_f32_16x16x32_bf16 v[44:47], v[60:63], v[180:183], v[44:47]\n"
    "ds_read_b64_tr_b16 v[76:77], v127 offset:16384\n"
    "v_lshrrev_b32 v108, 5, v0\n"
    "v_xor_b32 v108, v107, v108\n"
    "v_mfma_f32_16x16x32_bf16 v[40:43], v[60:63], v[176:179], v[40:43]\n"
    "s_waitcnt lgkmcnt(5)\n"
    "ds_read_b64_tr_b16 v[78:79], v127 offset:18432\n"
    "v_lshlrev_b32 v108, 4, v108\n"
    "v_mfma_f32_16x16x32_bf16 v[16:19], v[64:67], v[184:187], v[16:19]\n"
    "ds_read_b64_tr_b16 v[48:49], v124 offset:32768\n"
    "v_lshl_add_u32 v108, v106, 7, v108\n"
    "v_mfma_f32_16x16x32_bf16 v[20:23], v[64:67], v[188:191], v[20:23]\n"
    "s_waitcnt lgkmcnt(5)\n"
    "ds_read_b64_tr_b16 v[50:51], v124 offset:34816\n"
    "v_bfe_u32 v106, v0, 4, 1\n"
    "v_mfma_f32_16x16x32_bf16 v[28:31], v[68:71], v[188:191], v[28:31]\n"
    "s_lshl_b32 s65, s25, 12\n"
    "ds_read_b64_tr_b16 v[52:53], v125 offset:32768\n"
    "v_lshl_add_u32 v108, v106, 3, v108\n"
    "v_mfma_f32_16x16x32_bf16 v[24:27], v[68:71], v[184:187], v[24:27]\n"
    "s_waitcnt lgkmcnt(5)\n"
    "ds_read_b64_tr_b16 v[54:55], v125 offset:34816\n"
    "v_add_u32 v100, s65, v108\n"
    "v_mfma_f32_16x16x32_bf16 v[32:35], v[72:75], v[184:187], v[32:35]\n"
    "s_setprio 0\n"
    "ds_read_b64_tr_b16 v[56:57], v126 offset:32768\n"
    "v_xor_b32 v101, 32, v100\n"
    "v_mfma_f32_16x16x32_bf16 v[36:39], v[72:75], v[188:191], v[36:39]\n"
    "s_waitcnt lgkmcnt(5)\n"
    "ds_read_b64_tr_b16 v[58:59], v126 offset:34816\n"
    "v_xor_b32 v102, 64, v100\n"
    "v_mfma_f32_16x16x32_bf16 v[44:47], v[76:79], v[188:191], v[44:47]\n"
    "ds_read_b64_tr_b16 v[60:61], v127 offset:32768\n"
    "v_xor_b32 v103, 0x60, v100\n"
    "v_add_u32 v96, 0x14000, v100\n"
    "v_mfma_f32_16x16x32_bf16 v[40:43], v[76:79], v[184:187], v[40:43]\n"
    "ds_read_b64_tr_b16 v[62:63], v127 offset:34816\n"
    "v_add_u32 v97, 0x14000, v101\n"
    "s_barrier\n"
    "s_setprio 2\n"
    "s_waitcnt lgkmcnt(6)\n"
    "v_mfma_f32_16x16x32_bf16 v[16:19], v[48:51], v[192:195], v[16:19]\n"
    "ds_read_b64_tr_b16 v[64:65], v124 offset:49152\n"
    "v_add_u32 v98, 0x14000, v102\n"
    "v_mfma_f32_16x16x32_bf16 v[20:23], v[48:51], v[196:199], v[20:23]\n"
    "s_waitcnt lgkmcnt(5)\n"
    "ds_read_b64_tr_b16 v[66:67], v124 offset:51200\n"
    "v_add_u32 v99, 0x14000, v103\n"
    "v_mfma_f32_16x16x32_bf16 v[28:31], v[52:55], v[196:199], v[28:31]\n"
    "ds_read_b64_tr_b16 v[68:69], v125 offset:49152\n"
    "v_lshrrev_b32 v106, 3, v0\n"
    "v_mfma_f32_16x16x32_bf16 v[24:27], v[52:55], v[192:195], v[24:27]\n"
    "s_waitcnt lgkmcnt(5)\n"
    "ds_read_b64_tr_b16 v[70:71], v125 offset:51200\n"
    "v_xor_b32 v107, v107, v106\n"
    "v_lshlrev_b32 v107, 4, v107\n"
    "v_mfma_f32_16x16x32_bf16 v[32:35], v[56:59], v[192:195], v[32:35]\n"
    "ds_read_b64_tr_b16 v[72:73], v126 offset:49152\n"
    "v_lshl_add_u32 v107, v106, 7, v107\n"
    "v_mfma_f32_16x16x32_bf16 v[36:39], v[56:59], v[196:199], v[36:39]\n"
    "s_waitcnt lgkmcnt(5)\n"
    "ds_read_b64_tr_b16 v[74:75], v126 offset:51200\n"
    "v_add_u32 v105, s62, v107\n"
    "v_mfma_f32_16x16x32_bf16 v[44:47], v[60:63], v[196:199], v[44:47]\n"
    "ds_read_b64_tr_b16 v[76:77], v127 offset:49152\n"
    "v_add_u32 v104, 0x14000, v105\n"
    "v_mfma_f32_16x16x32_bf16 v[40:43], v[60:63], v[192:195], v[40:43]\n"
    "s_waitcnt lgkmcnt(5)\n"
    "ds_read_b64_tr_b16 v[78:79], v127 offset:51200\n"
    "v_and_b32 v106, 1, v0\n"
    "v_lshlrev_b32 v106, 4, v106\n"
    "v_mfma_f32_16x16x32_bf16 v[16:19], v[64:67], v[200:203], v[16:19]\n"
    "s_setprio 1\n"
    "v_add_u32 v124, 0xffff4000, v124\n"
    "ds_read_b64_tr_b16 v[48:49], v124 offset:0\n"
    "v_mov_b32 v107, 0x3f80\n"
    "v_mfma_f32_16x16x32_bf16 v[20:23], v[64:67], v[204:207], v[20:23]\n"
    "s_waitcnt lgkmcnt(5)\n"
    "ds_read_b64_tr_b16 v[50:51], v124 offset:2048\n"
    "v_lshlrev_b32 v106, v106, v107\n"
    "v_mfma_f32_16x16x32_bf16 v[28:31], v[68:71], v[204:207], v[28:31]\n"
    "s_mov_b32 s8, 0x00300003\n"
    "v_add_u32 v125, 0xffff4000, v125\n"
    "ds_read_b64_tr_b16 v[52:53], v125 offset:0\n"
    "v_mfma_f32_16x16x32_bf16 v[24:27], v[68:71], v[200:203], v[24:27]\n"
    "s_mov_b32 s9, 0x30000300\n"
    "ds_read_b64_tr_b16 v[54:55], v125 offset:2048\n"
    "v_cndmask_b32 v108, 0, v106, s[8:9]\n"
    "s_waitcnt lgkmcnt(6)\n"
    "v_mfma_f32_16x16x32_bf16 v[32:35], v[72:75], v[200:203], v[32:35]\n"
    "s_lshl_b64 s[8:9], s[8:9], 2\n"
    "v_add_u32 v126, 0xffff4000, v126\n"
    "ds_read_b64_tr_b16 v[56:57], v126 offset:0\n"
    "buffer_load_dwordx2 v[250:251], v7, s[56:59], 0 offen sc1\n"
    "v_mfma_f32_16x16x32_bf16 v[36:39], v[72:75], v[204:207], v[36:39]\n"
    "s_waitcnt lgkmcnt(5)\n"
    "ds_read_b64_tr_b16 v[58:59], v126 offset:2048\n"
    "v_cndmask_b32 v109, 0, v106, s[8:9]\n"
    "v_mfma_f32_16x16x32_bf16 v[44:47], v[76:79], v[204:207], v[44:47]\n"
    "s_setprio 0\n"
    "v_add_u32 v127, 0xffff4000, v127\n"
    "ds_read_b64_tr_b16 v[60:61], v127 offset:0\n"
    "v_mov_b32 v106, 0\n"
    "v_mfma_f32_16x16x32_bf16 v[40:43], v[76:79], v[200:203], v[40:43]\n"
    "ds_read_b64_tr_b16 v[62:63], v127 offset:2048\n"
    "v_mov_b32 v107, 0\n"
    "v_mov_b32 v110, 0\n"
    "s_barrier\n"
    "s_setprio 2\n"
    "s_waitcnt lgkmcnt(6)\n"
    "v_mfma_f32_16x16x32_bf16 v[16:19], v[48:51], v[208:211], v[16:19]\n"
    "ds_read_b64_tr_b16 v[64:65], v124 offset:16384\n"
    "v_mov_b32 v111, 0\n"
    "v_mfma_f32_16x16x32_bf16 v[20:23], v[48:51], v[212:215], v[20:23]\n"
    "s_waitcnt lgkmcnt(5)\n"
    "ds_read_b64_tr_b16 v[66:67], v124 offset:18432\n"
    "v_lshlrev_b32 v112, 16, v244\n"
    "v_mfma_f32_16x16x32_bf16 v[28:31], v[52:55], v[212:215], v[28:31]\n"
    "ds_read_b64_tr_b16 v[68:69], v125 offset:16384\n"
    "v_and_b32 v113, 0xffff0000, v244\n"
    "v_mfma_f32_16x16x32_bf16 v[24:27], v[52:55], v[208:211], v[24:27]\n"
    "s_waitcnt lgkmcnt(5)\n"
    "ds_read_b64_tr_b16 v[70:71], v125 offset:18432\n"
    "v_lshlrev_b32 v114, 16, v245\n"
    "v_and_b32 v115, 0xffff0000, v245\n"
    "v_mfma_f32_16x16x32_bf16 v[32:35], v[56:59], v[208:211], v[32:35]\n"
    "ds_read_b64_tr_b16 v[72:73], v126 offset:16384\n"
    "v_lshlrev_b32 v116, 16, v246\n"
    "v_mfma_f32_16x16x32_bf16 v[36:39], v[56:59], v[212:215], v[36:39]\n"
    "s_waitcnt lgkmcnt(5)\n"
    "ds_read_b64_tr_b16 v[74:75], v126 offset:18432\n"
    "v_and_b32 v117, 0xffff0000, v246\n"
    "v_mfma_f32_16x16x32_bf16 v[44:47], v[60:63], v[212:215], v[44:47]\n"
    "ds_read_b64_tr_b16 v[76:77], v127 offset:16384\n"
    "v_lshlrev_b32 v118, 16, v247\n"
    "v_mfma_f32_16x16x32_bf16 v[40:43], v[60:63], v[208:211], v[40:43]\n"
    "s_waitcnt lgkmcnt(5)\n"
    "ds_read_b64_tr_b16 v[78:79], v127 offset:18432\n"
    "v_and_b32 v119, 0xffff0000, v247\n"
    "v_mfma_f32_16x16x32_bf16 v[16:19], v[64:67], v[216:219], v[16:19]\n"
    "ds_read_b64_tr_b16 v[48:49], v124 offset:32768\n"
    "v_mfma_f32_16x16x32_bf16 v[20:23], v[64:67], v[220:223], v[20:23]\n"
    "s_waitcnt lgkmcnt(5)\n"
    "ds_read_b64_tr_b16 v[50:51], v124 offset:34816\n"
    "v_mfma_f32_16x16x32_bf16 v[28:31], v[68:71], v[220:223], v[28:31]\n"
    "ds_read_b64_tr_b16 v[52:53], v125 offset:32768\n"
    "v_mfma_f32_16x16x32_bf16 v[24:27], v[68:71], v[216:219], v[24:27]\n"
    "s_waitcnt lgkmcnt(5)\n"
    "ds_read_b64_tr_b16 v[54:55], v125 offset:34816\n"
    "v_mfma_f32_16x16x32_bf16 v[32:35], v[72:75], v[216:219], v[32:35]\n"
    "s_setprio 1\n"
    "ds_read_b64_tr_b16 v[56:57], v126 offset:32768\n"
    "s_waitcnt vmcnt(0)\n"
    "v_cmp_ne_u64 vcc, s[60:61], v[250:251]\n"
    "s_nop 0\n"
    "s_cbranch_vccz Lok_26\n"
    "s_mov_b32 s10, 0x8000\n"
    "Lspin_27:\n"
    "buffer_load_dwordx2 v[250:251], v7, s[56:59], 0 offen sc1\n"
    "s_sub_u32 s10, s10, 1\n"
    "s_waitcnt vmcnt(0)\n"
    "s_cbranch_scc1 Lok_26\n"
    "v_cmp_ne_u64 vcc, s[60:61], v[250:251]\n"
    "s_nop 0\n"
    "s_cbranch_vccnz Lspin_27\n"
    "Lok_26:\n"
    "v_mfma_f32_16x16x32_bf16 v[36:39], v[72:75], v[220:223], v[36:39]\n"
    "s_waitcnt lgkmcnt(5)\n"
    "ds_read_b64_tr_b16 v[58:59], v126 offset:34816\n"
    "v_mfma_f32_16x16x32_bf16 v[44:47], v[76:79], v[220:223], v[44:47]\n"
    "s_add_u32 s94, s98, 16384\n"
    "ds_read_b64_tr_b16 v[60:61], v127 offset:32768\n"
    "v_mfma_f32_16x16x32_bf16 v[40:43], v[76:79], v[216:219], v[40:43]\n"
    "s_waitcnt lgkmcnt(5)\n"
    "ds_read_b64_tr_b16 v[62:63], v127 offset:34816\n"
    "v_mfma_f32_16x16x32_bf16 v[16:19], v[48:51], v[224:227], v[16:19]\n"
    "v_add_u32 v124, 0x18000, v124\n"
    "ds_read_b64_tr_b16 v[64:65], v124 offset:49152\n"
    "v_mfma_f32_16x16x32_bf16 v[20:23], v[48:51], v[228:231], v[20:23]\n"
    "s_waitcnt lgkmcnt(5)\n"
    "ds_read_b64_tr_b16 v[66:67], v124 offset:51200\n"
    "buffer_load_dwordx4 v[128:131], v1, s[56:59], s98 offen offset:0 sc1\n"
    "v_mfma_f32_16x16x32_bf16 v[28:31], v[52:55], v[228:231], v[28:31]\n"
    "v_add_u32 v125, 0x18000, v125\n"
    "ds_read_b64_tr_b16 v[68:69], v125 offset:49152\n"
    "v_mfma_f32_16x16x32_bf16 v[24:27], v[52:55], v[224:227], v[24:27]\n"
    "s_waitcnt lgkmcnt(5)\n"
    "ds_read_b64_tr_b16 v[70:71], v125 offset:51200\n"
    "buffer_load_dwordx4 v[132:135], v1, s[56:59], s94 offen offset:0 sc1\n"
    "v_mfma_f32_16x16x32_bf16 v[32:35], v[56:59], v[224:227], v[32:35]\n"
    "v_add_u32 v126, 0x18000, v126\n"
    "ds_read_b64_tr_b16 v[72:73], v126 offset:49152\n"
    "v_mfma_f32_16x16x32_bf16 v[36:39], v[56:59], v[228:231], v[36:39]\n"
    "s_setprio 0\n"
    "ds_read_b64_tr_b16 v[74:75], v126 offset:51200\n"
    "s_waitcnt lgkmcnt(6)\n"
    "v_mfma_f32_16x16x32_bf16 v[44:47], v[60:63], v[228:231], v[44:47]\n"
    "v_add_u32 v127, 0x18000, v127\n"
    "ds_read_b64_tr_b16 v[76:77], v127 offset:49152\n"
    "buffer_load_dwordx4 v[136:139], v1, s[56:59], s98 offen offset:1024 sc1\n"
    "v_mfma_f32_16x16x32_bf16 v[40:43], v[60:63], v[224:227], v[40:43]\n"
    "ds_read_b64_tr_b16 v[78:79], v127 offset:51200\n"
    "s_barrier\n"
    "s_setprio 2\n"
    "s_waitcnt lgkmcnt(6)\n"
    "v_mfma_f32_16x16x32_bf16 v[16:19], v[64:67], v[232:235], v[16:19]\n"
    "ds_read_b64_tr_b16 v[48:49], v124 offset:16384\n"
    "buffer_load_dwordx4 v[140:143], v1, s[56:59], s94 offen offset:1024 sc1\n"
    "v_mfma_f32_16x16x32_bf16 v[20:23], v[64:67], v[236:239], v[20:23]\n"
    "s_waitcnt lgkmcnt(5)\n"
    "ds_read_b64_tr_b16 v[50:51], v124 offset:18432\n"
    "v_mfma_f32_16x16x32_bf16 v[28:31], v[68:71], v[236:239], v[28:31]\n"
    "ds_read_b64_tr_b16 v[52:53], v125 offset:16384\n"
    "v_mfma_f32_16x16x32_bf16 v[24:27], v[68:71], v[232:235], v[24:27]\n"
    "s_waitcnt lgkmcnt(5)\n"
    "ds_read_b64_tr_b16 v[54:55], v125 offset:18432\n"
    "buffer_load_dwordx4 v[144:147], v1, s[56:59], s98 offen offset:2048 sc1\n"
    "v_mfma_f32_16x16x32_bf16 v[32:35], v[72:75], v[232:235], v[32:35]\n"
    "ds_read_b64_tr_b16 v[56:57], v126 offset:16384\n"
    "v_mfma_f32_16x16x32_bf16 v[36:39], v[72:75], v[236:239], v[36:39]\n"
    "s_waitcnt lgkmcnt(5)\n"
    "ds_read_b64_tr_b16 v[58:59], v126 offset:18432\n"
    "buffer_load_dwordx4 v[148:151], v1, s[56:59], s94 offen offset:2048 sc1\n"
    "v_mfma_f32_16x16x32_bf16 v[44:47], v[76:79], v[236:239], v[44:47]\n"
    "ds_read_b64_tr_b16 v[60:61], v127 offset:16384\n"
    "v_mfma_f32_16x16x32_bf16 v[40:43], v[76:79], v[232:235], v[40:43]\n"
    "ds_read_b64_tr_b16 v[62:63], v127 offset:18432\n"
    "v_cvt_pk_bf16_f32 v16, v16, v17\n"
    "v_cvt_pk_bf16_f32 v17, v18, v19\n"
    "ds_write_b64 v96, v[16:17] offset:0\n"
    "v_cvt_pk_bf16_f32 v20, v20, v21\n"
    "v_cvt_pk_bf16_f32 v21, v22, v23\n"
    "ds_write_b64 v96, v[20:21] offset:2048\n"
    "s_waitcnt vmcnt(5) lgkmcnt(8)\n"
    "v_mfma_f32_16x16x32_bf16 v[160:163], v[48:51], v[128:131], 0\n"
    "ds_read_b64_tr_b16 v[64:65], v124 offset:32768\n"
    "s_waitcnt vmcnt(4)\n"
    "v_mfma_f32_16x16x32_bf16 v[164:167], v[48:51], v[132:135], 0\n"
    "s_waitcnt lgkmcnt(7)\n"
    "ds_read_b64_tr_b16 v[66:67], v124 offset:34816\n"
    "v_cvt_pk_bf16_f32 v28, v28, v29\n"
    "v_cvt_pk_bf16_f32 v29, v30, v31\n"
    "v_mfma_f32_16x16x32_bf16 v[172:175], v[52:55], v[132:135], 0\n"
    "ds_read_b64_tr_b16 v[68:69], v125 offset:32768\n"
    "ds_write_b64 v97, v[28:29] offset:2048\n"
    "v_mfma_f32_16x16x32_bf16 v[168:171], v[52:55], v[128:131], 0\n"
    "s_waitcnt lgkmcnt(8)\n"
    "ds_read_b64_tr_b16 v[70:71], v125 offset:34816\n"
    "v_cvt_pk_bf16_f32 v24, v24, v25\n"
    "v_mfma_f32_16x16x32_bf16 v[176:179], v[56:59], v[128:131], 0\n"
    "ds_read_b64_tr_b16 v[72:73], v126 offset:32768\n"
    "v_cvt_pk_bf16_f32 v25, v26, v27\n"
    "v_mfma_f32_16x16x32_bf16 v[180:183], v[56:59], v[132:135], 0\n"
    "s_waitcnt lgkmcnt(8)\n"
    "ds_read_b64_tr_b16 v[74:75], v126 offset:34816\n"
    "ds_write_b64 v97, v[24:25] offset:0\n"
    "v_mfma_f32_16x16x32_bf16 v[188:191], v[60:63], v[132:135], 0\n"
    "ds_read_b64_tr_b16 v[76:77], v127 offset:32768\n"
    "v_cvt_pk_bf16_f32 v32, v32, v33\n"
    "v_mfma_f32_16x16x32_bf16 v[184:187], v[60:63], v[128:131], 0\n"
    "ds_read_b64_tr_b16 v[78:79], v127 offset:34816\n"
    "v_cvt_pk_bf16_f32 v33, v34, v35\n"
    "s_waitcnt vmcnt(3) lgkmcnt(8)\n"
    "v_mfma_f32_16x16x32_bf16 v[160:163], v[64:67], v[136:139], v[160:163]\n"
    "s_setprio 1\n"
    "v_add_u32 v124, 0xffff4000, v124\n"
    "ds_read_b64_tr_b16 v[48:49], v124 offset:0\n"
    "ds_write_b64 v98, v[32:33] offset:0\n"
    "s_waitcnt vmcnt(2)\n"
    "v_mfma_f32_16x16x32_bf16 v[164:167], v[64:67], v[140:143], v[164:167]\n"
    "s_waitcnt lgkmcnt(7)\n"
    "ds_read_b64_tr_b16 v[50:51], v124 offset:2048\n"
    "v_cvt_pk_bf16_f32 v36, v36, v37\n"
    "v_mfma_f32_16x16x32_bf16 v[172:175], v[68:71], v[140:143], v[172:175]\n"
    "v_add_u32 v125, 0xffff4000, v125\n"
    "ds_read_b64_tr_b16 v[52:53], v125 offset:0\n"
    "v_cvt_pk_bf16_f32 v37, v38, v39\n"
    "v_mfma_f32_16x16x32_bf16 v[168:171], v[68:71], v[136:139], v[168:171]\n"
    "s_waitcnt lgkmcnt(7)\n"
    "ds_read_b64_tr_b16 v[54:55], v125 offset:2048\n"
    "ds_write_b64 v98, v[36:37] offset:2048\n"
    "v_mfma_f32_16x16x32_bf16 v[176:179], v[72:75], v[136:139], v[176:179]\n"
    "v_add_u32 v126, 0xffff4000, v126\n"
    "ds_read_b64_tr_b16 v[56:57], v126 offset:0\n"
    "v_cvt_pk_bf16_f32 v44, v44, v45\n"
    "v_mfma_f32_16x16x32_bf16 v[180:183], v[72:75], v[140:143], v[180:183]\n"
    "s_waitcnt lgkmcnt(7)\n"
    "ds_read_b64_tr_b16 v[58:59], v126 offset:2048\n"
    "v_cvt_pk_bf16_f32 v45, v46, v47\n"
    "v_mfma_f32_16x16x32_bf16 v[188:191], v[76:79], v[140:143], v[188:191]\n"
    "v_add_u32 v127, 0xffff4000, v127\n"
    "ds_read_b64_tr_b16 v[60:61], v127 offset:0\n"
    "ds_write_b64 v99, v[44:45] offset:2048\n"
    "v_mfma_f32_16x16x32_bf16 v[184:187], v[76:79], v[136:139], v[184:187]\n"
    "ds_read_b64_tr_b16 v[62:63], v127 offset:2048\n"
    "v_cvt_pk_bf16_f32 v40, v40, v41\n"
    "s_waitcnt vmcnt(1) lgkmcnt(8)\n"
    "v_mfma_f32_16x16x32_bf16 v[160:163], v[48:51], v[144:147], v[160:163]\n"
    "v_cvt_pk_bf16_f32 v41, v42, v43\n"
    "s_waitcnt vmcnt(0)\n"
    "v_mfma_f32_16x16x32_bf16 v[164:167], v[48:51], v[148:151], v[164:167]\n"
    "s_waitcnt lgkmcnt(6)\n"
    "ds_write_b64 v99, v[40:41] offset:0\n"
    "v_mfma_f32_16x16x32_bf16 v[172:175], v[52:55], v[148:151], v[172:175]\n"
    "v_mfma_f32_16x16x32_bf16 v[168:171], v[52:55], v[144:147], v[168:171]\n"
    "s_waitcnt lgkmcnt(4)\n"
    "v_cvt_pk_bf16_f32 v160, v160, v161\n"
    "v_cvt_pk_bf16_f32 v161, v162, v163\n"
    "ds_write_b64 v100, v[160:161] offset:0\n"
    "v_mfma_f32_16x16x32_bf16 v[176:179], v[56:59], v[144:147], v[176:179]\n"
    "s_setprio 0\n"
    "v_cvt_pk_bf16_f32 v164, v164, v165\n"
    "v_cvt_pk_bf16_f32 v165, v166, v167\n"
    "ds_write_b64 v100, v[164:165] offset:2048\n"
    "v_mfma_f32_16x16x32_bf16 v[180:183], v[56:59], v[148:151], v[180:183]\n"
    "s_waitcnt lgkmcnt(3)\n"
    "v_cvt_pk_bf16_f32 v172, v172, v173\n"
    "v_cvt_pk_bf16_f32 v173, v174, v175\n"
    "ds_write_b64 v101, v[172:173] offset:2048\n"
    "v_mfma_f32_16x16x32_bf16 v[188:191], v[60:63], v[148:151], v[188:191]\n"
    "v_cvt_pk_bf16_f32 v168, v168, v169\n"
    "v_cvt_pk_bf16_f32 v169, v170, v171\n"
    "ds_write_b64 v101, v[168:169] offset:0\n"
    "v_mfma_f32_16x16x32_bf16 v[184:187], v[60:63], v[144:147], v[184:187]\n"
    "v_cvt_pk_bf16_f32 v176, v176, v177\n"
    "v_cvt_pk_bf16_f32 v177, v178, v179\n"
    "ds_write_b64 v102, v[176:177] offset:0\n"
    "v_cvt_pk_bf16_f32 v180, v180, v181\n"
    "v_cvt_pk_bf16_f32 v181, v182, v183\n"
    "ds_write_b64 v102, v[180:181] offset:2048\n"
    "v_cvt_pk_bf16_f32 v188, v188, v189\n"
    "v_cvt_pk_bf16_f32 v189, v190, v191\n"
    "ds_write_b64 v103, v[188:189] offset:2048\n"
    "v_cvt_pk_bf16_f32 v184, v184, v185\n"
    "v_cvt_pk_bf16_f32 v185, v186, v187\n"
    "ds_write_b64 v103, v[184:185] offset:0\n"
    "s_waitcnt lgkmcnt(0)\n"
    "s_barrier\n"
    "ds_read_b128 v[16:19], v104 offset:0\n"
    "ds_read_b128 v[20:23], v104 offset:8192\n"
    "ds_read_b128 v[24:27], v104 offset:16384\n"
    "ds_read_b128 v[28:31], v104 offset:24576\n"
    "ds_read_b128 v[32:35], v105 offset:0\n"
    "ds_read_b128 v[36:39], v105 offset:8192\n"
    "ds_read_b128 v[40:43], v105 offset:16384\n"
    "ds_read_b128 v[44:47], v105 offset:24576\n"
    "s_waitcnt lgkmcnt(7)\n"
    "v_mfma_f32_16x16x32_bf16 v[80:83], v[108:111], v[16:19], 0\n"
    "v_mfma_f32_16x16x32_bf16 v[84:87], v[106:109], v[16:19], 0\n"
    "s_waitcnt lgkmcnt(6)\n"
    "v_mfma_f32_16x16x32_bf16 v[80:83], v[108:111], v[20:23], v[80:83]\n"
    "v_mfma_f32_16x16x32_bf16 v[84:87], v[106:109], v[20:23], v[84:87]\n"
    "s_waitcnt lgkmcnt(5)\n"
    "v_mfma_f32_16x16x32_bf16 v[80:83], v[108:111], v[24:27], v[80:83]\n"
    "v_mfma_f32_16x16x32_bf16 v[84:87], v[106:109], v[24:27], v[84:87]\n"
    "s_waitcnt lgkmcnt(4)\n"
    "v_mfma_f32_16x16x32_bf16 v[80:83], v[108:111], v[28:31], v[80:83]\n"
    "v_mfma_f32_16x16x32_bf16 v[84:87], v[106:109], v[28:31], v[84:87]\n"
    "s_waitcnt lgkmcnt(3)\n"
    "s_nop 5\n"
    "v_mul_f32 v88, 0xbfb8aa3b, v80\n"
    "v_mul_f32 v89, 0xbfb8aa3b, v81\n"
    "v_mul_f32 v90, 0xbfb8aa3b, v82\n"
    "v_mul_f32 v91, 0xbfb8aa3b, v83\n"
    "v_mul_f32 v92, 0xbfb8aa3b, v84\n"
    "v_mul_f32 v93, 0xbfb8aa3b, v85\n"
    "v_mul_f32 v94, 0xbfb8aa3b, v86\n"
    "v_mul_f32 v95, 0xbfb8aa3b, v87\n"
    "v_exp_f32 v88, v88\n"
    "v_mfma_f32_16x16x32_bf16 v[48:51], v[108:111], v[32:35], 0\n"
    "v_exp_f32 v89, v89\n"
    "v_mfma_f32_16x16x32_bf16 v[52:55], v[106:109], v[32:35], 0\n"
    "s_waitcnt lgkmcnt(2)\n"
    "v_exp_f32 v90, v90\n"
    "v_mfma_f32_16x16x32_bf16 v[48:51], v[108:111], v[36:39], v[48:51]\n"
    "v_exp_f32 v91, v91\n"
    "v_mfma_f32_16x16x32_bf16 v[52:55], v[106:109], v[36:39], v[52:55]\n"
    "s_waitcnt lgkmcnt(1)\n"
    "v_exp_f32 v92, v92\n"
    "v_mfma_f32_16x16x32_bf16 v[48:51], v[108:111], v[40:43], v[48:51]\n"
    "v_exp_f32 v93, v93\n"
    "v_mfma_f32_16x16x32_bf16 v[52:55], v[106:109], v[40:43], v[52:55]\n"
    "s_waitcnt lgkmcnt(0)\n"
    "v_exp_f32 v94, v94\n"
    "v_mfma_f32_16x16x32_bf16 v[48:51], v[108:111], v[44:47], v[48:51]\n"
    "v_exp_f32 v95, v95\n"
    "v_mfma_f32_16x16x32_bf16 v[52:55], v[106:109], v[44:47], v[52:55]\n"
    "v_add_f32 v88, 1.0, v88\n"
    "v_add_f32 v89, 1.0, v89\n"
    "v_add_f32 v90, 1.0, v90\n"
    "v_add_f32 v91, 1.0, v91\n"
    "v_add_f32 v92, 1.0, v92\n"
    "v_add_f32 v93, 1.0, v93\n"
    "v_add_f32 v94, 1.0, v94\n"
    "v_add_f32 v95, 1.0, v95\n"
    "v_rcp_f32 v88, v88\n"
    "v_rcp_f32 v89, v89\n"
    "v_rcp_f32 v90, v90\n"
    "v_rcp_f32 v91, v91\n"
    "v_rcp_f32 v92, v92\n"
    "v_rcp_f32 v93, v93\n"
    "v_rcp_f32 v94, v94\n"
    "v_rcp_f32 v95, v95\n"
    "v_mul_f32 v80, v80, v88\n"
    "v_mul_f32 v81, v81, v89\n"
    "v_mul_f32 v82, v82, v90\n"
    "v_mul_f32 v83, v83, v91\n"
    "v_mul_f32 v84, v84, v92\n"
    "v_mul_f32 v85, v85, v93\n"
    "v_mul_f32 v86, v86, v94\n"
    "v_mul_f32 v87, v87, v95\n"
    "v_fma_f32 v48, v48, v80, v112\n"
    "v_fma_f32 v49, v49, v81, v113\n"
    "v_fma_f32 v50, v50, v82, v114\n"
    "v_fma_f32 v51, v51, v83, v115\n"
    "v_fma_f32 v52, v52, v84, v116\n"
    "v_fma_f32 v53, v53, v85, v117\n"
    "v_fma_f32 v54, v54, v86, v118\n"
    "v_fma_f32 v55, v55, v87, v119\n"
    "v_cvt_pk_bf16_f32 v56, v48, v49\n"
    "v_cvt_pk_bf16_f32 v57, v50, v51\n"
    "v_cvt_pk_bf16_f32 v58, v52, v53\n"
    "v_cvt_pk_bf16_f32 v59, v54, v55\n"
    "buffer_store_dwordx4 v[56:59], v6, s[52:55], s99 offen\n"
    "Lfin_h1:\n"
    "Lend:\n"
    "s_endpgm\n"
    : "+v"(tid)
    : "s"(__builtin_amdgcn_kernarg_segment_ptr()), "s"(blockIdx.x), "s"((unsigned)(size_t)(__attribute__((address_space(3))) char*)lds)
    : "v1", "v2", "v3", "v4", "v5", "v6", "v7", "v8", "v9", "v10", "v11", "v12", "v13", "v14", "v15", "v16", "v17", "v18", "v19", "v20", "v21", "v22", "v23", "v24", "v25", "v26", "v27", "v28", "v29", "v30", "v31", "v32", "v33", "v34", "v35", "v36", "v37", "v38", "v39", "v40", "v41", "v42", "v43", "v44", "v45", "v46", "v47", "v48", "v49", "v50", "v51", "v52", "v53", "v54", "v55", "v56", "v57", "v58", "v59", "v60", "v61", "v62", "v63", "v64", "v65", "v66", "v67", "v68", "v69", "v70", "v71", "v72", "v73", "v74", "v75", "v76", "v77", "v78", "v79", "v80", "v81", "v82", "v83", "v84", "v85", "v86", "v87", "v88", "v89", "v90", "v91", "v92", "v93", "v94", "v95", "v96", "v97", "v98", "v99", "v100", "v101", "v102", "v103", "v104", "v105", "v106", "v107", "v108", "v109", "v110", "v111", "v112", "v113", "v114", "v115", "v116", "v117", "v118", "v119", "v120", "v121", "v122", "v123", "v124", "v125", "v126", "v127", "v128", "v129", "v130", "v131", "v132", "v133", "v134", "v135", "v136", "v137", "v138", "v139", "v140", "v141", "v142", "v143", "v144", "v145", "v146", "v147", "v148", "v149", "v150", "v151", "v152", "v153", "v154", "v155", "v156", "v157", "v158", "v159", "v160", "v161", "v162", "v163", "v164", "v165", "v166", "v167", "v168", "v169", "v170", "v171", "v172", "v173", "v174", "v175", "v176", "v177", "v178", "v179", "v180", "v181", "v182", "v183", "v184", "v185", "v186", "v187", "v188", "v189", "v190", "v191", "v192", "v193", "v194", "v195", "v196", "v197", "v198", "v199", "v200", "v201", "v202", "v203", "v204", "v205", "v206", "v207", "v208", "v209", "v210", "v211", "v212", "v213", "v214", "v215", "v216", "v217", "v218", "v219", "v220", "v221", "v222", "v223", "v224", "v225", "v226", "v227", "v228", "v229", "v230", "v231", "v232", "v233", "v234", "v235", "v236", "v237", "v238", "v239", "v240", "v241", "v242", "v243", "v244", "v245", "v246", "v247", "v248", "v249", "v250", "v251", "v252", "v253", "v254", "v255", "s4", "s5", "s6", "s7", "s8", "s9", "s10", "s11", "s12", "s13", "s14", "s15", "s16", "s17", "s18", "s19", "s20", "s21", "s22", "s23", "s24", "s25", "s26", "s27", "s28", "s29", "s30", "s31", "s33", "s34", "s35", "s36", "s37", "s38", "s39", "s40", "s41", "s42", "s43", "s44", "s45", "s46", "s47", "s48", "s49", "s50", "s51", "s52", "s53", "s54", "s55", "s56", "s57", "s58", "s59", "s60", "s61", "s62", "s63", "s64", "s65", "s66", "s67", "s68", "s69", "s70", "s71", "s72", "s73", "s74", "s75", "s76", "s77", "s78", "s79", "s80", "s81", "s82", "s83", "s84", "s85", "s86", "s87", "s88", "s89", "s90", "s91", "s92", "s93", "s94", "s95", "s96", "s97", "s98", "s99", "vcc", "scc", "memory");
}

extern "C" void kernel_launch(void* const* d_in, const int* in_sizes, int n_in,
                              void* d_out, int out_size, void* d_ws, size_t ws_size,
                              hipStream_t stream) {
  fused_block_kernel<<<256, 512, 0, stream>>>(d_in[0], d_in[1], d_in[2], d_in[4], d_in[5], d_in[6], d_out, d_ws);
}
```
